```python
import torch

def _softmax(x, axis=-1):
    return torch.softmax(x, dim=axis)
def _gelu(x):
    return torch.nn.functional.gelu(x, approximate="tanh")
def _silu(x):
    return torch.nn.functional.silu(x)
def _sigmoid(x):
    return torch.sigmoid(x)
def _layernorm(x, axis=-1, eps=1e-5):
    m = x.mean(dim=axis, keepdim=True)
    v = x.var(dim=axis, keepdim=True, unbiased=False)
    return (x - m) / torch.sqrt(v + eps)
def _scatter_add(tgt, idx, src, axis=0):
    return tgt.clone().index_add_(axis, idx, src)
def _segment_sum(src, seg, n):
    out = torch.zeros((n,) + src.shape[1:], dtype=src.dtype)
    return out.index_add_(0, seg, src)
def _pad(x, pw):
    flat = tuple(v for pair in reversed(pw) for v in pair)
    return torch.nn.functional.pad(x, flat)

kernel_name = 'recurrent_scan__softmax_full_mha__geglu_ffn__prompt'

def setup_inputs():

    _w = (lambda a: (lambda t: t.to(torch.bfloat16) if t.is_floating_point() else t)(torch.as_tensor(a)))
    def _A_setup():
        B, S, D = 1, 2048, 576
        return {
            "x": _w((lambda *s: torch.randn(*s, dtype=torch.bfloat16) * 0.05)(B, S, D)),
            "wqkv": _w((lambda *s: torch.randn(*s, dtype=torch.bfloat16) * 0.05)(D, 3 * D)),
            "wo": _w((lambda *s: torch.randn(*s, dtype=torch.bfloat16) * 0.05)(D, D)),
        }
    _A_in = _A_setup()

    def _B_setup():
        B, S, D, F = 1, 2048, 576, 4608
        return {
            "x": _w((lambda *s: torch.randn(*s, dtype=torch.bfloat16) * 0.05)(B, S, D)),
            "w_gate": _w((lambda *s: torch.randn(*s, dtype=torch.bfloat16) * 0.05)(D, F)),
            "w_up": _w((lambda *s: torch.randn(*s, dtype=torch.bfloat16) * 0.05)(D, F)),
            "w_down": _w((lambda *s: torch.randn(*s, dtype=torch.bfloat16) * 0.05)(F, D)),
        }
    _B_in = _B_setup()

    return {
        "A_x": _A_in["x"],
        "A_wqkv": _A_in["wqkv"],
        "A_wo": _A_in["wo"],
        "B_x": _B_in["x"],
        "B_w_gate": _B_in["w_gate"],
        "B_w_up": _B_in["w_up"],
        "B_w_down": _B_in["w_down"],
    }

def reference(A_x, A_wqkv, A_wo, B_x, B_w_gate, B_w_up, B_w_down):
    def _A_ref(x, wqkv, wo):
        B, S, D = x.shape
        H = 32; Dh = D // H
        qkv = (lambda x, *d: x.permute(*d))((x @ wqkv).reshape(B, S, 3, H, Dh), 2, 0, 3, 1, 4)
        q, k, v = qkv[0], qkv[1], qkv[2]
        scale = Dh ** -0.5
        scores = torch.einsum("bhqd,bhkd->bhqk", q, k) * scale
        p = _softmax(scores, axis=-1)
        o = torch.einsum("bhqk,bhkd->bhqd", p, v)
        return (lambda x, *d: x.permute(*d))(o, 0, 2, 1, 3).reshape(B, S, D) @ wo

    def _B_ref(x, w_gate, w_up, w_down):
        return (_gelu(x @ w_gate) * (x @ w_up)) @ w_down

    x = A_x
    xn = _layernorm(x, axis=-1)
    s = _A_ref(xn, A_wqkv, A_wo)
    g = _silu(_B_ref(xn, B_w_gate, B_w_up, B_w_down))
    return x + s * g
```

```cpp
#include <hip/hip_runtime.h>
#include <hip/hip_bf16.h>
typedef unsigned short u16;
__device__ __forceinline__ float bf2f(u16 v) { return __uint_as_float(((unsigned)v) << 16); }
__device__ __forceinline__ u16 f2bf(float x) { unsigned u = __float_as_uint(x); u += 0x7fffu + ((u >> 16) & 1u); return (u16)(u >> 16); }

__global__ void n_ln(const u16* x, float* xn) {
  int row = blockIdx.x, t = threadIdx.x;
  float v[9]; float s = 0;
  for (int i = 0; i < 9; ++i) { v[i] = bf2f(x[row * 576 + i * 64 + t]); s += v[i]; }
  for (int o = 32; o; o >>= 1) s += __shfl_xor(s, o);
  float m = s / 576.f, q = 0;
  for (int i = 0; i < 9; ++i) { float d = v[i] - m; q += d * d; }
  for (int o = 32; o; o >>= 1) q += __shfl_xor(q, o);
  float mb = bf2f(f2bf(m)); float vb = bf2f(f2bf(q / 576.f)); float den = bf2f(f2bf(sqrtf(bf2f(f2bf(vb + 1e-5f)))));
  for (int i = 0; i < 9; ++i) xn[row * 576 + i * 64 + t] = bf2f(f2bf(bf2f(f2bf(v[i] - mb)) / den));
}
__global__ void n_gemm(const float* A, const u16* B, float* C, int M, int N, int K) {
  __shared__ float sa[16][65], sb[16][64];
  int tx = threadIdx.x & 15, ty = threadIdx.x >> 4;
  int r0 = blockIdx.y * 64, c0 = blockIdx.x * 64;
  float acc[4][4] = {};
  for (int k0 = 0; k0 < K; k0 += 16) {
    for (int i = threadIdx.x; i < 1024; i += 256) { int r = i >> 4, k = i & 15; sa[k][r] = A[(long)(r0 + r) * K + k0 + k]; }
    for (int i = threadIdx.x; i < 1024; i += 256) { int k = i >> 6, c = i & 63; sb[k][c] = bf2f(B[(long)(k0 + k) * N + c0 + c]); }
    __syncthreads();
    for (int k = 0; k < 16; ++k)
      for (int i = 0; i < 4; ++i) for (int j = 0; j < 4; ++j) acc[i][j] += sa[k][ty * 4 + i] * sb[k][tx * 4 + j];
    __syncthreads();
  }
  for (int i = 0; i < 4; ++i) for (int j = 0; j < 4; ++j) C[(long)(r0 + ty * 4 + i) * N + c0 + tx * 4 + j] = acc[i][j];
}
__global__ void n_attn(const float* qkv, float* o) {
  int h = blockIdx.y, q = blockIdx.x * 256 + threadIdx.x;
  __shared__ float sk[64][18], sv[64][18];
  float qv[18], acc[18];
  for (int d = 0; d < 18; ++d) { qv[d] = qkv[(long)q * 1728 + h * 18 + d] * 0.23570226039551584f; acc[d] = 0; }
  float m = -1e30f, l = 0;
  for (int k0 = 0; k0 < 2048; k0 += 64) {
    __syncthreads();
    for (int i = threadIdx.x; i < 64 * 18; i += 256) { int r = i / 18, d = i % 18; sk[r][d] = qkv[(long)(k0 + r) * 1728 + 576 + h * 18 + d]; sv[r][d] = qkv[(long)(k0 + r) * 1728 + 1152 + h * 18 + d]; }
    __syncthreads();
    for (int r = 0; r < 64; ++r) {
      float s = 0;
      for (int d = 0; d < 18; ++d) s += qv[d] * sk[r][d];
      float mn = fmaxf(m, s); float a = __expf(m - mn), p = __expf(s - mn);
      l = l * a + p;
      for (int d = 0; d < 18; ++d) acc[d] = acc[d] * a + p * sv[r][d];
      m = mn;
    }
  }
  for (int d = 0; d < 18; ++d) o[(long)q * 576 + h * 18 + d] = acc[d] / l;
}
__global__ void n_geglu(const float* g, const float* u, float* hh, long n) {
  long i = (long)blockIdx.x * 256 + threadIdx.x;
  if (i < n) { float x = g[i]; float t = tanhf(0.7978845608028654f * (x + 0.044715f * x * x * x)); hh[i] = 0.5f * x * (1.f + t) * u[i]; }
}
__global__ void n_final(const u16* x, const float* s, const float* y, u16* out, int n) {
  int i = blockIdx.x * 256 + threadIdx.x;
  if (i < n) { float yy = y[i]; float g = yy / (1.f + __expf(-yy)); out[i] = f2bf(bf2f(x[i]) + s[i] * g); }
}
extern "C" void kernel_launch(void* const* d_in, const int* in_sizes, int n_in, void* d_out, int out_size, void* d_ws, size_t ws_size, hipStream_t stream) {
  const u16* x = (const u16*)d_in[0]; const u16* wqkv = (const u16*)d_in[1]; const u16* wo = (const u16*)d_in[2];
  const u16* wg = (const u16*)d_in[4]; const u16* wu = (const u16*)d_in[5]; const u16* wd = (const u16*)d_in[6];
  float* w = (float*)d_ws;
  float* xn = w; w += 2048 * 576; float* qkv = w; w += 2048 * 1728; float* o = w; w += 2048 * 576; float* s = w; w += 2048 * 576;
  float* y = w; w += 2048 * 576; float* g = w; w += 2048 * 4608; float* u = w; w += 2048 * 4608; float* hh = w;
  n_ln<<<2048, 64, 0, stream>>>(x, xn);
  n_gemm<<<dim3(27, 32), 256, 0, stream>>>(xn, wqkv, qkv, 2048, 1728, 576);
  n_attn<<<dim3(8, 32), 256, 0, stream>>>(qkv, o);
  n_gemm<<<dim3(9, 32), 256, 0, stream>>>(o, wo, s, 2048, 576, 576);
  n_gemm<<<dim3(72, 32), 256, 0, stream>>>(xn, wg, g, 2048, 4608, 576);
  n_gemm<<<dim3(72, 32), 256, 0, stream>>>(xn, wu, u, 2048, 4608, 576);
  n_geglu<<<2048 * 4608 / 256, 256, 0, stream>>>(g, u, hh, 2048L * 4608);
  n_gemm<<<dim3(9, 32), 256, 0, stream>>>(hh, wd, y, 2048, 576, 4608);
  n_final<<<2048 * 576 / 256, 256, 0, stream>>>(x, s, y, (u16*)d_out, 2048 * 576);
}
```

```cpp
#include <hip/hip_runtime.h>
typedef unsigned short u16;
typedef unsigned int u32;
typedef __attribute__((ext_vector_type(2))) unsigned u32x2;
typedef __attribute__((ext_vector_type(4))) unsigned u32x4;
typedef __attribute__((ext_vector_type(8))) unsigned u32x8;
typedef __attribute__((ext_vector_type(16))) unsigned u32x16;
#define DI __device__ __forceinline__
#define WS_XN 0u
#define WS_H 2359296u
#define WS_QM 21233664u
#define WS_KV 23330816u
#define WS_QT 27787264u
#define WS_KT 28049408u
#define WS_WD 28311552u
#define WS_WO (WS_WD + 144u * 36864u)
#define WS_O 34283520u
#define WS_Y 36642816u
#define WS_F 39002112u
#define WS_END 39133312u
#define MEGA 1
#define MEGA2 1
DI u32 rfl(u32 v) { return __builtin_amdgcn_readfirstlane(v); }
DI u32x4 mk_srd(const void* p, u32 bytes) {
  unsigned long long a = (unsigned long long)p;
  u32x4 r; r.x = (u32)a; r.y = (u32)(a >> 32) & 0xffffu; r.z = bytes; r.w = 0x00020000u; return r;
}
DI u32 sel(bool c, u32 a, u32 b) { return c ? a : b; }
DI u32 mn(u32 a, u32 b) { return a < b ? a : b; }

__global__ __launch_bounds__(512, 2) void k_main(const u16* __restrict__ x, char* __restrict__ ws, const u16* __restrict__ wq, const u16* __restrict__ wg, const u16* __restrict__ wd,
                                                 const u16* __restrict__ wo, const u16* __restrict__ wu, u16* __restrict__ out) {
  __shared__ __attribute__((aligned(16))) char sm[163840];
  u32 b = blockIdx.x, l = threadIdx.x & 63, w = rfl(threadIdx.x >> 6);
  __builtin_assume(w < 8);
  const u32x4 sw = mk_srd(ws, WS_END), sxs = mk_srd(x, 2048 * 1152);
  u32x16 xa; u32x2 xb; u32 ep;
  asm volatile(
  "s_nop 4\n"
  "s_cmp_ge_u32 s68, 2\n"
  "s_cbranch_scc1 LA0_%=\n"
  "s_setprio 3\n"
  "buffer_load_dwordx2 v[112:113], v130, s[92:95], 0 offen offset:0\n"
  "buffer_load_dwordx2 v[114:115], v130, s[92:95], 0 offen offset:128\n"
  "buffer_load_dwordx2 v[116:117], v130, s[92:95], 0 offen offset:256\n"
  "buffer_load_dwordx2 v[118:119], v130, s[92:95], 0 offen offset:384\n"
  "buffer_load_dwordx2 v[120:121], v130, s[92:95], 0 offen offset:512\n"
  "buffer_load_dwordx2 v[122:123], v130, s[92:95], 0 offen offset:640\n"
  "buffer_load_dwordx2 v[124:125], v130, s[92:95], 0 offen offset:768\n"
  "buffer_load_dwordx2 v[126:127], v130, s[92:95], 0 offen offset:896\n"
  "buffer_load_dwordx2 v[128:129], v130, s[92:95], 0 offen offset:1024\n"
  "LA0_%=:\n"
  "s_cmp_lg_u32 s68, 7\n"
  "s_cbranch_scc1 LA0C_%=\n"
  "s_getpc_b64 s[52:53]\n"
  "KCODE0:\n"
  "s_mov_b32 s54, KCODE1-KCODE0\n"
  "s_mov_b32 s55, 0x20000\n"
  "buffer_load_dword v131, v137, s[52:55], 0 offen\n"
  "LA0C_%=:\n"
  "s_mov_b32 s43, 39133184\n"
  "buffer_load_dword v131, off, s[20:23], s43 sc1\n"
  : "={v[112:127]}"(xa), "={v[128:129]}"(xb), "={v131}"(ep), "+v"(l)
  : "{v130}"(((b >> 5) * 256 + (b & 7) * 32 + ((b >> 3) & 3) * 8 + 4 * w + (l >> 4)) * 1152 + (l & 15) * 8), "{s68}"(w), "{s[20:23]}"(sw), "{s[92:95]}"(sxs),
    "{v137}"(((b >> 3) & 7) * 8192 + l * 128)
  : "s43", "s52", "s53", "s54", "s55", "scc", "memory");
#define ROT(n) sel((b >> 5) + n < 9, (b >> 5) + n, (b >> 5) + n - 9)
#define DMAOPS "{v93}"(vwq), "{v94}"(vwg), "{s[8:11]}"(sq), "{s[12:15]}"(sg), "{s[48:51]}"(spf), "{v137}"(vpp)
#define DMACLB "v134", "v135", "v138", "s44", "s46", "s47", "scc", "memory"
#define FRESH asm volatile("" : "+s"(b), "+s"(w)); __builtin_assume(l < 64); __builtin_assume(b < 256); __builtin_assume(w < 8);
  FRESH
  const u32 lb = (u32)(size_t)(__attribute__((address_space(3))) char*)sm;
  const u32x4 sq = mk_srd(wq, 576 * 1728 * 2), sg = mk_srd(wg, 576 * 4608 * 2);
  const u32 swave = lb + w * 1024;
  const u32x4 spf = mk_srd(w & 2 ? wg : wu, 576 * 4608 * 2);
  u32 vwq, vwg, sln, vpp; const u32 l16 = l * 16;
  {
    const u32 j = (b & 7) * 4 + ((b >> 3) & 3);
    const u32 rr = l >> 3, sp16 = ((l & 7) ^ (rr & 2) ^ ((w & 1) << 2)) * 16;
    vwq = rr * 3456 + sp16 + (w * 8 * 3456 + sel(j < 27, j, j - 27) * 128); vwg = rr * 9216 + sp16 + (w * 8 * 9216 + j * 256);
    vpp = mn(l, 35) * 9216 + (((b >> 5) * 72 + (w & 1) * 36) * 9216 + j * 256 + (w & 2) * 64);
    sln = WS_XN + ((b >> 5) * 8 + (b & 7)) * 36864 + (j & 3) * 512 + w * 256;
  }
  asm volatile(
  "s_nop 4\n"
  "s_cmp_ge_u32 s68, 2\n"
  "s_cbranch_scc1 LA1N_%=\n"
  "s_lshl_b32 s43, s68, 13\n"
  "s_add_u32 s43, s43, 147456\n"
  "v_and_b32 v190, 0xe0, v100\n"
  "v_lshlrev_b32 v190, 1, v190\n"
  "v_lshrrev_b32 v176, 8, v100\n"
  "v_lshl_add_u32 v190, v176, 4, v190\n"
  "v_and_b32 v187, 0xf0, v100\n"
  "v_lshl_add_u32 v187, v176, 11, v187\n"
  "v_bfe_u32 v176, v100, 4, 1\n"
  "v_lshl_add_u32 v190, v176, 3, v190\n"
  "v_add_u32 v190, s43, v190\n"
  "v_add_u32 v191, s43, v100\n"
  "s_waitcnt vmcnt(9)\n"
  "v_lshlrev_b32 v188, 16, v112\n"
  "s_nop 1\n"
  "v_mov_b32_dpp v188, v188 row_newbcast:0 row_mask:0xf bank_mask:0xf\n"
  "v_lshlrev_b32 v140, 16, v112\n"
  "v_and_b32 v141, 0xffff0000, v112\n"
  "v_pk_add_f32 v[140:141], v[140:141], v[188:189] op_sel_hi:[1,0] neg_lo:[0,1] neg_hi:[0,1]\n"
  "v_lshlrev_b32 v142, 16, v113\n"
  "v_and_b32 v143, 0xffff0000, v113\n"
  "v_pk_add_f32 v[142:143], v[142:143], v[188:189] op_sel_hi:[1,0] neg_lo:[0,1] neg_hi:[0,1]\n"
  "v_pk_mul_f32 v[180:181], v[140:141], v[140:141]\n"
  "s_waitcnt vmcnt(8)\n"
  "v_lshlrev_b32 v144, 16, v114\n"
  "v_and_b32 v145, 0xffff0000, v114\n"
  "v_pk_add_f32 v[144:145], v[144:145], v[188:189] op_sel_hi:[1,0] neg_lo:[0,1] neg_hi:[0,1]\n"
  "v_pk_add_f32 v[176:177], v[140:141], v[142:143]\n"
  "v_pk_fma_f32 v[180:181], v[142:143], v[142:143], v[180:181]\n"
  "v_lshlrev_b32 v146, 16, v115\n"
  "v_and_b32 v147, 0xffff0000, v115\n"
  "v_pk_add_f32 v[146:147], v[146:147], v[188:189] op_sel_hi:[1,0] neg_lo:[0,1] neg_hi:[0,1]\n"
  "v_pk_add_f32 v[176:177], v[176:177], v[144:145]\n"
  "v_pk_fma_f32 v[180:181], v[144:145], v[144:145], v[180:181]\n"
  "s_waitcnt vmcnt(7)\n"
  "v_lshlrev_b32 v148, 16, v116\n"
  "v_and_b32 v149, 0xffff0000, v116\n"
  "v_pk_add_f32 v[148:149], v[148:149], v[188:189] op_sel_hi:[1,0] neg_lo:[0,1] neg_hi:[0,1]\n"
  "v_pk_add_f32 v[176:177], v[176:177], v[146:147]\n"
  "v_pk_fma_f32 v[180:181], v[146:147], v[146:147], v[180:181]\n"
  "v_lshlrev_b32 v150, 16, v117\n"
  "v_and_b32 v151, 0xffff0000, v117\n"
  "v_pk_add_f32 v[150:151], v[150:151], v[188:189] op_sel_hi:[1,0] neg_lo:[0,1] neg_hi:[0,1]\n"
  "v_pk_add_f32 v[176:177], v[176:177], v[148:149]\n"
  "v_pk_fma_f32 v[180:181], v[148:149], v[148:149], v[180:181]\n"
  "s_waitcnt vmcnt(6)\n"
  "v_lshlrev_b32 v152, 16, v118\n"
  "v_and_b32 v153, 0xffff0000, v118\n"
  "v_pk_add_f32 v[152:153], v[152:153], v[188:189] op_sel_hi:[1,0] neg_lo:[0,1] neg_hi:[0,1]\n"
  "v_pk_add_f32 v[176:177], v[176:177], v[150:151]\n"
  "v_pk_fma_f32 v[180:181], v[150:151], v[150:151], v[180:181]\n"
  "v_lshlrev_b32 v154, 16, v119\n"
  "v_and_b32 v155, 0xffff0000, v119\n"
  "v_pk_add_f32 v[154:155], v[154:155], v[188:189] op_sel_hi:[1,0] neg_lo:[0,1] neg_hi:[0,1]\n"
  "v_pk_add_f32 v[176:177], v[176:177], v[152:153]\n"
  "v_pk_fma_f32 v[180:181], v[152:153], v[152:153], v[180:181]\n"
  "s_waitcnt vmcnt(5)\n"
  "v_lshlrev_b32 v156, 16, v120\n"
  "v_and_b32 v157, 0xffff0000, v120\n"
  "v_pk_add_f32 v[156:157], v[156:157], v[188:189] op_sel_hi:[1,0] neg_lo:[0,1] neg_hi:[0,1]\n"
  "v_pk_add_f32 v[176:177], v[176:177], v[154:155]\n"
  "v_pk_fma_f32 v[180:181], v[154:155], v[154:155], v[180:181]\n"
  "v_lshlrev_b32 v158, 16, v121\n"
  "v_and_b32 v159, 0xffff0000, v121\n"
  "v_pk_add_f32 v[158:159], v[158:159], v[188:189] op_sel_hi:[1,0] neg_lo:[0,1] neg_hi:[0,1]\n"
  "v_pk_add_f32 v[176:177], v[176:177], v[156:157]\n"
  "v_pk_fma_f32 v[180:181], v[156:157], v[156:157], v[180:181]\n"
  "s_waitcnt vmcnt(4)\n"
  "v_lshlrev_b32 v160, 16, v122\n"
  "v_and_b32 v161, 0xffff0000, v122\n"
  "v_pk_add_f32 v[160:161], v[160:161], v[188:189] op_sel_hi:[1,0] neg_lo:[0,1] neg_hi:[0,1]\n"
  "v_pk_add_f32 v[176:177], v[176:177], v[158:159]\n"
  "v_pk_fma_f32 v[180:181], v[158:159], v[158:159], v[180:181]\n"
  "v_lshlrev_b32 v162, 16, v123\n"
  "v_and_b32 v163, 0xffff0000, v123\n"
  "v_pk_add_f32 v[162:163], v[162:163], v[188:189] op_sel_hi:[1,0] neg_lo:[0,1] neg_hi:[0,1]\n"
  "v_pk_add_f32 v[176:177], v[176:177], v[160:161]\n"
  "v_pk_fma_f32 v[180:181], v[160:161], v[160:161], v[180:181]\n"
  "s_waitcnt vmcnt(3)\n"
  "v_lshlrev_b32 v164, 16, v124\n"
  "v_and_b32 v165, 0xffff0000, v124\n"
  "v_pk_add_f32 v[164:165], v[164:165], v[188:189] op_sel_hi:[1,0] neg_lo:[0,1] neg_hi:[0,1]\n"
  "v_pk_add_f32 v[176:177], v[176:177], v[162:163]\n"
  "v_pk_fma_f32 v[180:181], v[162:163], v[162:163], v[180:181]\n"
  "v_lshlrev_b32 v166, 16, v125\n"
  "v_and_b32 v167, 0xffff0000, v125\n"
  "v_pk_add_f32 v[166:167], v[166:167], v[188:189] op_sel_hi:[1,0] neg_lo:[0,1] neg_hi:[0,1]\n"
  "v_pk_add_f32 v[176:177], v[176:177], v[164:165]\n"
  "v_pk_fma_f32 v[180:181], v[164:165], v[164:165], v[180:181]\n"
  "s_waitcnt vmcnt(2)\n"
  "v_lshlrev_b32 v168, 16, v126\n"
  "v_and_b32 v169, 0xffff0000, v126\n"
  "v_pk_add_f32 v[168:169], v[168:169], v[188:189] op_sel_hi:[1,0] neg_lo:[0,1] neg_hi:[0,1]\n"
  "v_pk_add_f32 v[176:177], v[176:177], v[166:167]\n"
  "v_pk_fma_f32 v[180:181], v[166:167], v[166:167], v[180:181]\n"
  "v_lshlrev_b32 v170, 16, v127\n"
  "v_and_b32 v171, 0xffff0000, v127\n"
  "v_pk_add_f32 v[170:171], v[170:171], v[188:189] op_sel_hi:[1,0] neg_lo:[0,1] neg_hi:[0,1]\n"
  "v_pk_add_f32 v[176:177], v[176:177], v[168:169]\n"
  "v_pk_fma_f32 v[180:181], v[168:169], v[168:169], v[180:181]\n"
  "s_waitcnt vmcnt(1)\n"
  "v_lshlrev_b32 v172, 16, v128\n"
  "v_and_b32 v173, 0xffff0000, v128\n"
  "v_pk_add_f32 v[172:173], v[172:173], v[188:189] op_sel_hi:[1,0] neg_lo:[0,1] neg_hi:[0,1]\n"
  "v_pk_add_f32 v[176:177], v[176:177], v[170:171]\n"
  "v_pk_fma_f32 v[180:181], v[170:171], v[170:171], v[180:181]\n"
  "v_lshlrev_b32 v174, 16, v129\n"
  "v_and_b32 v175, 0xffff0000, v129\n"
  "v_pk_add_f32 v[174:175], v[174:175], v[188:189] op_sel_hi:[1,0] neg_lo:[0,1] neg_hi:[0,1]\n"
  "v_pk_add_f32 v[176:177], v[176:177], v[172:173]\n"
  "v_pk_fma_f32 v[180:181], v[172:173], v[172:173], v[180:181]\n"
  "v_pk_add_f32 v[176:177], v[176:177], v[174:175]\n"
  "v_pk_fma_f32 v[180:181], v[174:175], v[174:175], v[180:181]\n"
  "v_add_f32 v176, v176, v177\n"
  "v_add_f32 v180, v180, v181\n"
  "s_nop 0\n"
  "v_add_f32_dpp v176, v176, v176 row_ror:8 row_mask:0xf bank_mask:0xf\n"
  "v_add_f32_dpp v180, v180, v180 row_ror:8 row_mask:0xf bank_mask:0xf\n"
  "s_nop 0\n"
  "v_add_f32_dpp v176, v176, v176 row_ror:4 row_mask:0xf bank_mask:0xf\n"
  "v_add_f32_dpp v180, v180, v180 row_ror:4 row_mask:0xf bank_mask:0xf\n"
  "s_nop 0\n"
  "v_add_f32_dpp v176, v176, v176 row_ror:2 row_mask:0xf bank_mask:0xf\n"
  "v_add_f32_dpp v180, v180, v180 row_ror:2 row_mask:0xf bank_mask:0xf\n"
  "s_nop 0\n"
  "v_add_f32_dpp v176, v176, v176 row_ror:1 row_mask:0xf bank_mask:0xf\n"
  "v_add_f32_dpp v180, v180, v180 row_ror:1 row_mask:0xf bank_mask:0xf\n"
  "v_mul_f32 v176, 0x3ae38e39, v176\n"
  "v_mul_f32 v180, 0x3ae38e39, v180\n"
  "v_fma_f32 v180, -v176, v176, v180\n"
  "v_max_f32 v180, 0, v180\n"
  "v_add_f32 v176, v176, v188\n"
  "v_cvt_pk_bf16_f32 v180, v180, v176\n"
  "v_and_b32 v176, 0xffff0000, v180\n"
  "v_lshlrev_b32 v180, 16, v180\n"
  "v_sub_f32 v176, v188, v176\n"
  "v_add_f32 v180, 0x3727c5ac, v180\n"
  "s_add_u32 s43, s70, 0\n"
  "v_pk_add_f32 v[140:141], v[140:141], v[176:177] op_sel_hi:[1,0]\n"
  "v_cvt_pk_bf16_f32 v180, v180, v180\n"
  "v_pk_add_f32 v[142:143], v[142:143], v[176:177] op_sel_hi:[1,0]\n"
  "v_lshlrev_b32 v180, 16, v180\n"
  "v_cvt_pk_bf16_f32 v112, v140, v141\n"
  "v_pk_add_f32 v[144:145], v[144:145], v[176:177] op_sel_hi:[1,0]\n"
  "v_sqrt_f32 v180, v180\n"
  "v_cvt_pk_bf16_f32 v113, v142, v143\n"
  "v_pk_add_f32 v[146:147], v[146:147], v[176:177] op_sel_hi:[1,0]\n"
  "v_cvt_pk_bf16_f32 v114, v144, v145\n"
  "v_pk_add_f32 v[148:149], v[148:149], v[176:177] op_sel_hi:[1,0]\n"
  "v_cvt_pk_bf16_f32 v180, v180, v180\n"
  "v_cvt_pk_bf16_f32 v115, v146, v147\n"
  "v_pk_add_f32 v[150:151], v[150:151], v[176:177] op_sel_hi:[1,0]\n"
  "v_lshlrev_b32 v180, 16, v180\n"
  "v_cvt_pk_bf16_f32 v116, v148, v149\n"
  "v_pk_add_f32 v[152:153], v[152:153], v[176:177] op_sel_hi:[1,0]\n"
  "v_rcp_f32 v180, v180\n"
  "v_cvt_pk_bf16_f32 v117, v150, v151\n"
  "v_pk_add_f32 v[154:155], v[154:155], v[176:177] op_sel_hi:[1,0]\n"
  "v_cvt_pk_bf16_f32 v118, v152, v153\n"
  "v_pk_add_f32 v[156:157], v[156:157], v[176:177] op_sel_hi:[1,0]\n"
  "v_cvt_pk_bf16_f32 v119, v154, v155\n"
  "v_pk_add_f32 v[158:159], v[158:159], v[176:177] op_sel_hi:[1,0]\n"
  "v_cvt_pk_bf16_f32 v120, v156, v157\n"
  "v_pk_add_f32 v[160:161], v[160:161], v[176:177] op_sel_hi:[1,0]\n"
  "v_cvt_pk_bf16_f32 v121, v158, v159\n"
  "v_pk_add_f32 v[162:163], v[162:163], v[176:177] op_sel_hi:[1,0]\n"
  "v_cvt_pk_bf16_f32 v122, v160, v161\n"
  "v_pk_add_f32 v[164:165], v[164:165], v[176:177] op_sel_hi:[1,0]\n"
  "v_cvt_pk_bf16_f32 v123, v162, v163\n"
  "v_pk_add_f32 v[166:167], v[166:167], v[176:177] op_sel_hi:[1,0]\n"
  "v_cvt_pk_bf16_f32 v124, v164, v165\n"
  "v_pk_add_f32 v[168:169], v[168:169], v[176:177] op_sel_hi:[1,0]\n"
  "v_cvt_pk_bf16_f32 v125, v166, v167\n"
  "v_pk_add_f32 v[170:171], v[170:171], v[176:177] op_sel_hi:[1,0]\n"
  "v_cvt_pk_bf16_f32 v126, v168, v169\n"
  "v_pk_add_f32 v[172:173], v[172:173], v[176:177] op_sel_hi:[1,0]\n"
  "v_cvt_pk_bf16_f32 v127, v170, v171\n"
  "v_pk_add_f32 v[174:175], v[174:175], v[176:177] op_sel_hi:[1,0]\n"
  "v_cvt_pk_bf16_f32 v128, v172, v173\n"
  "v_cvt_pk_bf16_f32 v129, v174, v175\n"
  "v_lshlrev_b32 v140, 16, v112\n"
  "v_and_b32 v141, 0xffff0000, v112\n"
  "v_lshlrev_b32 v142, 16, v113\n"
  "v_and_b32 v143, 0xffff0000, v113\n"
  "v_pk_mul_f32 v[140:141], v[140:141], v[180:181] op_sel_hi:[1,0]\n"
  "v_pk_mul_f32 v[142:143], v[142:143], v[180:181] op_sel_hi:[1,0]\n"
  "v_cvt_pk_bf16_f32 v112, v140, v141\n"
  "v_cvt_pk_bf16_f32 v113, v142, v143\n"
  "ds_write_b64 v190, v[112:113] offset:0\n"
  "v_lshlrev_b32 v144, 16, v114\n"
  "v_and_b32 v145, 0xffff0000, v114\n"
  "v_lshlrev_b32 v146, 16, v115\n"
  "v_and_b32 v147, 0xffff0000, v115\n"
  "v_pk_mul_f32 v[144:145], v[144:145], v[180:181] op_sel_hi:[1,0]\n"
  "v_pk_mul_f32 v[146:147], v[146:147], v[180:181] op_sel_hi:[1,0]\n"
  "v_cvt_pk_bf16_f32 v114, v144, v145\n"
  "v_cvt_pk_bf16_f32 v115, v146, v147\n"
  "ds_write_b64 v190, v[114:115] offset:512\n"
  "ds_read_b128 v[192:195], v191 offset:0\n"
  "v_lshlrev_b32 v148, 16, v116\n"
  "v_and_b32 v149, 0xffff0000, v116\n"
  "v_lshlrev_b32 v150, 16, v117\n"
  "v_and_b32 v151, 0xffff0000, v117\n"
  "v_pk_mul_f32 v[148:149], v[148:149], v[180:181] op_sel_hi:[1,0]\n"
  "v_pk_mul_f32 v[150:151], v[150:151], v[180:181] op_sel_hi:[1,0]\n"
  "v_cvt_pk_bf16_f32 v116, v148, v149\n"
  "v_cvt_pk_bf16_f32 v117, v150, v151\n"
  "ds_write_b64 v190, v[116:117] offset:1024\n"
  "v_lshlrev_b32 v152, 16, v118\n"
  "v_and_b32 v153, 0xffff0000, v118\n"
  "v_lshlrev_b32 v154, 16, v119\n"
  "v_and_b32 v155, 0xffff0000, v119\n"
  "v_pk_mul_f32 v[152:153], v[152:153], v[180:181] op_sel_hi:[1,0]\n"
  "v_pk_mul_f32 v[154:155], v[154:155], v[180:181] op_sel_hi:[1,0]\n"
  "v_cvt_pk_bf16_f32 v118, v152, v153\n"
  "v_cvt_pk_bf16_f32 v119, v154, v155\n"
  "ds_write_b64 v190, v[118:119] offset:1536\n"
  "ds_read_b128 v[196:199], v191 offset:1024\n"
  "v_lshlrev_b32 v156, 16, v120\n"
  "v_and_b32 v157, 0xffff0000, v120\n"
  "v_lshlrev_b32 v158, 16, v121\n"
  "v_and_b32 v159, 0xffff0000, v121\n"
  "v_pk_mul_f32 v[156:157], v[156:157], v[180:181] op_sel_hi:[1,0]\n"
  "v_pk_mul_f32 v[158:159], v[158:159], v[180:181] op_sel_hi:[1,0]\n"
  "v_cvt_pk_bf16_f32 v120, v156, v157\n"
  "v_cvt_pk_bf16_f32 v121, v158, v159\n"
  "ds_write_b64 v190, v[120:121] offset:2048\n"
  "v_lshlrev_b32 v160, 16, v122\n"
  "v_and_b32 v161, 0xffff0000, v122\n"
  "v_lshlrev_b32 v162, 16, v123\n"
  "v_and_b32 v163, 0xffff0000, v123\n"
  "v_pk_mul_f32 v[160:161], v[160:161], v[180:181] op_sel_hi:[1,0]\n"
  "v_pk_mul_f32 v[162:163], v[162:163], v[180:181] op_sel_hi:[1,0]\n"
  "v_cvt_pk_bf16_f32 v122, v160, v161\n"
  "v_cvt_pk_bf16_f32 v123, v162, v163\n"
  "ds_write_b64 v190, v[122:123] offset:2560\n"
  "ds_read_b128 v[200:203], v191 offset:2048\n"
  "v_lshlrev_b32 v164, 16, v124\n"
  "v_and_b32 v165, 0xffff0000, v124\n"
  "v_lshlrev_b32 v166, 16, v125\n"
  "v_and_b32 v167, 0xffff0000, v125\n"
  "v_pk_mul_f32 v[164:165], v[164:165], v[180:181] op_sel_hi:[1,0]\n"
  "v_pk_mul_f32 v[166:167], v[166:167], v[180:181] op_sel_hi:[1,0]\n"
  "v_cvt_pk_bf16_f32 v124, v164, v165\n"
  "v_cvt_pk_bf16_f32 v125, v166, v167\n"
  "ds_write_b64 v190, v[124:125] offset:3072\n"
  "v_lshlrev_b32 v168, 16, v126\n"
  "v_and_b32 v169, 0xffff0000, v126\n"
  "v_lshlrev_b32 v170, 16, v127\n"
  "v_and_b32 v171, 0xffff0000, v127\n"
  "v_pk_mul_f32 v[168:169], v[168:169], v[180:181] op_sel_hi:[1,0]\n"
  "v_pk_mul_f32 v[170:171], v[170:171], v[180:181] op_sel_hi:[1,0]\n"
  "v_cvt_pk_bf16_f32 v126, v168, v169\n"
  "v_cvt_pk_bf16_f32 v127, v170, v171\n"
  "ds_write_b64 v190, v[126:127] offset:3584\n"
  "ds_read_b128 v[204:207], v191 offset:3072\n"
  "s_waitcnt lgkmcnt(9)\n"
  "buffer_store_dwordx4 v[192:195], v187, s[20:23], s43 offen sc1\n"
  "v_lshlrev_b32 v172, 16, v128\n"
  "v_and_b32 v173, 0xffff0000, v128\n"
  "v_lshlrev_b32 v174, 16, v129\n"
  "v_and_b32 v175, 0xffff0000, v129\n"
  "v_pk_mul_f32 v[172:173], v[172:173], v[180:181] op_sel_hi:[1,0]\n"
  "v_pk_mul_f32 v[174:175], v[174:175], v[180:181] op_sel_hi:[1,0]\n"
  "v_cvt_pk_bf16_f32 v128, v172, v173\n"
  "v_cvt_pk_bf16_f32 v129, v174, v175\n"
  "ds_write_b64 v190, v[128:129] offset:4096\n"
  "ds_read_b128 v[208:211], v191 offset:4096\n"
  "s_add_u32 s43, s70, 8192\n"
  "s_waitcnt lgkmcnt(8)\n"
  "buffer_store_dwordx4 v[196:199], v187, s[20:23], s43 offen sc1\n"
  "s_add_u32 s43, s70, 16384\n"
  "s_waitcnt lgkmcnt(5)\n"
  "buffer_store_dwordx4 v[200:203], v187, s[20:23], s43 offen sc1\n"
  "s_add_u32 s43, s70, 24576\n"
  "s_waitcnt lgkmcnt(2)\n"
  "buffer_store_dwordx4 v[204:207], v187, s[20:23], s43 offen sc1\n"
  "s_add_u32 s43, s70, 32768\n"
  "s_mov_b32 exec_hi, 0\n"
  "s_waitcnt lgkmcnt(0)\n"
  "buffer_store_dwordx4 v[208:211], v187, s[20:23], s43 offen sc1\n"
  "s_mov_b64 exec, -1\n"
  "s_branch LA1_%=\n"
  "LA1N_%=:\n"
  "v_subrev_u32 v134, 55296, v93\n"
  "v_subrev_u32 v135, 147456, v94\n"
  "s_mov_b32 s47, 1\n"
  "s_mov_b32 s44, s34\n"
  "LA1L_%=:\n"
  "s_lshl_b32 m0, s44, 14\n"
  "s_add_u32 m0, m0, s28\n"
  "s_mul_i32 s46, s44, 221184\n"
  "buffer_load_dwordx4 v93, s[8:11], s46 offen lds\n"
  "s_cmp_ge_u32 s68, 4\n"
  "s_cbranch_scc1 LA1S_%=\n"
  "s_sub_u32 m0, m0, 2048\n"
  "s_nop 0\n"
  "buffer_load_dwordx4 v134, s[8:11], s46 offen lds\n"
  "s_add_u32 m0, m0, 8192\n"
  "s_mul_i32 s46, s44, 589824\n"
  "buffer_load_dwordx4 v135, s[12:15], s46 offen lds\n"
  "s_sub_u32 m0, m0, 6144\n"
  "LA1S_%=:\n"
  "s_add_u32 m0, m0, 8192\n"
  "s_mul_i32 s46, s44, 589824\n"
  "buffer_load_dwordx4 v94, s[12:15], s46 offen lds\n"
  "s_add_u32 s44, s44, 1\n"
  "s_cmp_eq_u32 s44, 9\n"
  "s_cselect_b32 s44, 0, s44\n"
  "s_sub_u32 s47, s47, 1\n"
  "s_cmp_lg_u32 s47, 0\n"
  "s_cbranch_scc1 LA1L_%=\n"
  "LA1_%=:\n"
  : "+v"(l), "+{v[112:127]}"(xa), "+{v[128:129]}"(xb)
  : DMAOPS, "{v100}"(l16), "{s68}"(w), "{s70}"(sln), "{s28}"(swave), "{s[20:23]}"(sw), "{s34}"(b >> 5)
  : "v139", "v140", "v141", "v142", "v143", "v144", "v145", "v146", "v147", "v148", "v149", "v150", "v151", "v152", "v153", "v154", "v155", "v156", "v157", "v158", "v159", "v160", "v161", "v162", "v163", "v164", "v165", "v166", "v167", "v168", "v169", "v170", "v171", "v172", "v173", "v174", "v175", "v176", "v177", "v178", "v179", "v180", "v181", "v182", "v183", "v184", "v185", "v186", "v187", "v188", "v189", "v190", "v191", "v192", "v193", "v194", "v195", "v196", "v197", "v198", "v199", "v200", "v201", "v202", "v203", "v204", "v205", "v206", "v207", "v208", "v209", "v210", "v211", "s43", DMACLB);
  FRESH
  u32 g = l >> 4, i = l & 15, q = i >> 2, p = i & 3, rd16, vw16;
  u32x4 rd, st;
  {
    const u32 j = (b & 7) * 4 + ((b >> 3) & 3), rt = b >> 5;
    const u32 key = (q >> 1) | ((g & 1) << 1);
#pragma unroll
    for (int c = 0; c < 4; ++c) rd[c] = lb + (8 * g + q) * 128 + ((c ^ key) * 32) + 8 * p;
    rd16 = lb + (q | ((g & 1) << 2) | ((g >> 1) << 4)) * 32 + 8 * p;
    const u32 pos = l >> 1, row16 = (pos & 3) | (((pos >> 3) & 1) << 2) | (((pos >> 2) & 1) << 3) | (pos & 16);
    vw16 = row16 * 9216 + (l & 1) * 16 + (4096 + 16 * j) * 2;
    const u32 tile = rt * 8 + w;
    const u32 sec = (j * 57) >> 9; const bool qk = sec < 2;
    const u32 G0 = (j - 9 * sec) * 16 + sel(qk, g, q);
    const u32 mb = sel(sec == 0, WS_QM + tile * 1024, WS_KV + (tile >> 4) * 34816 + sel(qk, (tile & 15) * 1024, 16384 + (tile & 15) * 1152));
    const u32 hs = sel(sec == 0, 65536u, 139264u), ts = sel(qk, 16384u, 2 * 139264u), a1 = sel(qk, 9, 8), a2 = sel(qk, 7, 8);
    const u32 ml = sel(sec < 3, mb, 0x7ff00000u) + sel(qk, i * 16, (g & 1) * 128 + (g >> 1) * 64 + p * 16);
    const u32 tl = sel(sec < 3, sel(qk, sel(sec == 0, WS_QT, WS_KT) + tile * 256, mb + 1024), 0x7ff00000u) + sel(qk, i * 16, (p >> 1) * 139264 + (g >> 1) * 64 + (g & 1) * 32 + (p & 1) * 16);
#pragma unroll
    for (int c = 0; c < 4; ++c) {
      const u32 G = G0 + 4 * c, P = (G * 57) >> 9, r9 = G - 9 * P, up = r9 > 4, m = r9 - 5 * up;
      st[c] = sel(r9 == 4, tl + __umul24(P, ts), ml + __umul24(2 * P + up, hs) + ((m & 1) << a1) + ((m & 2) << a2));
    }
  }
  asm volatile(
  "s_nop 4\n"
  "s_cmp_lt_u32 s68, 2\n"
  "s_cbranch_scc1 LA1_%=\n"
  "LA1N_%=:\n"
  "v_subrev_u32 v134, 55296, v93\n"
  "v_subrev_u32 v135, 147456, v94\n"
  "s_mov_b32 s47, 3\n"
  "s_mov_b32 s44, s34\n"
  "LA1L_%=:\n"
  "s_lshl_b32 m0, s44, 14\n"
  "s_add_u32 m0, m0, s28\n"
  "s_mul_i32 s46, s44, 221184\n"
  "buffer_load_dwordx4 v93, s[8:11], s46 offen lds\n"
  "s_cmp_ge_u32 s68, 4\n"
  "s_cbranch_scc1 LA1S_%=\n"
  "s_sub_u32 m0, m0, 2048\n"
  "s_nop 0\n"
  "buffer_load_dwordx4 v134, s[8:11], s46 offen lds\n"
  "s_add_u32 m0, m0, 8192\n"
  "s_mul_i32 s46, s44, 589824\n"
  "buffer_load_dwordx4 v135, s[12:15], s46 offen lds\n"
  "s_sub_u32 m0, m0, 6144\n"
  "LA1S_%=:\n"
  "s_add_u32 m0, m0, 8192\n"
  "s_mul_i32 s46, s44, 589824\n"
  "buffer_load_dwordx4 v94, s[12:15], s46 offen lds\n"
  "s_add_u32 s44, s44, 1\n"
  "s_cmp_eq_u32 s44, 9\n"
  "s_cselect_b32 s44, 0, s44\n"
  "s_sub_u32 s47, s47, 1\n"
  "s_cmp_lg_u32 s47, 0\n"
  "s_cbranch_scc1 LA1L_%=\n"
  "LA1_%=:\n"
  : "+v"(l), "+{v[88:91]}"(rd), "+{v92}"(rd16), "+{v95}"(vw16), "+{v[96:99]}"(st), "+v"(g), "+v"(i), "+v"(q), "+v"(p)
  : DMAOPS, "{s68}"(w), "{s28}"(swave), "{s34}"(ROT(1))
  : DMACLB);
  FRESH
  const u32 j = (b & 7) * 4 + ((b >> 3) & 3), rt = b >> 5, tile = rt * 8 + w, rr = (l >> 2) & 7;
  const u32 grp = (b * 57) >> 9, db = b - 9 * grp;
  const bool isd = grp < 18, ism = grp < 20;
  const u32 ustr = sel(isd, 32, sel(ism, 36, 288)) * 1152, ub0 = sel(isd, grp * 256, sel(ism, (grp - 18) * 288, 16)) * 1152 + db * 128;
  const u32 sra = sel(isd, 8 * 1152, sel(ism, 18 * 1152, 144 * 1152)); u32 srb = sel(isd, 16 * 1152, sel(ism, 8 * 1152, 36 * 1152));
  u32x4 srel = mk_srd(isd ? wd : wo, sel(isd, 4608u * 1152u, 576u * 1152u)), su = mk_srd(wu, 576 * 4608 * 2);
  u32x2 vrel, v100;
  vrel[0] = sel(ism, rr, 72 * (rr >> 2) + 9 * (rr & 2) + (rr & 1)) * 1152 + p * 16 + sel(l < 32, 0, sra) + sel(grp * 8 + w < 162, ub0 + w * ustr, 0x7ff00000u);
  vrel[1] = (rt * 72 + w * 9 + mn(l & 15, 8)) * 9216 + sel(l < 32, j * 256 + 128, 8192 + j * 32);
  u32x4 ssel = mk_srd(w < 2 ? wg : wu, 576 * 4608 * 2), sout = mk_srd(out, 2048 * 1152);
  const u32 hb = WS_H + (tile >> 1) * 589824;
  u32x4 s28; s28[0] = swave; s28[1] = WS_XN + tile * 36864; s28[2] = hb + (w & 1) * 2048 + 4 * j * 4096; s28[3] = hb + 128 * 4096 + j * 2048 + (w & 1) * 1024;
  u32x4 s40; s40[0] = sel(j < 9, __float_as_uint(0.23570226039551584f * 1.4426950408889634f), 0x3f800000u); s40[1] = w * 32 * 9216; s40[2] = (j + 14) >> 5; s40[3] = 0;
  u32 s16x = sel(w < 2, 16u * 32 * 9216, 0u - 2u * 32 * 9216);
  v100[0] = l16; v100[1] = q * 256 + g * 64 + p * 16;
  u32 vpx = (rt * 32 + ((l >> 1) & 3) * 8 + w) * 512 + (l & 1) * 4;
  asm volatile("" : "+{s[36:39]}"(srel), "+{s[24:27]}"(ssel), "+{s[28:31]}"(s28), "+{s[40:43]}"(s40), "+{s45}"(s16x), "+{s35}"(srb));
  asm volatile(
  "s_nop 4\n"
  "s_cmp_ge_u32 s68, 2\n"
  "s_cbranch_scc1 LA1N_%=\n"
  "s_waitcnt vmcnt(5)\n"
  "v_readfirstlane_b32 s47, v131\n"
  "s_nop 3\n"
  "s_mul_i32 s47, s47, 0x9e3779b1\n"
  "s_add_u32 s47, s47, 0x6d2b79f5\n"
  "s_waitcnt vmcnt(0)\n"
  "v_mov_b32 v134, s47\n"
  "s_mov_b64 exec, 1\n"
  "buffer_store_dword v134, off, s[20:23], s69 offset:0 sc1\n"
  "s_mov_b64 exec, -1\n"
  "s_setprio 0\n"
  "s_branch LA1_%=\n"
  "LA1N_%=:\n"
  "v_subrev_u32 v134, 55296, v93\n"
  "v_subrev_u32 v135, 147456, v94\n"
  "s_mov_b32 s47, 3\n"
  "s_mov_b32 s44, s34\n"
  "LA1L_%=:\n"
  "s_lshl_b32 m0, s44, 14\n"
  "s_add_u32 m0, m0, s28\n"
  "s_mul_i32 s46, s44, 221184\n"
  "buffer_load_dwordx4 v93, s[8:11], s46 offen lds\n"
  "s_cmp_ge_u32 s68, 4\n"
  "s_cbranch_scc1 LA1S_%=\n"
  "s_sub_u32 m0, m0, 2048\n"
  "s_nop 0\n"
  "buffer_load_dwordx4 v134, s[8:11], s46 offen lds\n"
  "s_add_u32 m0, m0, 8192\n"
  "s_mul_i32 s46, s44, 589824\n"
  "buffer_load_dwordx4 v135, s[12:15], s46 offen lds\n"
  "s_sub_u32 m0, m0, 6144\n"
  "LA1S_%=:\n"
  "s_add_u32 m0, m0, 8192\n"
  "s_mul_i32 s46, s44, 589824\n"
  "buffer_load_dwordx4 v94, s[12:15], s46 offen lds\n"
  "s_add_u32 s44, s44, 1\n"
  "s_cmp_eq_u32 s44, 9\n"
  "s_cselect_b32 s44, 0, s44\n"
  "s_sub_u32 s47, s47, 1\n"
  "s_cmp_lg_u32 s47, 0\n"
  "s_cbranch_scc1 LA1L_%=\n"
  "s_cmp_lt_u32 s68, 4\n"
  "s_cbranch_scc1 LA1_%=\n"
  "buffer_load_dword v138, v137, s[48:51], 0 offen\n"
  "LA1_%=:\n"
  : "+v"(l), "+{v[84:85]}"(vrel), "+{v[100:101]}"(v100), "+{v136}"(vpx), "+v"(g), "+v"(i), "+v"(q), "+v"(p)
  : DMAOPS, "{s68}"(w), "{s69}"(WS_F + b * 512 + w * 4), "{s[20:23]}"(sw), "{v131}"(ep), "{s[28:31]}"(s28), "{s34}"(ROT(4))
  : DMACLB);
  FRESH
  __builtin_assume(g < 4); __builtin_assume(i < 16); __builtin_assume(q < 4); __builtin_assume(p < 4);
  u32x16 sa; u32x8 sb; u32x2 v110;
  {
    const u32 grp = (b * 57) >> 9, db = b - 9 * grp, ksr = grp * 8 + w;
    const u32 xcd = b & 7, idx = b >> 3, w3 = w & 3; const bool att = w < 4;
    const u32 head = xcd * 4 + (idx >> 3), tile0 = (idx & 7) * 8 + 2 * w3, P = head >> 1, odd = head & 1;
    const u32 sk = xcd & 1, R = (xcd >> 1) * 8 + (idx >> 2), cg = idx & 3, c = sk * 4 + w3, rb = 2 * R + sk;
    const u32 nl = sel(att, 8 - 8 * (cg & 1), (cg & 1) + 2 * w3), nt = 9 * cg + nl, ah = sel(att, w & 1, 0);
    sa[0] = w; sa[1] = WS_F + b * 512 + w * 4; sa[2] = 0;
    sa[3] = WS_WD + (mn(ksr, 161) * 36 + 4 * db) * 1024;
    sa[4] = WS_KV + head * 139264 + w * 1024; sa[5] = WS_KT + P * 16384 + 4 * odd + (w >> 1) * 1024 + (w & 1) * 128;
    sa[6] = WS_QM + (head * 64 + tile0) * 1024; sa[7] = WS_QT + (P * 64 + tile0) * 256 + 4 * odd;
    sa[8] = sel(ksr < 162, WS_END, 0u);
    sa[9] = sel(att, WS_O + tile0 * 36864 + head * 512, WS_H + R * 589824 + c * 65536);
    sa[10] = sel(att, WS_O + tile0 * 36864 + 32768 + head * 128, WS_WD + 16 * c * 36864 + 9 * cg * 1024);
    sa[11] = 128 - 14 * c; sa[12] = 0; sa[13] = 0; sa[14] = 0; sa[15] = WS_Y + ((R * 4 + cg) * 4 + w3) * 4608;
    sb[0] = (w3 >> 1) ^ sk ^ 1; sb[1] = WS_O + rb * 36864; sb[2] = WS_WO + nt * 1024;
    sb[3] = (rb * 32 + ah * 16) * 1152 + (nt >> 1) * 64 + (nt & 1) * 8;
    sb[4] = 36864 + (2 * sk + ah) * 27648 + nl * 1024; sb[5] = 36864 + 2 * 27648 - sk * 2 * 27648; sb[6] = (b * 8 + (w ^ 3) - 1) >> 31; sb[7] = 0;
    asm volatile(
  "s_nop 4\n"
  "s_cmp_lt_u32 s68, 2\n"
  "s_cbranch_scc1 LA1_%=\n"
  "LA1N_%=:\n"
  "v_subrev_u32 v134, 55296, v93\n"
  "v_subrev_u32 v135, 147456, v94\n"
  "s_mov_b32 s47, 2\n"
  "s_mov_b32 s44, s34\n"
  "LA1L_%=:\n"
  "s_lshl_b32 m0, s44, 14\n"
  "s_add_u32 m0, m0, s28\n"
  "s_mul_i32 s46, s44, 221184\n"
  "buffer_load_dwordx4 v93, s[8:11], s46 offen lds\n"
  "s_cmp_ge_u32 s68, 4\n"
  "s_cbranch_scc1 LA1S_%=\n"
  "s_sub_u32 m0, m0, 2048\n"
  "s_nop 0\n"
  "buffer_load_dwordx4 v134, s[8:11], s46 offen lds\n"
  "s_add_u32 m0, m0, 8192\n"
  "s_mul_i32 s46, s44, 589824\n"
  "buffer_load_dwordx4 v135, s[12:15], s46 offen lds\n"
  "s_sub_u32 m0, m0, 6144\n"
  "LA1S_%=:\n"
  "s_add_u32 m0, m0, 8192\n"
  "s_mul_i32 s46, s44, 589824\n"
  "buffer_load_dwordx4 v94, s[12:15], s46 offen lds\n"
  "s_add_u32 s44, s44, 1\n"
  "s_cmp_eq_u32 s44, 9\n"
  "s_cselect_b32 s44, 0, s44\n"
  "s_sub_u32 s47, s47, 1\n"
  "s_cmp_lg_u32 s47, 0\n"
  "s_cbranch_scc1 LA1L_%=\n"
  "LA1_%=:\n"
    : "+v"(l), "+v"(g), "+v"(i), "+v"(q), "+v"(p)
    : DMAOPS, "{s[68:83]}"(sa), "{s[84:91]}"(sb), "{s[28:31]}"(s28), "{s34}"(ROT(7))
    : DMACLB);
    __builtin_assume(l < 64); __builtin_assume(g < 4); __builtin_assume(i < 16); __builtin_assume(q < 4); __builtin_assume(p < 4);
    const bool lo = l < 32;
    const u32 jq = sel(l & 8, (18 * head + 17) >> 6, (18 * head) >> 6) + sel(lo, 9 + 9 * g, 0);
    const u32 pq = (sel(lo, w, idx & 7) * 32 + (jq & 3) * 8 + (jq >> 2)) * 128 + 32 + (l & 7);
    const u32 ph = (l & 6) * 512 + (l & 1) + (((R >> 2) * 32 + c) * 128 + 2 * (R & 3));
    const u32 ko = mn(l & 31, 17), pw = (ko >> 3) * 1152 + (ko & 7) + ((162 + (nt >> 2)) * 128 + 64);
    const u32 kd = sel(l < 48, 16 * c + i, 128 + 2 * c + (l & 1)), ed = sel(l < 48, g, mn((l >> 1) & 3, 2));
    const u32 pa = sel(att, pw, sel(l < 56, (kd >> 3) * 1152 + ed * 128 + (kd & 7) + (cg * 256 + 64), ph + 64));
    const u32 pb = sel(lo, (l >> 2) * 128 + p * 8192 + ((rb >> 3) * 1024 + 96 + ((rb & 7) >> 1)), sel(l < 55, pw, sel(l == 55, (b ^ 1) * 128 + 96 + w, ph + 96)));
    const u32 e = (l & 31) - 2 * odd;
    const u32 vvl = 16384 + sel(e < 16, (e >> 2) * 256 + (l >> 5) * 128 + (e & 3) * 16, 1024 + (l >> 5) * 32 + (l & 1) * 16);
    const u32 vtd = (p << 8) | ((l >> 5) << 6) | (q << 4) | ((g & 1) << 3);
    v110[0] = pq | (pa << 16); v110[1] = sel(att, vvl, pb) | (vtd << 16);
  }
  asm volatile(
  "s_nop 4\n"
  "s_mov_b32 s46, 262144\n"
  "s_mov_b32 s80, 39002112\n"
  "buffer_load_dword v137, v136, s[20:23], s80 offen sc1\n"
  "s_waitcnt vmcnt(0)\n"
  "v_readfirstlane_b32 s47, v131\n"
  "s_nop 3\n"
  "s_mul_i32 s47, s47, 0x9e3779b1\n"
  "s_add_u32 s47, s47, 0x6d2b79f5\n"
  "v_cmp_ne_u32 vcc, s47, v137\n"
  "s_cbranch_vccnz LPX_%=\n"
  "LPXB_%=:\n"
  "s_cmp_eq_u32 s42, 1\n"
  "s_cbranch_scc1 LK1V_%=\n"
  "s_add_u32 s44, s29, 0\n"
  "buffer_load_dwordx4 v[112:115], v101, s[20:23], s44 offen offset:0 sc1\n"
  "buffer_load_dwordx4 v[116:119], v101, s[20:23], s44 offen offset:1024 sc1\n"
  "buffer_load_dwordx4 v[120:123], v101, s[20:23], s44 offen offset:2048 sc1\n"
  "buffer_load_dwordx4 v[124:127], v101, s[20:23], s44 offen offset:3072 sc1\n"
  "s_add_u32 s44, s29, 4096\n"
  "buffer_load_dwordx4 v[128:131], v101, s[20:23], s44 offen offset:0 sc1\n"
  "buffer_load_dwordx4 v[132:135], v101, s[20:23], s44 offen offset:1024 sc1\n"
  "s_barrier\n"
  "ds_read_b64_tr_b16 v[64:65], v88 offset:0\n"
  "ds_read_b64_tr_b16 v[66:67], v88 offset:512\n"
  "ds_read_b64_tr_b16 v[68:69], v89 offset:0\n"
  "ds_read_b64_tr_b16 v[70:71], v89 offset:512\n"
  "ds_read_b64_tr_b16 v[72:73], v90 offset:0\n"
  "ds_read_b64_tr_b16 v[74:75], v90 offset:512\n"
  "ds_read_b64_tr_b16 v[76:77], v91 offset:0\n"
  "ds_read_b64_tr_b16 v[78:79], v91 offset:512\n"
  "s_waitcnt vmcnt(5) lgkmcnt(6)\n"
  "v_mfma_f32_16x16x32_bf16 v[0:3], v[64:67], v[112:115], 0\n"
  "ds_read_b64_tr_b16 v[80:81], v88 offset:8192\n"
  "ds_read_b64_tr_b16 v[82:83], v88 offset:8704\n"
  "buffer_load_dwordx4 v[136:139], v101, s[20:23], s44 offen offset:2048 sc1\n"
  "s_setprio 3\n"
  "s_waitcnt vmcnt(5)\n"
  "v_mfma_f32_16x16x32_bf16 v[4:7], v[64:67], v[116:119], 0\n"
  "s_waitcnt lgkmcnt(6)\n"
  "v_mfma_f32_16x16x32_bf16 v[8:11], v[68:71], v[112:115], 0\n"
  "ds_read_b64_tr_b16 v[64:65], v89 offset:8192\n"
  "ds_read_b64_tr_b16 v[66:67], v89 offset:8704\n"
  "v_mfma_f32_16x16x32_bf16 v[12:15], v[68:71], v[116:119], 0\n"
  "s_waitcnt lgkmcnt(6)\n"
  "v_mfma_f32_16x16x32_bf16 v[16:19], v[72:75], v[112:115], 0\n"
  "ds_read_b64_tr_b16 v[68:69], v90 offset:8192\n"
  "ds_read_b64_tr_b16 v[70:71], v90 offset:8704\n"
  "buffer_load_dwordx4 v[140:143], v101, s[20:23], s44 offen offset:3072 sc1\n"
  "v_mfma_f32_16x16x32_bf16 v[20:23], v[72:75], v[116:119], 0\n"
  "s_waitcnt lgkmcnt(6)\n"
  "v_mfma_f32_16x16x32_bf16 v[24:27], v[76:79], v[112:115], 0\n"
  "ds_read_b64_tr_b16 v[72:73], v91 offset:8192\n"
  "ds_read_b64_tr_b16 v[74:75], v91 offset:8704\n"
  "v_mfma_f32_16x16x32_bf16 v[28:31], v[76:79], v[116:119], 0\n"
  "s_waitcnt lgkmcnt(6)\n"
  "v_mfma_f32_16x16x32_bf16 v[32:35], v[80:83], v[112:115], 0\n"
  "ds_read_b64_tr_b16 v[76:77], v88 offset:4096\n"
  "ds_read_b64_tr_b16 v[78:79], v88 offset:4608\n"
  "s_add_u32 s44, s29, 8192\n"
  "buffer_load_dwordx4 v[144:147], v101, s[20:23], s44 offen offset:0 sc1\n"
  "v_mfma_f32_16x16x32_bf16 v[36:39], v[80:83], v[116:119], 0\n"
  "s_waitcnt lgkmcnt(6)\n"
  "v_mfma_f32_16x16x32_bf16 v[40:43], v[64:67], v[112:115], 0\n"
  "ds_read_b64_tr_b16 v[80:81], v89 offset:4096\n"
  "ds_read_b64_tr_b16 v[82:83], v89 offset:4608\n"
  "s_setprio 2\n"
  "v_mfma_f32_16x16x32_bf16 v[44:47], v[64:67], v[116:119], 0\n"
  "s_waitcnt lgkmcnt(6)\n"
  "v_mfma_f32_16x16x32_bf16 v[48:51], v[68:71], v[112:115], 0\n"
  "ds_read_b64_tr_b16 v[64:65], v90 offset:4096\n"
  "ds_read_b64_tr_b16 v[66:67], v90 offset:4608\n"
  "buffer_load_dwordx4 v[148:151], v101, s[20:23], s44 offen offset:1024 sc1\n"
  "v_mfma_f32_16x16x32_bf16 v[52:55], v[68:71], v[116:119], 0\n"
  "s_waitcnt lgkmcnt(6)\n"
  "v_mfma_f32_16x16x32_bf16 v[56:59], v[72:75], v[112:115], 0\n"
  "ds_read_b64_tr_b16 v[68:69], v91 offset:4096\n"
  "ds_read_b64_tr_b16 v[70:71], v91 offset:4608\n"
  "v_mfma_f32_16x16x32_bf16 v[60:63], v[72:75], v[116:119], 0\n"
  "s_waitcnt vmcnt(7) lgkmcnt(6)\n"
  "v_mfma_f32_16x16x32_bf16 v[0:3], v[76:79], v[120:123], v[0:3]\n"
  "ds_read_b64_tr_b16 v[72:73], v88 offset:12288\n"
  "ds_read_b64_tr_b16 v[74:75], v88 offset:12800\n"
  "buffer_load_dwordx4 v[152:155], v101, s[20:23], s44 offen offset:2048 sc1\n"
  "s_waitcnt vmcnt(7)\n"
  "v_mfma_f32_16x16x32_bf16 v[4:7], v[76:79], v[124:127], v[4:7]\n"
  "s_waitcnt lgkmcnt(6)\n"
  "v_mfma_f32_16x16x32_bf16 v[8:11], v[80:83], v[120:123], v[8:11]\n"
  "ds_read_b64_tr_b16 v[76:77], v89 offset:12288\n"
  "ds_read_b64_tr_b16 v[78:79], v89 offset:12800\n"
  "v_mfma_f32_16x16x32_bf16 v[12:15], v[80:83], v[124:127], v[12:15]\n"
  "s_waitcnt lgkmcnt(6)\n"
  "v_mfma_f32_16x16x32_bf16 v[16:19], v[64:67], v[120:123], v[16:19]\n"
  "ds_read_b64_tr_b16 v[80:81], v90 offset:12288\n"
  "ds_read_b64_tr_b16 v[82:83], v90 offset:12800\n"
  "buffer_load_dwordx4 v[156:159], v101, s[20:23], s44 offen offset:3072 sc1\n"
  "s_setprio 1\n"
  "v_mfma_f32_16x16x32_bf16 v[20:23], v[64:67], v[124:127], v[20:23]\n"
  "s_add_u32 m0, s28, 147456\n"
  "s_mov_b32 s43, 0\n"
  "buffer_load_dwordx4 v94, s[16:19], s43 offen lds\n"
  "s_waitcnt lgkmcnt(6)\n"
  "v_mfma_f32_16x16x32_bf16 v[24:27], v[68:71], v[120:123], v[24:27]\n"
  "ds_read_b64_tr_b16 v[64:65], v91 offset:12288\n"
  "ds_read_b64_tr_b16 v[66:67], v91 offset:12800\n"
  "v_mfma_f32_16x16x32_bf16 v[28:31], v[68:71], v[124:127], v[28:31]\n"
  "s_waitcnt lgkmcnt(6)\n"
  "v_mfma_f32_16x16x32_bf16 v[32:35], v[72:75], v[120:123], v[32:35]\n"
  "ds_read_b64_tr_b16 v[68:69], v88 offset:16384\n"
  "ds_read_b64_tr_b16 v[70:71], v88 offset:16896\n"
  "s_add_u32 s44, s29, 12288\n"
  "buffer_load_dwordx4 v[160:163], v101, s[20:23], s44 offen offset:0 sc1\n"
  "v_mfma_f32_16x16x32_bf16 v[36:39], v[72:75], v[124:127], v[36:39]\n"
  "s_waitcnt lgkmcnt(6)\n"
  "v_mfma_f32_16x16x32_bf16 v[40:43], v[76:79], v[120:123], v[40:43]\n"
  "ds_read_b64_tr_b16 v[72:73], v89 offset:16384\n"
  "ds_read_b64_tr_b16 v[74:75], v89 offset:16896\n"
  "v_mfma_f32_16x16x32_bf16 v[44:47], v[76:79], v[124:127], v[44:47]\n"
  "s_waitcnt lgkmcnt(6)\n"
  "v_mfma_f32_16x16x32_bf16 v[48:51], v[80:83], v[120:123], v[48:51]\n"
  "ds_read_b64_tr_b16 v[76:77], v90 offset:16384\n"
  "ds_read_b64_tr_b16 v[78:79], v90 offset:16896\n"
  "buffer_load_dwordx4 v[164:167], v101, s[20:23], s44 offen offset:1024 sc1\n"
  "v_mfma_f32_16x16x32_bf16 v[52:55], v[80:83], v[124:127], v[52:55]\n"
  "s_waitcnt lgkmcnt(6)\n"
  "v_mfma_f32_16x16x32_bf16 v[56:59], v[64:67], v[120:123], v[56:59]\n"
  "ds_read_b64_tr_b16 v[80:81], v91 offset:16384\n"
  "ds_read_b64_tr_b16 v[82:83], v91 offset:16896\n"
  "s_setprio 0\n"
  "v_mfma_f32_16x16x32_bf16 v[60:63], v[64:67], v[124:127], v[60:63]\n"
  "s_waitcnt vmcnt(10) lgkmcnt(6)\n"
  "v_mfma_f32_16x16x32_bf16 v[0:3], v[68:71], v[128:131], v[0:3]\n"
  "ds_read_b64_tr_b16 v[64:65], v88 offset:24576\n"
  "ds_read_b64_tr_b16 v[66:67], v88 offset:25088\n"
  "buffer_load_dwordx4 v[168:171], v101, s[20:23], s44 offen offset:2048 sc1\n"
  "s_waitcnt vmcnt(10)\n"
  "v_mfma_f32_16x16x32_bf16 v[4:7], v[68:71], v[132:135], v[4:7]\n"
  "s_waitcnt lgkmcnt(6)\n"
  "v_mfma_f32_16x16x32_bf16 v[8:11], v[72:75], v[128:131], v[8:11]\n"
  "ds_read_b64_tr_b16 v[68:69], v89 offset:24576\n"
  "ds_read_b64_tr_b16 v[70:71], v89 offset:25088\n"
  "v_mfma_f32_16x16x32_bf16 v[12:15], v[72:75], v[132:135], v[12:15]\n"
  "s_waitcnt lgkmcnt(6)\n"
  "v_mfma_f32_16x16x32_bf16 v[16:19], v[76:79], v[128:131], v[16:19]\n"
  "ds_read_b64_tr_b16 v[72:73], v90 offset:24576\n"
  "ds_read_b64_tr_b16 v[74:75], v90 offset:25088\n"
  "buffer_load_dwordx4 v[172:175], v101, s[20:23], s44 offen offset:3072 sc1\n"
  "v_mfma_f32_16x16x32_bf16 v[20:23], v[76:79], v[132:135], v[20:23]\n"
  "s_add_u32 m0, s28, 155648\n"
  "s_mov_b32 s43, 589824\n"
  "buffer_load_dwordx4 v94, s[16:19], s43 offen lds\n"
  "s_waitcnt lgkmcnt(6)\n"
  "v_mfma_f32_16x16x32_bf16 v[24:27], v[80:83], v[128:131], v[24:27]\n"
  "ds_read_b64_tr_b16 v[76:77], v91 offset:24576\n"
  "ds_read_b64_tr_b16 v[78:79], v91 offset:25088\n"
  "v_mfma_f32_16x16x32_bf16 v[28:31], v[80:83], v[132:135], v[28:31]\n"
  "s_waitcnt lgkmcnt(6)\n"
  "v_mfma_f32_16x16x32_bf16 v[32:35], v[64:67], v[128:131], v[32:35]\n"
  "s_barrier\n"
  "ds_read_b64_tr_b16 v[80:81], v88 offset:20480\n"
  "ds_read_b64_tr_b16 v[82:83], v88 offset:20992\n"
  "s_add_u32 s44, s29, 16384\n"
  "buffer_load_dwordx4 v[176:179], v101, s[20:23], s44 offen offset:0 sc1\n"
  "s_setprio 3\n"
  "v_mfma_f32_16x16x32_bf16 v[36:39], v[64:67], v[132:135], v[36:39]\n"
  "s_waitcnt lgkmcnt(6)\n"
  "v_mfma_f32_16x16x32_bf16 v[40:43], v[68:71], v[128:131], v[40:43]\n"
  "ds_read_b64_tr_b16 v[64:65], v89 offset:20480\n"
  "ds_read_b64_tr_b16 v[66:67], v89 offset:20992\n"
  "v_mfma_f32_16x16x32_bf16 v[44:47], v[68:71], v[132:135], v[44:47]\n"
  "s_waitcnt lgkmcnt(6)\n"
  "v_mfma_f32_16x16x32_bf16 v[48:51], v[72:75], v[128:131], v[48:51]\n"
  "ds_read_b64_tr_b16 v[68:69], v90 offset:20480\n"
  "ds_read_b64_tr_b16 v[70:71], v90 offset:20992\n"
  "v_mfma_f32_16x16x32_bf16 v[52:55], v[72:75], v[132:135], v[52:55]\n"
  "s_waitcnt lgkmcnt(6)\n"
  "v_mfma_f32_16x16x32_bf16 v[56:59], v[76:79], v[128:131], v[56:59]\n"
  "ds_read_b64_tr_b16 v[72:73], v91 offset:20480\n"
  "ds_read_b64_tr_b16 v[74:75], v91 offset:20992\n"
  "buffer_load_dwordx4 v[180:183], v101, s[20:23], s44 offen offset:1024 sc1\n"
  "v_mfma_f32_16x16x32_bf16 v[60:63], v[76:79], v[132:135], v[60:63]\n"
  "s_waitcnt vmcnt(13) lgkmcnt(6)\n"
  "v_mfma_f32_16x16x32_bf16 v[0:3], v[80:83], v[136:139], v[0:3]\n"
  "ds_read_b64_tr_b16 v[76:77], v88 offset:28672\n"
  "ds_read_b64_tr_b16 v[78:79], v88 offset:29184\n"
  "s_setprio 2\n"
  "s_waitcnt vmcnt(12)\n"
  "v_mfma_f32_16x16x32_bf16 v[4:7], v[80:83], v[140:143], v[4:7]\n"
  "s_waitcnt lgkmcnt(6)\n"
  "v_mfma_f32_16x16x32_bf16 v[8:11], v[64:67], v[136:139], v[8:11]\n"
  "ds_read_b64_tr_b16 v[80:81], v89 offset:28672\n"
  "ds_read_b64_tr_b16 v[82:83], v89 offset:29184\n"
  "v_mfma_f32_16x16x32_bf16 v[12:15], v[64:67], v[140:143], v[12:15]\n"
  "s_waitcnt lgkmcnt(6)\n"
  "v_mfma_f32_16x16x32_bf16 v[16:19], v[68:71], v[136:139], v[16:19]\n"
  "ds_read_b64_tr_b16 v[64:65], v90 offset:28672\n"
  "ds_read_b64_tr_b16 v[66:67], v90 offset:29184\n"
  "buffer_load_dwordx4 v[184:187], v101, s[20:23], s44 offen offset:2048 sc1\n"
  "v_mfma_f32_16x16x32_bf16 v[20:23], v[68:71], v[140:143], v[20:23]\n"
  "s_add_u32 m0, s28, 0\n"
  "s_mov_b32 s43, 1179648\n"
  "buffer_load_dwordx4 v94, s[16:19], s43 offen lds\n"
  "s_waitcnt lgkmcnt(6)\n"
  "v_mfma_f32_16x16x32_bf16 v[24:27], v[72:75], v[136:139], v[24:27]\n"
  "ds_read_b64_tr_b16 v[68:69], v91 offset:28672\n"
  "ds_read_b64_tr_b16 v[70:71], v91 offset:29184\n"
  "v_mfma_f32_16x16x32_bf16 v[28:31], v[72:75], v[140:143], v[28:31]\n"
  "s_waitcnt lgkmcnt(6)\n"
  "v_mfma_f32_16x16x32_bf16 v[32:35], v[76:79], v[136:139], v[32:35]\n"
  "ds_read_b64_tr_b16 v[72:73], v88 offset:32768\n"
  "ds_read_b64_tr_b16 v[74:75], v88 offset:33280\n"
  "s_setprio 1\n"
  "v_mfma_f32_16x16x32_bf16 v[36:39], v[76:79], v[140:143], v[36:39]\n"
  "s_waitcnt lgkmcnt(6)\n"
  "v_mfma_f32_16x16x32_bf16 v[40:43], v[80:83], v[136:139], v[40:43]\n"
  "ds_read_b64_tr_b16 v[76:77], v89 offset:32768\n"
  "ds_read_b64_tr_b16 v[78:79], v89 offset:33280\n"
  "buffer_load_dwordx4 v[188:191], v101, s[20:23], s44 offen offset:3072 sc1\n"
  "v_mfma_f32_16x16x32_bf16 v[44:47], v[80:83], v[140:143], v[44:47]\n"
  "s_waitcnt lgkmcnt(6)\n"
  "v_mfma_f32_16x16x32_bf16 v[48:51], v[64:67], v[136:139], v[48:51]\n"
  "ds_read_b64_tr_b16 v[80:81], v90 offset:32768\n"
  "ds_read_b64_tr_b16 v[82:83], v90 offset:33280\n"
  "v_mfma_f32_16x16x32_bf16 v[52:55], v[64:67], v[140:143], v[52:55]\n"
  "s_waitcnt lgkmcnt(6)\n"
  "v_mfma_f32_16x16x32_bf16 v[56:59], v[68:71], v[136:139], v[56:59]\n"
  "ds_read_b64_tr_b16 v[64:65], v91 offset:32768\n"
  "ds_read_b64_tr_b16 v[66:67], v91 offset:33280\n"
  "v_mfma_f32_16x16x32_bf16 v[60:63], v[68:71], v[140:143], v[60:63]\n"
  "s_waitcnt vmcnt(14) lgkmcnt(6)\n"
  "v_mfma_f32_16x16x32_bf16 v[0:3], v[72:75], v[144:147], v[0:3]\n"
  "ds_read_b64_tr_b16 v[68:69], v88 offset:40960\n"
  "ds_read_b64_tr_b16 v[70:71], v88 offset:41472\n"
  "s_add_u32 s44, s29, 20480\n"
  "buffer_load_dwordx4 v[192:195], v101, s[20:23], s44 offen offset:0 sc1\n"
  "s_setprio 0\n"
  "s_waitcnt vmcnt(14)\n"
  "v_mfma_f32_16x16x32_bf16 v[4:7], v[72:75], v[148:151], v[4:7]\n"
  "s_waitcnt lgkmcnt(6)\n"
  "v_mfma_f32_16x16x32_bf16 v[8:11], v[76:79], v[144:147], v[8:11]\n"
  "ds_read_b64_tr_b16 v[72:73], v89 offset:40960\n"
  "ds_read_b64_tr_b16 v[74:75], v89 offset:41472\n"
  "v_mfma_f32_16x16x32_bf16 v[12:15], v[76:79], v[148:151], v[12:15]\n"
  "s_waitcnt lgkmcnt(6)\n"
  "v_mfma_f32_16x16x32_bf16 v[16:19], v[80:83], v[144:147], v[16:19]\n"
  "ds_read_b64_tr_b16 v[76:77], v90 offset:40960\n"
  "ds_read_b64_tr_b16 v[78:79], v90 offset:41472\n"
  "v_mfma_f32_16x16x32_bf16 v[20:23], v[80:83], v[148:151], v[20:23]\n"
  "s_add_u32 m0, s28, 8192\n"
  "s_mov_b32 s43, 1769472\n"
  "buffer_load_dwordx4 v94, s[16:19], s43 offen lds\n"
  "s_waitcnt lgkmcnt(6)\n"
  "v_mfma_f32_16x16x32_bf16 v[24:27], v[64:67], v[144:147], v[24:27]\n"
  "ds_read_b64_tr_b16 v[80:81], v91 offset:40960\n"
  "ds_read_b64_tr_b16 v[82:83], v91 offset:41472\n"
  "buffer_load_dwordx4 v[196:199], v101, s[20:23], s44 offen offset:1024 sc1\n"
  "v_mfma_f32_16x16x32_bf16 v[28:31], v[64:67], v[148:151], v[28:31]\n"
  "s_waitcnt lgkmcnt(6)\n"
  "v_mfma_f32_16x16x32_bf16 v[32:35], v[68:71], v[144:147], v[32:35]\n"
  "s_barrier\n"
  "ds_read_b64_tr_b16 v[64:65], v88 offset:36864\n"
  "ds_read_b64_tr_b16 v[66:67], v88 offset:37376\n"
  "s_setprio 3\n"
  "v_mfma_f32_16x16x32_bf16 v[36:39], v[68:71], v[148:151], v[36:39]\n"
  "s_waitcnt lgkmcnt(6)\n"
  "v_mfma_f32_16x16x32_bf16 v[40:43], v[72:75], v[144:147], v[40:43]\n"
  "ds_read_b64_tr_b16 v[68:69], v89 offset:36864\n"
  "ds_read_b64_tr_b16 v[70:71], v89 offset:37376\n"
  "v_mfma_f32_16x16x32_bf16 v[44:47], v[72:75], v[148:151], v[44:47]\n"
  "s_waitcnt lgkmcnt(6)\n"
  "v_mfma_f32_16x16x32_bf16 v[48:51], v[76:79], v[144:147], v[48:51]\n"
  "ds_read_b64_tr_b16 v[72:73], v90 offset:36864\n"
  "ds_read_b64_tr_b16 v[74:75], v90 offset:37376\n"
  "buffer_load_dwordx4 v[200:203], v101, s[20:23], s44 offen offset:2048 sc1\n"
  "v_mfma_f32_16x16x32_bf16 v[52:55], v[76:79], v[148:151], v[52:55]\n"
  "s_waitcnt lgkmcnt(6)\n"
  "v_mfma_f32_16x16x32_bf16 v[56:59], v[80:83], v[144:147], v[56:59]\n"
  "ds_read_b64_tr_b16 v[76:77], v91 offset:36864\n"
  "ds_read_b64_tr_b16 v[78:79], v91 offset:37376\n"
  "v_mfma_f32_16x16x32_bf16 v[60:63], v[80:83], v[148:151], v[60:63]\n"
  "s_waitcnt vmcnt(16) lgkmcnt(6)\n"
  "v_mfma_f32_16x16x32_bf16 v[0:3], v[64:67], v[152:155], v[0:3]\n"
  "ds_read_b64_tr_b16 v[80:81], v88 offset:45056\n"
  "ds_read_b64_tr_b16 v[82:83], v88 offset:45568\n"
  "s_setprio 2\n"
  "s_waitcnt vmcnt(15)\n"
  "v_mfma_f32_16x16x32_bf16 v[4:7], v[64:67], v[156:159], v[4:7]\n"
  "s_waitcnt lgkmcnt(6)\n"
  "v_mfma_f32_16x16x32_bf16 v[8:11], v[68:71], v[152:155], v[8:11]\n"
  "ds_read_b64_tr_b16 v[64:65], v89 offset:45056\n"
  "ds_read_b64_tr_b16 v[66:67], v89 offset:45568\n"
  "buffer_load_dwordx4 v[204:207], v101, s[20:23], s44 offen offset:3072 sc1\n"
  "v_mfma_f32_16x16x32_bf16 v[12:15], v[68:71], v[156:159], v[12:15]\n"
  "s_waitcnt lgkmcnt(6)\n"
  "v_mfma_f32_16x16x32_bf16 v[16:19], v[72:75], v[152:155], v[16:19]\n"
  "ds_read_b64_tr_b16 v[68:69], v90 offset:45056\n"
  "ds_read_b64_tr_b16 v[70:71], v90 offset:45568\n"
  "v_mfma_f32_16x16x32_bf16 v[20:23], v[72:75], v[156:159], v[20:23]\n"
  "s_add_u32 m0, s28, 16384\n"
  "s_mov_b32 s43, 2359296\n"
  "buffer_load_dwordx4 v94, s[16:19], s43 offen lds\n"
  "s_waitcnt lgkmcnt(6)\n"
  "v_mfma_f32_16x16x32_bf16 v[24:27], v[76:79], v[152:155], v[24:27]\n"
  "ds_read_b64_tr_b16 v[72:73], v91 offset:45056\n"
  "ds_read_b64_tr_b16 v[74:75], v91 offset:45568\n"
  "v_mfma_f32_16x16x32_bf16 v[28:31], v[76:79], v[156:159], v[28:31]\n"
  "s_waitcnt lgkmcnt(6)\n"
  "v_mfma_f32_16x16x32_bf16 v[32:35], v[80:83], v[152:155], v[32:35]\n"
  "ds_read_b64_tr_b16 v[76:77], v88 offset:49152\n"
  "ds_read_b64_tr_b16 v[78:79], v88 offset:49664\n"
  "s_add_u32 s44, s29, 24576\n"
  "buffer_load_dwordx4 v[208:211], v101, s[20:23], s44 offen offset:0 sc1\n"
  "s_setprio 1\n"
  "v_mfma_f32_16x16x32_bf16 v[36:39], v[80:83], v[156:159], v[36:39]\n"
  "s_waitcnt lgkmcnt(6)\n"
  "v_mfma_f32_16x16x32_bf16 v[40:43], v[64:67], v[152:155], v[40:43]\n"
  "ds_read_b64_tr_b16 v[80:81], v89 offset:49152\n"
  "ds_read_b64_tr_b16 v[82:83], v89 offset:49664\n"
  "v_mfma_f32_16x16x32_bf16 v[44:47], v[64:67], v[156:159], v[44:47]\n"
  "s_waitcnt lgkmcnt(6)\n"
  "v_mfma_f32_16x16x32_bf16 v[48:51], v[68:71], v[152:155], v[48:51]\n"
  "ds_read_b64_tr_b16 v[64:65], v90 offset:49152\n"
  "ds_read_b64_tr_b16 v[66:67], v90 offset:49664\n"
  "v_mfma_f32_16x16x32_bf16 v[52:55], v[68:71], v[156:159], v[52:55]\n"
  "s_waitcnt lgkmcnt(6)\n"
  "v_mfma_f32_16x16x32_bf16 v[56:59], v[72:75], v[152:155], v[56:59]\n"
  "ds_read_b64_tr_b16 v[68:69], v91 offset:49152\n"
  "ds_read_b64_tr_b16 v[70:71], v91 offset:49664\n"
  "buffer_load_dwordx4 v[212:215], v101, s[20:23], s44 offen offset:1024 sc1\n"
  "v_mfma_f32_16x16x32_bf16 v[60:63], v[72:75], v[156:159], v[60:63]\n"
  "s_waitcnt vmcnt(17) lgkmcnt(6)\n"
  "v_mfma_f32_16x16x32_bf16 v[0:3], v[76:79], v[160:163], v[0:3]\n"
  "ds_read_b64_tr_b16 v[72:73], v88 offset:57344\n"
  "ds_read_b64_tr_b16 v[74:75], v88 offset:57856\n"
  "s_setprio 0\n"
  "s_waitcnt vmcnt(16)\n"
  "v_mfma_f32_16x16x32_bf16 v[4:7], v[76:79], v[164:167], v[4:7]\n"
  "s_waitcnt lgkmcnt(6)\n"
  "v_mfma_f32_16x16x32_bf16 v[8:11], v[80:83], v[160:163], v[8:11]\n"
  "ds_read_b64_tr_b16 v[76:77], v89 offset:57344\n"
  "ds_read_b64_tr_b16 v[78:79], v89 offset:57856\n"
  "v_mfma_f32_16x16x32_bf16 v[12:15], v[80:83], v[164:167], v[12:15]\n"
  "s_waitcnt lgkmcnt(6)\n"
  "v_mfma_f32_16x16x32_bf16 v[16:19], v[64:67], v[160:163], v[16:19]\n"
  "ds_read_b64_tr_b16 v[80:81], v90 offset:57344\n"
  "ds_read_b64_tr_b16 v[82:83], v90 offset:57856\n"
  "buffer_load_dwordx4 v[216:219], v101, s[20:23], s44 offen offset:2048 sc1\n"
  "v_mfma_f32_16x16x32_bf16 v[20:23], v[64:67], v[164:167], v[20:23]\n"
  "s_add_u32 m0, s28, 24576\n"
  "s_mov_b32 s43, 2949120\n"
  "buffer_load_dwordx4 v94, s[16:19], s43 offen lds\n"
  "s_waitcnt lgkmcnt(6)\n"
  "v_mfma_f32_16x16x32_bf16 v[24:27], v[68:71], v[160:163], v[24:27]\n"
  "ds_read_b64_tr_b16 v[64:65], v91 offset:57344\n"
  "ds_read_b64_tr_b16 v[66:67], v91 offset:57856\n"
  "v_mfma_f32_16x16x32_bf16 v[28:31], v[68:71], v[164:167], v[28:31]\n"
  "s_waitcnt lgkmcnt(6)\n"
  "v_mfma_f32_16x16x32_bf16 v[32:35], v[72:75], v[160:163], v[32:35]\n"
  "s_barrier\n"
  "ds_read_b64_tr_b16 v[68:69], v88 offset:53248\n"
  "ds_read_b64_tr_b16 v[70:71], v88 offset:53760\n"
  "s_setprio 3\n"
  "v_mfma_f32_16x16x32_bf16 v[36:39], v[72:75], v[164:167], v[36:39]\n"
  "s_waitcnt lgkmcnt(6)\n"
  "v_mfma_f32_16x16x32_bf16 v[40:43], v[76:79], v[160:163], v[40:43]\n"
  "ds_read_b64_tr_b16 v[72:73], v89 offset:53248\n"
  "ds_read_b64_tr_b16 v[74:75], v89 offset:53760\n"
  "buffer_load_dwordx4 v[220:223], v101, s[20:23], s44 offen offset:3072 sc1\n"
  "v_mfma_f32_16x16x32_bf16 v[44:47], v[76:79], v[164:167], v[44:47]\n"
  "s_waitcnt lgkmcnt(6)\n"
  "v_mfma_f32_16x16x32_bf16 v[48:51], v[80:83], v[160:163], v[48:51]\n"
  "ds_read_b64_tr_b16 v[76:77], v90 offset:53248\n"
  "ds_read_b64_tr_b16 v[78:79], v90 offset:53760\n"
  "v_mfma_f32_16x16x32_bf16 v[52:55], v[80:83], v[164:167], v[52:55]\n"
  "s_waitcnt lgkmcnt(6)\n"
  "v_mfma_f32_16x16x32_bf16 v[56:59], v[64:67], v[160:163], v[56:59]\n"
  "ds_read_b64_tr_b16 v[80:81], v91 offset:53248\n"
  "ds_read_b64_tr_b16 v[82:83], v91 offset:53760\n"
  "v_mfma_f32_16x16x32_bf16 v[60:63], v[64:67], v[164:167], v[60:63]\n"
  "s_waitcnt vmcnt(18) lgkmcnt(6)\n"
  "v_mfma_f32_16x16x32_bf16 v[0:3], v[68:71], v[168:171], v[0:3]\n"
  "ds_read_b64_tr_b16 v[64:65], v88 offset:61440\n"
  "ds_read_b64_tr_b16 v[66:67], v88 offset:61952\n"
  "s_add_u32 s44, s29, 28672\n"
  "buffer_load_dwordx4 v[224:227], v101, s[20:23], s44 offen offset:0 sc1\n"
  "s_setprio 2\n"
  "s_waitcnt vmcnt(18)\n"
  "v_mfma_f32_16x16x32_bf16 v[4:7], v[68:71], v[172:175], v[4:7]\n"
  "s_waitcnt lgkmcnt(6)\n"
  "v_mfma_f32_16x16x32_bf16 v[8:11], v[72:75], v[168:171], v[8:11]\n"
  "ds_read_b64_tr_b16 v[68:69], v89 offset:61440\n"
  "ds_read_b64_tr_b16 v[70:71], v89 offset:61952\n"
  "v_mfma_f32_16x16x32_bf16 v[12:15], v[72:75], v[172:175], v[12:15]\n"
  "s_waitcnt lgkmcnt(6)\n"
  "v_mfma_f32_16x16x32_bf16 v[16:19], v[76:79], v[168:171], v[16:19]\n"
  "ds_read_b64_tr_b16 v[72:73], v90 offset:61440\n"
  "ds_read_b64_tr_b16 v[74:75], v90 offset:61952\n"
  "v_mfma_f32_16x16x32_bf16 v[20:23], v[76:79], v[172:175], v[20:23]\n"
  "s_add_u32 m0, s28, 32768\n"
  "s_mov_b32 s43, 3538944\n"
  "buffer_load_dwordx4 v94, s[16:19], s43 offen lds\n"
  "s_waitcnt lgkmcnt(6)\n"
  "v_mfma_f32_16x16x32_bf16 v[24:27], v[80:83], v[168:171], v[24:27]\n"
  "ds_read_b64_tr_b16 v[76:77], v91 offset:61440\n"
  "ds_read_b64_tr_b16 v[78:79], v91 offset:61952\n"
  "buffer_load_dwordx4 v[228:231], v101, s[20:23], s44 offen offset:1024 sc1\n"
  "v_mfma_f32_16x16x32_bf16 v[28:31], v[80:83], v[172:175], v[28:31]\n"
  "s_waitcnt lgkmcnt(6)\n"
  "v_mfma_f32_16x16x32_bf16 v[32:35], v[64:67], v[168:171], v[32:35]\n"
  "v_add_u32 v88, 0x10000, v88\n"
  "ds_read_b64_tr_b16 v[80:81], v88 offset:0\n"
  "ds_read_b64_tr_b16 v[82:83], v88 offset:512\n"
  "s_setprio 1\n"
  "v_mfma_f32_16x16x32_bf16 v[36:39], v[64:67], v[172:175], v[36:39]\n"
  "s_waitcnt lgkmcnt(6)\n"
  "v_mfma_f32_16x16x32_bf16 v[40:43], v[68:71], v[168:171], v[40:43]\n"
  "v_add_u32 v89, 0x10000, v89\n"
  "ds_read_b64_tr_b16 v[64:65], v89 offset:0\n"
  "ds_read_b64_tr_b16 v[66:67], v89 offset:512\n"
  "v_mfma_f32_16x16x32_bf16 v[44:47], v[68:71], v[172:175], v[44:47]\n"
  "s_waitcnt lgkmcnt(6)\n"
  "v_mfma_f32_16x16x32_bf16 v[48:51], v[72:75], v[168:171], v[48:51]\n"
  "v_add_u32 v90, 0x10000, v90\n"
  "ds_read_b64_tr_b16 v[68:69], v90 offset:0\n"
  "ds_read_b64_tr_b16 v[70:71], v90 offset:512\n"
  "buffer_load_dwordx4 v[232:235], v101, s[20:23], s44 offen offset:2048 sc1\n"
  "v_mfma_f32_16x16x32_bf16 v[52:55], v[72:75], v[172:175], v[52:55]\n"
  "s_waitcnt lgkmcnt(6)\n"
  "v_mfma_f32_16x16x32_bf16 v[56:59], v[76:79], v[168:171], v[56:59]\n"
  "v_add_u32 v91, 0x10000, v91\n"
  "ds_read_b64_tr_b16 v[72:73], v91 offset:0\n"
  "ds_read_b64_tr_b16 v[74:75], v91 offset:512\n"
  "v_mfma_f32_16x16x32_bf16 v[60:63], v[76:79], v[172:175], v[60:63]\n"
  "s_waitcnt vmcnt(19) lgkmcnt(6)\n"
  "v_mfma_f32_16x16x32_bf16 v[0:3], v[80:83], v[176:179], v[0:3]\n"
  "ds_read_b64_tr_b16 v[76:77], v88 offset:8192\n"
  "ds_read_b64_tr_b16 v[78:79], v88 offset:8704\n"
  "s_setprio 0\n"
  "s_waitcnt vmcnt(18)\n"
  "v_mfma_f32_16x16x32_bf16 v[4:7], v[80:83], v[180:183], v[4:7]\n"
  "s_waitcnt lgkmcnt(6)\n"
  "v_mfma_f32_16x16x32_bf16 v[8:11], v[64:67], v[176:179], v[8:11]\n"
  "ds_read_b64_tr_b16 v[80:81], v89 offset:8192\n"
  "ds_read_b64_tr_b16 v[82:83], v89 offset:8704\n"
  "buffer_load_dwordx4 v[236:239], v101, s[20:23], s44 offen offset:3072 sc1\n"
  "v_mfma_f32_16x16x32_bf16 v[12:15], v[64:67], v[180:183], v[12:15]\n"
  "s_waitcnt lgkmcnt(6)\n"
  "v_mfma_f32_16x16x32_bf16 v[16:19], v[68:71], v[176:179], v[16:19]\n"
  "ds_read_b64_tr_b16 v[64:65], v90 offset:8192\n"
  "ds_read_b64_tr_b16 v[66:67], v90 offset:8704\n"
  "v_mfma_f32_16x16x32_bf16 v[20:23], v[68:71], v[180:183], v[20:23]\n"
  "s_add_u32 m0, s28, 40960\n"
  "s_mov_b32 s43, 4128768\n"
  "buffer_load_dwordx4 v94, s[16:19], s43 offen lds\n"
  "s_waitcnt lgkmcnt(6)\n"
  "v_mfma_f32_16x16x32_bf16 v[24:27], v[72:75], v[176:179], v[24:27]\n"
  "ds_read_b64_tr_b16 v[68:69], v91 offset:8192\n"
  "ds_read_b64_tr_b16 v[70:71], v91 offset:8704\n"
  "v_mfma_f32_16x16x32_bf16 v[28:31], v[72:75], v[180:183], v[28:31]\n"
  "s_waitcnt lgkmcnt(6)\n"
  "v_mfma_f32_16x16x32_bf16 v[32:35], v[76:79], v[176:179], v[32:35]\n"
  "s_barrier\n"
  "ds_read_b64_tr_b16 v[72:73], v88 offset:4096\n"
  "ds_read_b64_tr_b16 v[74:75], v88 offset:4608\n"
  "s_add_u32 s44, s29, 32768\n"
  "buffer_load_dwordx4 v[240:243], v101, s[20:23], s44 offen offset:0 sc1\n"
  "s_setprio 3\n"
  "v_mfma_f32_16x16x32_bf16 v[36:39], v[76:79], v[180:183], v[36:39]\n"
  "s_waitcnt lgkmcnt(6)\n"
  "v_mfma_f32_16x16x32_bf16 v[40:43], v[80:83], v[176:179], v[40:43]\n"
  "ds_read_b64_tr_b16 v[76:77], v89 offset:4096\n"
  "ds_read_b64_tr_b16 v[78:79], v89 offset:4608\n"
  "v_mfma_f32_16x16x32_bf16 v[44:47], v[80:83], v[180:183], v[44:47]\n"
  "s_waitcnt lgkmcnt(6)\n"
  "v_mfma_f32_16x16x32_bf16 v[48:51], v[64:67], v[176:179], v[48:51]\n"
  "ds_read_b64_tr_b16 v[80:81], v90 offset:4096\n"
  "ds_read_b64_tr_b16 v[82:83], v90 offset:4608\n"
  "v_mfma_f32_16x16x32_bf16 v[52:55], v[64:67], v[180:183], v[52:55]\n"
  "s_waitcnt lgkmcnt(6)\n"
  "v_mfma_f32_16x16x32_bf16 v[56:59], v[68:71], v[176:179], v[56:59]\n"
  "ds_read_b64_tr_b16 v[64:65], v91 offset:4096\n"
  "ds_read_b64_tr_b16 v[66:67], v91 offset:4608\n"
  "buffer_load_dwordx4 v[244:247], v101, s[20:23], s44 offen offset:1024 sc1\n"
  "v_mfma_f32_16x16x32_bf16 v[60:63], v[68:71], v[180:183], v[60:63]\n"
  "s_waitcnt vmcnt(21) lgkmcnt(6)\n"
  "v_mfma_f32_16x16x32_bf16 v[0:3], v[72:75], v[184:187], v[0:3]\n"
  "ds_read_b64_tr_b16 v[68:69], v88 offset:12288\n"
  "ds_read_b64_tr_b16 v[70:71], v88 offset:12800\n"
  "s_setprio 2\n"
  "s_waitcnt vmcnt(19)\n"
  "v_mfma_f32_16x16x32_bf16 v[4:7], v[72:75], v[188:191], v[4:7]\n"
  "s_waitcnt lgkmcnt(6)\n"
  "v_mfma_f32_16x16x32_bf16 v[8:11], v[76:79], v[184:187], v[8:11]\n"
  "ds_read_b64_tr_b16 v[72:73], v89 offset:12288\n"
  "ds_read_b64_tr_b16 v[74:75], v89 offset:12800\n"
  "v_mfma_f32_16x16x32_bf16 v[12:15], v[76:79], v[188:191], v[12:15]\n"
  "s_waitcnt lgkmcnt(6)\n"
  "v_mfma_f32_16x16x32_bf16 v[16:19], v[80:83], v[184:187], v[16:19]\n"
  "ds_read_b64_tr_b16 v[76:77], v90 offset:12288\n"
  "ds_read_b64_tr_b16 v[78:79], v90 offset:12800\n"
  "buffer_load_dwordx4 v[248:251], v101, s[20:23], s44 offen offset:2048 sc1\n"
  "v_mfma_f32_16x16x32_bf16 v[20:23], v[80:83], v[188:191], v[20:23]\n"
  "s_add_u32 m0, s28, 49152\n"
  "s_mov_b32 s43, 4718592\n"
  "buffer_load_dwordx4 v94, s[16:19], s43 offen lds\n"
  "s_waitcnt lgkmcnt(6)\n"
  "v_mfma_f32_16x16x32_bf16 v[24:27], v[64:67], v[184:187], v[24:27]\n"
  "ds_read_b64_tr_b16 v[80:81], v91 offset:12288\n"
  "ds_read_b64_tr_b16 v[82:83], v91 offset:12800\n"
  "v_mfma_f32_16x16x32_bf16 v[28:31], v[64:67], v[188:191], v[28:31]\n"
  "s_waitcnt lgkmcnt(6)\n"
  "v_mfma_f32_16x16x32_bf16 v[32:35], v[68:71], v[184:187], v[32:35]\n"
  "ds_read_b64_tr_b16 v[64:65], v88 offset:16384\n"
  "ds_read_b64_tr_b16 v[66:67], v88 offset:16896\n"
  "s_setprio 1\n"
  "v_mfma_f32_16x16x32_bf16 v[36:39], v[68:71], v[188:191], v[36:39]\n"
  "s_waitcnt lgkmcnt(6)\n"
  "v_mfma_f32_16x16x32_bf16 v[40:43], v[72:75], v[184:187], v[40:43]\n"
  "ds_read_b64_tr_b16 v[68:69], v89 offset:16384\n"
  "ds_read_b64_tr_b16 v[70:71], v89 offset:16896\n"
  "buffer_load_dwordx4 v[252:255], v101, s[20:23], s44 offen offset:3072 sc1\n"
  "v_mfma_f32_16x16x32_bf16 v[44:47], v[72:75], v[188:191], v[44:47]\n"
  "s_waitcnt lgkmcnt(6)\n"
  "v_mfma_f32_16x16x32_bf16 v[48:51], v[76:79], v[184:187], v[48:51]\n"
  "ds_read_b64_tr_b16 v[72:73], v90 offset:16384\n"
  "ds_read_b64_tr_b16 v[74:75], v90 offset:16896\n"
  "v_mfma_f32_16x16x32_bf16 v[52:55], v[76:79], v[188:191], v[52:55]\n"
  "s_waitcnt lgkmcnt(6)\n"
  "v_mfma_f32_16x16x32_bf16 v[56:59], v[80:83], v[184:187], v[56:59]\n"
  "ds_read_b64_tr_b16 v[76:77], v91 offset:16384\n"
  "ds_read_b64_tr_b16 v[78:79], v91 offset:16896\n"
  "v_mfma_f32_16x16x32_bf16 v[60:63], v[80:83], v[188:191], v[60:63]\n"
  "s_waitcnt vmcnt(21) lgkmcnt(6)\n"
  "v_mfma_f32_16x16x32_bf16 v[0:3], v[64:67], v[192:195], v[0:3]\n"
  "ds_read_b64_tr_b16 v[80:81], v88 offset:24576\n"
  "ds_read_b64_tr_b16 v[82:83], v88 offset:25088\n"
  "s_setprio 0\n"
  "s_waitcnt vmcnt(19)\n"
  "v_mfma_f32_16x16x32_bf16 v[4:7], v[64:67], v[196:199], v[4:7]\n"
  "s_waitcnt lgkmcnt(6)\n"
  "v_mfma_f32_16x16x32_bf16 v[8:11], v[68:71], v[192:195], v[8:11]\n"
  "ds_read_b64_tr_b16 v[64:65], v89 offset:24576\n"
  "ds_read_b64_tr_b16 v[66:67], v89 offset:25088\n"
  "v_mfma_f32_16x16x32_bf16 v[12:15], v[68:71], v[196:199], v[12:15]\n"
  "s_waitcnt lgkmcnt(6)\n"
  "v_mfma_f32_16x16x32_bf16 v[16:19], v[72:75], v[192:195], v[16:19]\n"
  "ds_read_b64_tr_b16 v[68:69], v90 offset:24576\n"
  "ds_read_b64_tr_b16 v[70:71], v90 offset:25088\n"
  "v_mfma_f32_16x16x32_bf16 v[20:23], v[72:75], v[196:199], v[20:23]\n"
  "s_add_u32 m0, s28, 57344\n"
  "s_mov_b32 s43, 128\n"
  "buffer_load_dwordx4 v94, s[12:15], s43 offen lds\n"
  "s_waitcnt lgkmcnt(6)\n"
  "v_mfma_f32_16x16x32_bf16 v[24:27], v[76:79], v[192:195], v[24:27]\n"
  "ds_read_b64_tr_b16 v[72:73], v91 offset:24576\n"
  "ds_read_b64_tr_b16 v[74:75], v91 offset:25088\n"
  "v_mfma_f32_16x16x32_bf16 v[28:31], v[76:79], v[196:199], v[28:31]\n"
  "s_waitcnt lgkmcnt(6)\n"
  "v_mfma_f32_16x16x32_bf16 v[32:35], v[80:83], v[192:195], v[32:35]\n"
  "s_barrier\n"
  "ds_read_b64_tr_b16 v[76:77], v88 offset:20480\n"
  "ds_read_b64_tr_b16 v[78:79], v88 offset:20992\n"
  "s_setprio 3\n"
  "v_mfma_f32_16x16x32_bf16 v[36:39], v[80:83], v[196:199], v[36:39]\n"
  "s_waitcnt lgkmcnt(6)\n"
  "v_mfma_f32_16x16x32_bf16 v[40:43], v[64:67], v[192:195], v[40:43]\n"
  "ds_read_b64_tr_b16 v[80:81], v89 offset:20480\n"
  "ds_read_b64_tr_b16 v[82:83], v89 offset:20992\n"
  "v_mfma_f32_16x16x32_bf16 v[44:47], v[64:67], v[196:199], v[44:47]\n"
  "s_waitcnt lgkmcnt(6)\n"
  "v_mfma_f32_16x16x32_bf16 v[48:51], v[68:71], v[192:195], v[48:51]\n"
  "ds_read_b64_tr_b16 v[64:65], v90 offset:20480\n"
  "ds_read_b64_tr_b16 v[66:67], v90 offset:20992\n"
  "v_mfma_f32_16x16x32_bf16 v[52:55], v[68:71], v[196:199], v[52:55]\n"
  "s_waitcnt lgkmcnt(6)\n"
  "v_mfma_f32_16x16x32_bf16 v[56:59], v[72:75], v[192:195], v[56:59]\n"
  "ds_read_b64_tr_b16 v[68:69], v91 offset:20480\n"
  "ds_read_b64_tr_b16 v[70:71], v91 offset:20992\n"
  "v_mfma_f32_16x16x32_bf16 v[60:63], v[72:75], v[196:199], v[60:63]\n"
  "s_add_u32 m0, s28, 65536\n"
  "s_mov_b32 s43, 589952\n"
  "buffer_load_dwordx4 v94, s[12:15], s43 offen lds\n"
  "s_waitcnt vmcnt(20) lgkmcnt(6)\n"
  "v_mfma_f32_16x16x32_bf16 v[0:3], v[76:79], v[200:203], v[0:3]\n"
  "ds_read_b64_tr_b16 v[72:73], v88 offset:28672\n"
  "ds_read_b64_tr_b16 v[74:75], v88 offset:29184\n"
  "s_setprio 2\n"
  "s_waitcnt vmcnt(19)\n"
  "v_mfma_f32_16x16x32_bf16 v[4:7], v[76:79], v[204:207], v[4:7]\n"
  "s_waitcnt lgkmcnt(6)\n"
  "v_mfma_f32_16x16x32_bf16 v[8:11], v[80:83], v[200:203], v[8:11]\n"
  "ds_read_b64_tr_b16 v[76:77], v89 offset:28672\n"
  "ds_read_b64_tr_b16 v[78:79], v89 offset:29184\n"
  "v_mfma_f32_16x16x32_bf16 v[12:15], v[80:83], v[204:207], v[12:15]\n"
  "s_waitcnt lgkmcnt(6)\n"
  "v_mfma_f32_16x16x32_bf16 v[16:19], v[64:67], v[200:203], v[16:19]\n"
  "ds_read_b64_tr_b16 v[80:81], v90 offset:28672\n"
  "ds_read_b64_tr_b16 v[82:83], v90 offset:29184\n"
  "v_mfma_f32_16x16x32_bf16 v[20:23], v[64:67], v[204:207], v[20:23]\n"
  "s_waitcnt lgkmcnt(6)\n"
  "v_mfma_f32_16x16x32_bf16 v[24:27], v[68:71], v[200:203], v[24:27]\n"
  "ds_read_b64_tr_b16 v[64:65], v91 offset:28672\n"
  "ds_read_b64_tr_b16 v[66:67], v91 offset:29184\n"
  "v_mfma_f32_16x16x32_bf16 v[28:31], v[68:71], v[204:207], v[28:31]\n"
  "s_waitcnt lgkmcnt(6)\n"
  "v_mfma_f32_16x16x32_bf16 v[32:35], v[72:75], v[200:203], v[32:35]\n"
  "ds_read_b64_tr_b16 v[68:69], v88 offset:32768\n"
  "ds_read_b64_tr_b16 v[70:71], v88 offset:33280\n"
  "s_setprio 1\n"
  "v_mfma_f32_16x16x32_bf16 v[36:39], v[72:75], v[204:207], v[36:39]\n"
  "s_add_u32 m0, s28, 73728\n"
  "s_mov_b32 s43, 1179776\n"
  "buffer_load_dwordx4 v94, s[12:15], s43 offen lds\n"
  "s_waitcnt lgkmcnt(6)\n"
  "v_mfma_f32_16x16x32_bf16 v[40:43], v[76:79], v[200:203], v[40:43]\n"
  "ds_read_b64_tr_b16 v[72:73], v89 offset:32768\n"
  "ds_read_b64_tr_b16 v[74:75], v89 offset:33280\n"
  "v_mfma_f32_16x16x32_bf16 v[44:47], v[76:79], v[204:207], v[44:47]\n"
  "s_waitcnt lgkmcnt(6)\n"
  "v_mfma_f32_16x16x32_bf16 v[48:51], v[80:83], v[200:203], v[48:51]\n"
  "ds_read_b64_tr_b16 v[76:77], v90 offset:32768\n"
  "ds_read_b64_tr_b16 v[78:79], v90 offset:33280\n"
  "v_mfma_f32_16x16x32_bf16 v[52:55], v[80:83], v[204:207], v[52:55]\n"
  "s_waitcnt lgkmcnt(6)\n"
  "v_mfma_f32_16x16x32_bf16 v[56:59], v[64:67], v[200:203], v[56:59]\n"
  "ds_read_b64_tr_b16 v[80:81], v91 offset:32768\n"
  "ds_read_b64_tr_b16 v[82:83], v91 offset:33280\n"
  "v_mfma_f32_16x16x32_bf16 v[60:63], v[64:67], v[204:207], v[60:63]\n"
  "s_waitcnt vmcnt(18) lgkmcnt(6)\n"
  "v_mfma_f32_16x16x32_bf16 v[0:3], v[68:71], v[208:211], v[0:3]\n"
  "ds_read_b64_tr_b16 v[64:65], v88 offset:40960\n"
  "ds_read_b64_tr_b16 v[66:67], v88 offset:41472\n"
  "s_setprio 0\n"
  "s_waitcnt vmcnt(17)\n"
  "v_mfma_f32_16x16x32_bf16 v[4:7], v[68:71], v[212:215], v[4:7]\n"
  "s_waitcnt lgkmcnt(6)\n"
  "v_mfma_f32_16x16x32_bf16 v[8:11], v[72:75], v[208:211], v[8:11]\n"
  "ds_read_b64_tr_b16 v[68:69], v89 offset:40960\n"
  "ds_read_b64_tr_b16 v[70:71], v89 offset:41472\n"
  "v_mfma_f32_16x16x32_bf16 v[12:15], v[72:75], v[212:215], v[12:15]\n"
  "s_waitcnt lgkmcnt(6)\n"
  "v_mfma_f32_16x16x32_bf16 v[16:19], v[76:79], v[208:211], v[16:19]\n"
  "ds_read_b64_tr_b16 v[72:73], v90 offset:40960\n"
  "ds_read_b64_tr_b16 v[74:75], v90 offset:41472\n"
  "buffer_load_dword v87, v84, s[36:39], 0 offen\n"
  "v_mfma_f32_16x16x32_bf16 v[20:23], v[76:79], v[212:215], v[20:23]\n"
  "s_waitcnt lgkmcnt(6)\n"
  "v_mfma_f32_16x16x32_bf16 v[24:27], v[80:83], v[208:211], v[24:27]\n"
  "ds_read_b64_tr_b16 v[76:77], v91 offset:40960\n"
  "ds_read_b64_tr_b16 v[78:79], v91 offset:41472\n"
  "v_mfma_f32_16x16x32_bf16 v[28:31], v[80:83], v[212:215], v[28:31]\n"
  "s_waitcnt lgkmcnt(6)\n"
  "v_mfma_f32_16x16x32_bf16 v[32:35], v[64:67], v[208:211], v[32:35]\n"
  "s_barrier\n"
  "ds_read_b64_tr_b16 v[80:81], v88 offset:36864\n"
  "ds_read_b64_tr_b16 v[82:83], v88 offset:37376\n"
  "s_setprio 3\n"
  "v_mfma_f32_16x16x32_bf16 v[36:39], v[64:67], v[212:215], v[36:39]\n"
  "s_add_u32 m0, s28, 81920\n"
  "s_mov_b32 s43, 1769600\n"
  "buffer_load_dwordx4 v94, s[12:15], s43 offen lds\n"
  "s_waitcnt lgkmcnt(6)\n"
  "v_mfma_f32_16x16x32_bf16 v[40:43], v[68:71], v[208:211], v[40:43]\n"
  "ds_read_b64_tr_b16 v[64:65], v89 offset:36864\n"
  "ds_read_b64_tr_b16 v[66:67], v89 offset:37376\n"
  "v_mfma_f32_16x16x32_bf16 v[44:47], v[68:71], v[212:215], v[44:47]\n"
  "s_waitcnt lgkmcnt(6)\n"
  "v_mfma_f32_16x16x32_bf16 v[48:51], v[72:75], v[208:211], v[48:51]\n"
  "ds_read_b64_tr_b16 v[68:69], v90 offset:36864\n"
  "ds_read_b64_tr_b16 v[70:71], v90 offset:37376\n"
  "v_mfma_f32_16x16x32_bf16 v[52:55], v[72:75], v[212:215], v[52:55]\n"
  "s_waitcnt lgkmcnt(6)\n"
  "v_mfma_f32_16x16x32_bf16 v[56:59], v[76:79], v[208:211], v[56:59]\n"
  "ds_read_b64_tr_b16 v[72:73], v91 offset:36864\n"
  "ds_read_b64_tr_b16 v[74:75], v91 offset:37376\n"
  "v_mfma_f32_16x16x32_bf16 v[60:63], v[76:79], v[212:215], v[60:63]\n"
  "s_waitcnt vmcnt(18) lgkmcnt(6)\n"
  "v_mfma_f32_16x16x32_bf16 v[0:3], v[80:83], v[216:219], v[0:3]\n"
  "ds_read_b64_tr_b16 v[76:77], v88 offset:45056\n"
  "ds_read_b64_tr_b16 v[78:79], v88 offset:45568\n"
  "v_lshrrev_b32 v93, 16, v111\n"
  "s_setprio 2\n"
  "s_waitcnt vmcnt(16)\n"
  "v_mfma_f32_16x16x32_bf16 v[4:7], v[80:83], v[220:223], v[4:7]\n"
  "s_waitcnt lgkmcnt(6)\n"
  "v_mfma_f32_16x16x32_bf16 v[8:11], v[64:67], v[216:219], v[8:11]\n"
  "ds_read_b64_tr_b16 v[80:81], v89 offset:45056\n"
  "ds_read_b64_tr_b16 v[82:83], v89 offset:45568\n"
  "v_mfma_f32_16x16x32_bf16 v[12:15], v[64:67], v[220:223], v[12:15]\n"
  "s_add_u32 m0, s28, 90112\n"
  "s_mov_b32 s43, 2359424\n"
  "buffer_load_dwordx4 v94, s[12:15], s43 offen lds\n"
  "s_waitcnt lgkmcnt(6)\n"
  "v_mfma_f32_16x16x32_bf16 v[16:19], v[68:71], v[216:219], v[16:19]\n"
  "ds_read_b64_tr_b16 v[64:65], v90 offset:45056\n"
  "ds_read_b64_tr_b16 v[66:67], v90 offset:45568\n"
  "v_mfma_f32_16x16x32_bf16 v[20:23], v[68:71], v[220:223], v[20:23]\n"
  "s_waitcnt lgkmcnt(6)\n"
  "v_mfma_f32_16x16x32_bf16 v[24:27], v[72:75], v[216:219], v[24:27]\n"
  "ds_read_b64_tr_b16 v[68:69], v91 offset:45056\n"
  "ds_read_b64_tr_b16 v[70:71], v91 offset:45568\n"
  "v_mfma_f32_16x16x32_bf16 v[28:31], v[72:75], v[220:223], v[28:31]\n"
  "s_waitcnt lgkmcnt(6)\n"
  "v_mfma_f32_16x16x32_bf16 v[32:35], v[76:79], v[216:219], v[32:35]\n"
  "ds_read_b64_tr_b16 v[72:73], v88 offset:49152\n"
  "ds_read_b64_tr_b16 v[74:75], v88 offset:49664\n"
  "buffer_load_dword v87, v84, s[36:39], s35 offen\n"
  "s_setprio 1\n"
  "v_mfma_f32_16x16x32_bf16 v[36:39], v[76:79], v[220:223], v[36:39]\n"
  "s_waitcnt lgkmcnt(6)\n"
  "v_mfma_f32_16x16x32_bf16 v[40:43], v[80:83], v[216:219], v[40:43]\n"
  "ds_read_b64_tr_b16 v[76:77], v89 offset:49152\n"
  "ds_read_b64_tr_b16 v[78:79], v89 offset:49664\n"
  "v_mfma_f32_16x16x32_bf16 v[44:47], v[80:83], v[220:223], v[44:47]\n"
  "s_waitcnt lgkmcnt(6)\n"
  "v_mfma_f32_16x16x32_bf16 v[48:51], v[64:67], v[216:219], v[48:51]\n"
  "ds_read_b64_tr_b16 v[80:81], v90 offset:49152\n"
  "ds_read_b64_tr_b16 v[82:83], v90 offset:49664\n"
  "v_mfma_f32_16x16x32_bf16 v[52:55], v[64:67], v[220:223], v[52:55]\n"
  "s_waitcnt lgkmcnt(6)\n"
  "v_mfma_f32_16x16x32_bf16 v[56:59], v[68:71], v[216:219], v[56:59]\n"
  "ds_read_b64_tr_b16 v[64:65], v91 offset:49152\n"
  "ds_read_b64_tr_b16 v[66:67], v91 offset:49664\n"
  "v_mfma_f32_16x16x32_bf16 v[60:63], v[68:71], v[220:223], v[60:63]\n"
  "s_waitcnt vmcnt(17) lgkmcnt(6)\n"
  "v_mfma_f32_16x16x32_bf16 v[0:3], v[72:75], v[224:227], v[0:3]\n"
  "ds_read_b64_tr_b16 v[68:69], v88 offset:53248\n"
  "ds_read_b64_tr_b16 v[70:71], v88 offset:53760\n"
  "s_setprio 0\n"
  "s_waitcnt vmcnt(15)\n"
  "v_mfma_f32_16x16x32_bf16 v[4:7], v[72:75], v[228:231], v[4:7]\n"
  "s_waitcnt lgkmcnt(6)\n"
  "v_mfma_f32_16x16x32_bf16 v[8:11], v[76:79], v[224:227], v[8:11]\n"
  "ds_read_b64_tr_b16 v[72:73], v89 offset:53248\n"
  "ds_read_b64_tr_b16 v[74:75], v89 offset:53760\n"
  "v_mfma_f32_16x16x32_bf16 v[12:15], v[76:79], v[228:231], v[12:15]\n"
  "s_waitcnt lgkmcnt(6)\n"
  "v_mfma_f32_16x16x32_bf16 v[16:19], v[80:83], v[224:227], v[16:19]\n"
  "ds_read_b64_tr_b16 v[76:77], v90 offset:53248\n"
  "ds_read_b64_tr_b16 v[78:79], v90 offset:53760\n"
  "s_add_u32 s43, s28, 131072\n"
  "v_add_u32 v86, s43, v100\n"
  "v_mfma_f32_16x16x32_bf16 v[20:23], v[80:83], v[228:231], v[20:23]\n"
  "s_waitcnt lgkmcnt(6)\n"
  "v_mfma_f32_16x16x32_bf16 v[24:27], v[64:67], v[224:227], v[24:27]\n"
  "ds_read_b64_tr_b16 v[80:81], v91 offset:53248\n"
  "ds_read_b64_tr_b16 v[82:83], v91 offset:53760\n"
  "v_mfma_f32_16x16x32_bf16 v[28:31], v[64:67], v[228:231], v[28:31]\n"
  "s_waitcnt vmcnt(14) lgkmcnt(6)\n"
  "v_mfma_f32_16x16x32_bf16 v[0:3], v[68:71], v[232:235], v[0:3]\n"
  "s_barrier\n"
  "v_add_u32 v88, 0x10000, v88\n"
  "ds_read_b64_tr_b16 v[64:65], v88 offset:0\n"
  "ds_read_b64_tr_b16 v[66:67], v88 offset:512\n"
  "s_setprio 3\n"
  "s_waitcnt vmcnt(13)\n"
  "v_mfma_f32_16x16x32_bf16 v[4:7], v[68:71], v[236:239], v[4:7]\n"
  "s_add_u32 m0, s28, 98304\n"
  "s_mov_b32 s43, 2949248\n"
  "buffer_load_dwordx4 v94, s[12:15], s43 offen lds\n"
  "s_waitcnt lgkmcnt(6)\n"
  "v_mfma_f32_16x16x32_bf16 v[8:11], v[72:75], v[232:235], v[8:11]\n"
  "v_add_u32 v89, 0x10000, v89\n"
  "ds_read_b64_tr_b16 v[68:69], v89 offset:0\n"
  "ds_read_b64_tr_b16 v[70:71], v89 offset:512\n"
  "v_mfma_f32_16x16x32_bf16 v[12:15], v[72:75], v[236:239], v[12:15]\n"
  "s_waitcnt lgkmcnt(6)\n"
  "v_mfma_f32_16x16x32_bf16 v[16:19], v[76:79], v[232:235], v[16:19]\n"
  "v_add_u32 v90, 0x10000, v90\n"
  "ds_read_b64_tr_b16 v[72:73], v90 offset:0\n"
  "ds_read_b64_tr_b16 v[74:75], v90 offset:512\n"
  "v_mfma_f32_16x16x32_bf16 v[20:23], v[76:79], v[236:239], v[20:23]\n"
  "s_waitcnt lgkmcnt(6)\n"
  "v_mfma_f32_16x16x32_bf16 v[24:27], v[80:83], v[232:235], v[24:27]\n"
  "v_add_u32 v91, 0x10000, v91\n"
  "ds_read_b64_tr_b16 v[76:77], v91 offset:0\n"
  "ds_read_b64_tr_b16 v[78:79], v91 offset:512\n"
  "v_mfma_f32_16x16x32_bf16 v[28:31], v[80:83], v[236:239], v[28:31]\n"
  "s_waitcnt vmcnt(12) lgkmcnt(6)\n"
  "v_mfma_f32_16x16x32_bf16 v[0:3], v[64:67], v[240:243], v[0:3]\n"
  "ds_read_b64_tr_b16 v[80:81], v88 offset:4096\n"
  "ds_read_b64_tr_b16 v[82:83], v88 offset:4608\n"
  "s_waitcnt vmcnt(11)\n"
  "v_mfma_f32_16x16x32_bf16 v[4:7], v[64:67], v[244:247], v[4:7]\n"
  "s_waitcnt lgkmcnt(6)\n"
  "v_mfma_f32_16x16x32_bf16 v[8:11], v[68:71], v[240:243], v[8:11]\n"
  "ds_read_b64_tr_b16 v[64:65], v89 offset:4096\n"
  "ds_read_b64_tr_b16 v[66:67], v89 offset:4608\n"
  "v_mfma_f32_16x16x32_bf16 v[12:15], v[68:71], v[244:247], v[12:15]\n"
  "s_add_u32 m0, s28, 106496\n"
  "s_mov_b32 s43, 3539072\n"
  "buffer_load_dwordx4 v94, s[12:15], s43 offen lds\n"
  "s_waitcnt lgkmcnt(6)\n"
  "v_mfma_f32_16x16x32_bf16 v[16:19], v[72:75], v[240:243], v[16:19]\n"
  "ds_read_b64_tr_b16 v[68:69], v90 offset:4096\n"
  "ds_read_b64_tr_b16 v[70:71], v90 offset:4608\n"
  "s_setprio 2\n"
  "v_mfma_f32_16x16x32_bf16 v[20:23], v[72:75], v[244:247], v[20:23]\n"
  "s_waitcnt lgkmcnt(6)\n"
  "v_mfma_f32_16x16x32_bf16 v[24:27], v[76:79], v[240:243], v[24:27]\n"
  "ds_read_b64_tr_b16 v[72:73], v91 offset:4096\n"
  "ds_read_b64_tr_b16 v[74:75], v91 offset:4608\n"
  "v_mfma_f32_16x16x32_bf16 v[28:31], v[76:79], v[244:247], v[28:31]\n"
  "s_waitcnt vmcnt(11) lgkmcnt(6)\n"
  "v_mfma_f32_16x16x32_bf16 v[0:3], v[80:83], v[248:251], v[0:3]\n"
  "v_add_u32 v88, 0xffffe000, v88\n"
  "ds_read_b64_tr_b16 v[76:77], v88 offset:0\n"
  "ds_read_b64_tr_b16 v[78:79], v88 offset:512\n"
  "s_waitcnt vmcnt(9)\n"
  "v_mfma_f32_16x16x32_bf16 v[4:7], v[80:83], v[252:255], v[4:7]\n"
  "s_waitcnt lgkmcnt(6)\n"
  "v_mfma_f32_16x16x32_bf16 v[8:11], v[64:67], v[248:251], v[8:11]\n"
  "v_add_u32 v89, 0xffffe000, v89\n"
  "ds_read_b64_tr_b16 v[80:81], v89 offset:0\n"
  "ds_read_b64_tr_b16 v[82:83], v89 offset:512\n"
  "v_mfma_f32_16x16x32_bf16 v[12:15], v[64:67], v[252:255], v[12:15]\n"
  "s_waitcnt lgkmcnt(6)\n"
  "v_mfma_f32_16x16x32_bf16 v[16:19], v[68:71], v[248:251], v[16:19]\n"
  "v_add_u32 v90, 0xffffe000, v90\n"
  "ds_read_b64_tr_b16 v[64:65], v90 offset:0\n"
  "ds_read_b64_tr_b16 v[66:67], v90 offset:512\n"
  "v_mfma_f32_16x16x32_bf16 v[20:23], v[68:71], v[252:255], v[20:23]\n"
  "s_waitcnt lgkmcnt(6)\n"
  "v_mfma_f32_16x16x32_bf16 v[24:27], v[72:75], v[248:251], v[24:27]\n"
  "v_add_u32 v91, 0xffffe000, v91\n"
  "ds_read_b64_tr_b16 v[68:69], v91 offset:0\n"
  "ds_read_b64_tr_b16 v[70:71], v91 offset:512\n"
  "v_mfma_f32_16x16x32_bf16 v[28:31], v[72:75], v[252:255], v[28:31]\n"
  "s_waitcnt lgkmcnt(6)\n"
  "v_mfma_f32_16x16x32_bf16 v[32:35], v[76:79], v[224:227], v[32:35]\n"
  "ds_read_b64_tr_b16 v[72:73], v88 offset:4096\n"
  "ds_read_b64_tr_b16 v[74:75], v88 offset:4608\n"
  "s_branch LJOIN_%=\n"
  "LPX_%=:\n"
  "s_cmp_eq_u32 s46, 0\n"
  "s_cbranch_scc1 LPXB_%=\n"
  "s_sub_u32 s46, s46, 1\n"
  "s_sleep 1\n"
  "buffer_load_dword v137, v136, s[20:23], s80 offen sc1\n"
  "s_waitcnt vmcnt(0) lgkmcnt(0)\n"
  "v_cmp_ne_u32 vcc, s47, v137\n"
  "s_cbranch_vccnz LPX_%=\n"
  "s_branch LPXB_%=\n"
  "LK1V_%=:\n"
  "s_add_u32 s44, s29, 0\n"
  "buffer_load_dwordx4 v[112:115], v101, s[20:23], s44 offen offset:0 sc1\n"
  "buffer_load_dwordx4 v[116:119], v101, s[20:23], s44 offen offset:1024 sc1\n"
  "buffer_load_dwordx4 v[120:123], v101, s[20:23], s44 offen offset:2048 sc1\n"
  "buffer_load_dwordx4 v[124:127], v101, s[20:23], s44 offen offset:3072 sc1\n"
  "s_add_u32 s44, s29, 4096\n"
  "buffer_load_dwordx4 v[128:131], v101, s[20:23], s44 offen offset:0 sc1\n"
  "buffer_load_dwordx4 v[132:135], v101, s[20:23], s44 offen offset:1024 sc1\n"
  "s_barrier\n"
  "ds_read_b64_tr_b16 v[64:65], v88 offset:0\n"
  "ds_read_b64_tr_b16 v[66:67], v88 offset:512\n"
  "ds_read_b64_tr_b16 v[68:69], v89 offset:0\n"
  "ds_read_b64_tr_b16 v[70:71], v89 offset:512\n"
  "ds_read_b64_tr_b16 v[72:73], v90 offset:0\n"
  "ds_read_b64_tr_b16 v[74:75], v90 offset:512\n"
  "ds_read_b64_tr_b16 v[76:77], v91 offset:0\n"
  "ds_read_b64_tr_b16 v[78:79], v91 offset:512\n"
  "s_waitcnt vmcnt(5) lgkmcnt(6)\n"
  "v_mfma_f32_16x16x32_bf16 v[0:3], v[112:115], v[64:67], 0\n"
  "ds_read_b64_tr_b16 v[80:81], v88 offset:8192\n"
  "ds_read_b64_tr_b16 v[82:83], v88 offset:8704\n"
  "buffer_load_dwordx4 v[136:139], v101, s[20:23], s44 offen offset:2048 sc1\n"
  "s_setprio 3\n"
  "s_waitcnt vmcnt(5)\n"
  "v_mfma_f32_16x16x32_bf16 v[4:7], v[116:119], v[64:67], 0\n"
  "s_waitcnt lgkmcnt(6)\n"
  "v_mfma_f32_16x16x32_bf16 v[8:11], v[112:115], v[68:71], 0\n"
  "ds_read_b64_tr_b16 v[64:65], v89 offset:8192\n"
  "ds_read_b64_tr_b16 v[66:67], v89 offset:8704\n"
  "v_mfma_f32_16x16x32_bf16 v[12:15], v[116:119], v[68:71], 0\n"
  "s_waitcnt lgkmcnt(6)\n"
  "v_mfma_f32_16x16x32_bf16 v[16:19], v[112:115], v[72:75], 0\n"
  "ds_read_b64_tr_b16 v[68:69], v90 offset:8192\n"
  "ds_read_b64_tr_b16 v[70:71], v90 offset:8704\n"
  "buffer_load_dwordx4 v[140:143], v101, s[20:23], s44 offen offset:3072 sc1\n"
  "v_mfma_f32_16x16x32_bf16 v[20:23], v[116:119], v[72:75], 0\n"
  "s_waitcnt lgkmcnt(6)\n"
  "v_mfma_f32_16x16x32_bf16 v[24:27], v[112:115], v[76:79], 0\n"
  "ds_read_b64_tr_b16 v[72:73], v91 offset:8192\n"
  "ds_read_b64_tr_b16 v[74:75], v91 offset:8704\n"
  "v_mfma_f32_16x16x32_bf16 v[28:31], v[116:119], v[76:79], 0\n"
  "s_waitcnt lgkmcnt(6)\n"
  "v_mfma_f32_16x16x32_bf16 v[32:35], v[80:83], v[112:115], 0\n"
  "ds_read_b64_tr_b16 v[76:77], v88 offset:4096\n"
  "ds_read_b64_tr_b16 v[78:79], v88 offset:4608\n"
  "s_add_u32 s44, s29, 8192\n"
  "buffer_load_dwordx4 v[144:147], v101, s[20:23], s44 offen offset:0 sc1\n"
  "v_mfma_f32_16x16x32_bf16 v[36:39], v[80:83], v[116:119], 0\n"
  "s_waitcnt lgkmcnt(6)\n"
  "v_mfma_f32_16x16x32_bf16 v[40:43], v[64:67], v[112:115], 0\n"
  "ds_read_b64_tr_b16 v[80:81], v89 offset:4096\n"
  "ds_read_b64_tr_b16 v[82:83], v89 offset:4608\n"
  "s_setprio 2\n"
  "v_mfma_f32_16x16x32_bf16 v[44:47], v[64:67], v[116:119], 0\n"
  "s_waitcnt lgkmcnt(6)\n"
  "v_mfma_f32_16x16x32_bf16 v[48:51], v[68:71], v[112:115], 0\n"
  "ds_read_b64_tr_b16 v[64:65], v90 offset:4096\n"
  "ds_read_b64_tr_b16 v[66:67], v90 offset:4608\n"
  "buffer_load_dwordx4 v[148:151], v101, s[20:23], s44 offen offset:1024 sc1\n"
  "v_mfma_f32_16x16x32_bf16 v[52:55], v[68:71], v[116:119], 0\n"
  "s_waitcnt lgkmcnt(6)\n"
  "v_mfma_f32_16x16x32_bf16 v[56:59], v[72:75], v[112:115], 0\n"
  "ds_read_b64_tr_b16 v[68:69], v91 offset:4096\n"
  "ds_read_b64_tr_b16 v[70:71], v91 offset:4608\n"
  "v_mfma_f32_16x16x32_bf16 v[60:63], v[72:75], v[116:119], 0\n"
  "s_waitcnt vmcnt(7) lgkmcnt(6)\n"
  "v_mfma_f32_16x16x32_bf16 v[0:3], v[120:123], v[76:79], v[0:3]\n"
  "ds_read_b64_tr_b16 v[72:73], v88 offset:12288\n"
  "ds_read_b64_tr_b16 v[74:75], v88 offset:12800\n"
  "buffer_load_dwordx4 v[152:155], v101, s[20:23], s44 offen offset:2048 sc1\n"
  "s_waitcnt vmcnt(7)\n"
  "v_mfma_f32_16x16x32_bf16 v[4:7], v[124:127], v[76:79], v[4:7]\n"
  "s_waitcnt lgkmcnt(6)\n"
  "v_mfma_f32_16x16x32_bf16 v[8:11], v[120:123], v[80:83], v[8:11]\n"
  "ds_read_b64_tr_b16 v[76:77], v89 offset:12288\n"
  "ds_read_b64_tr_b16 v[78:79], v89 offset:12800\n"
  "v_mfma_f32_16x16x32_bf16 v[12:15], v[124:127], v[80:83], v[12:15]\n"
  "s_waitcnt lgkmcnt(6)\n"
  "v_mfma_f32_16x16x32_bf16 v[16:19], v[120:123], v[64:67], v[16:19]\n"
  "ds_read_b64_tr_b16 v[80:81], v90 offset:12288\n"
  "ds_read_b64_tr_b16 v[82:83], v90 offset:12800\n"
  "buffer_load_dwordx4 v[156:159], v101, s[20:23], s44 offen offset:3072 sc1\n"
  "s_setprio 1\n"
  "v_mfma_f32_16x16x32_bf16 v[20:23], v[124:127], v[64:67], v[20:23]\n"
  "s_add_u32 m0, s28, 147456\n"
  "s_mov_b32 s43, 0\n"
  "buffer_load_dwordx4 v94, s[16:19], s43 offen lds\n"
  "s_waitcnt lgkmcnt(6)\n"
  "v_mfma_f32_16x16x32_bf16 v[24:27], v[120:123], v[68:71], v[24:27]\n"
  "ds_read_b64_tr_b16 v[64:65], v91 offset:12288\n"
  "ds_read_b64_tr_b16 v[66:67], v91 offset:12800\n"
  "v_mfma_f32_16x16x32_bf16 v[28:31], v[124:127], v[68:71], v[28:31]\n"
  "s_waitcnt lgkmcnt(6)\n"
  "v_mfma_f32_16x16x32_bf16 v[32:35], v[72:75], v[120:123], v[32:35]\n"
  "ds_read_b64_tr_b16 v[68:69], v88 offset:16384\n"
  "ds_read_b64_tr_b16 v[70:71], v88 offset:16896\n"
  "s_add_u32 s44, s29, 12288\n"
  "buffer_load_dwordx4 v[160:163], v101, s[20:23], s44 offen offset:0 sc1\n"
  "v_mfma_f32_16x16x32_bf16 v[36:39], v[72:75], v[124:127], v[36:39]\n"
  "s_waitcnt lgkmcnt(6)\n"
  "v_mfma_f32_16x16x32_bf16 v[40:43], v[76:79], v[120:123], v[40:43]\n"
  "ds_read_b64_tr_b16 v[72:73], v89 offset:16384\n"
  "ds_read_b64_tr_b16 v[74:75], v89 offset:16896\n"
  "v_mfma_f32_16x16x32_bf16 v[44:47], v[76:79], v[124:127], v[44:47]\n"
  "s_waitcnt lgkmcnt(6)\n"
  "v_mfma_f32_16x16x32_bf16 v[48:51], v[80:83], v[120:123], v[48:51]\n"
  "ds_read_b64_tr_b16 v[76:77], v90 offset:16384\n"
  "ds_read_b64_tr_b16 v[78:79], v90 offset:16896\n"
  "buffer_load_dwordx4 v[164:167], v101, s[20:23], s44 offen offset:1024 sc1\n"
  "v_mfma_f32_16x16x32_bf16 v[52:55], v[80:83], v[124:127], v[52:55]\n"
  "s_waitcnt lgkmcnt(6)\n"
  "v_mfma_f32_16x16x32_bf16 v[56:59], v[64:67], v[120:123], v[56:59]\n"
  "ds_read_b64_tr_b16 v[80:81], v91 offset:16384\n"
  "ds_read_b64_tr_b16 v[82:83], v91 offset:16896\n"
  "s_setprio 0\n"
  "v_mfma_f32_16x16x32_bf16 v[60:63], v[64:67], v[124:127], v[60:63]\n"
  "s_waitcnt vmcnt(10) lgkmcnt(6)\n"
  "v_mfma_f32_16x16x32_bf16 v[0:3], v[128:131], v[68:71], v[0:3]\n"
  "ds_read_b64_tr_b16 v[64:65], v88 offset:24576\n"
  "ds_read_b64_tr_b16 v[66:67], v88 offset:25088\n"
  "buffer_load_dwordx4 v[168:171], v101, s[20:23], s44 offen offset:2048 sc1\n"
  "s_waitcnt vmcnt(10)\n"
  "v_mfma_f32_16x16x32_bf16 v[4:7], v[132:135], v[68:71], v[4:7]\n"
  "s_waitcnt lgkmcnt(6)\n"
  "v_mfma_f32_16x16x32_bf16 v[8:11], v[128:131], v[72:75], v[8:11]\n"
  "ds_read_b64_tr_b16 v[68:69], v89 offset:24576\n"
  "ds_read_b64_tr_b16 v[70:71], v89 offset:25088\n"
  "v_mfma_f32_16x16x32_bf16 v[12:15], v[132:135], v[72:75], v[12:15]\n"
  "s_waitcnt lgkmcnt(6)\n"
  "v_mfma_f32_16x16x32_bf16 v[16:19], v[128:131], v[76:79], v[16:19]\n"
  "ds_read_b64_tr_b16 v[72:73], v90 offset:24576\n"
  "ds_read_b64_tr_b16 v[74:75], v90 offset:25088\n"
  "buffer_load_dwordx4 v[172:175], v101, s[20:23], s44 offen offset:3072 sc1\n"
  "v_mfma_f32_16x16x32_bf16 v[20:23], v[132:135], v[76:79], v[20:23]\n"
  "s_add_u32 m0, s28, 155648\n"
  "s_mov_b32 s43, 589824\n"
  "buffer_load_dwordx4 v94, s[16:19], s43 offen lds\n"
  "s_waitcnt lgkmcnt(6)\n"
  "v_mfma_f32_16x16x32_bf16 v[24:27], v[128:131], v[80:83], v[24:27]\n"
  "ds_read_b64_tr_b16 v[76:77], v91 offset:24576\n"
  "ds_read_b64_tr_b16 v[78:79], v91 offset:25088\n"
  "v_mfma_f32_16x16x32_bf16 v[28:31], v[132:135], v[80:83], v[28:31]\n"
  "s_waitcnt lgkmcnt(6)\n"
  "v_mfma_f32_16x16x32_bf16 v[32:35], v[64:67], v[128:131], v[32:35]\n"
  "s_barrier\n"
  "ds_read_b64_tr_b16 v[80:81], v88 offset:20480\n"
  "ds_read_b64_tr_b16 v[82:83], v88 offset:20992\n"
  "s_add_u32 s44, s29, 16384\n"
  "buffer_load_dwordx4 v[176:179], v101, s[20:23], s44 offen offset:0 sc1\n"
  "s_setprio 3\n"
  "v_mfma_f32_16x16x32_bf16 v[36:39], v[64:67], v[132:135], v[36:39]\n"
  "s_waitcnt lgkmcnt(6)\n"
  "v_mfma_f32_16x16x32_bf16 v[40:43], v[68:71], v[128:131], v[40:43]\n"
  "ds_read_b64_tr_b16 v[64:65], v89 offset:20480\n"
  "ds_read_b64_tr_b16 v[66:67], v89 offset:20992\n"
  "v_mfma_f32_16x16x32_bf16 v[44:47], v[68:71], v[132:135], v[44:47]\n"
  "s_waitcnt lgkmcnt(6)\n"
  "v_mfma_f32_16x16x32_bf16 v[48:51], v[72:75], v[128:131], v[48:51]\n"
  "ds_read_b64_tr_b16 v[68:69], v90 offset:20480\n"
  "ds_read_b64_tr_b16 v[70:71], v90 offset:20992\n"
  "v_mfma_f32_16x16x32_bf16 v[52:55], v[72:75], v[132:135], v[52:55]\n"
  "s_waitcnt lgkmcnt(6)\n"
  "v_mfma_f32_16x16x32_bf16 v[56:59], v[76:79], v[128:131], v[56:59]\n"
  "ds_read_b64_tr_b16 v[72:73], v91 offset:20480\n"
  "ds_read_b64_tr_b16 v[74:75], v91 offset:20992\n"
  "buffer_load_dwordx4 v[180:183], v101, s[20:23], s44 offen offset:1024 sc1\n"
  "v_mfma_f32_16x16x32_bf16 v[60:63], v[76:79], v[132:135], v[60:63]\n"
  "s_waitcnt vmcnt(13) lgkmcnt(6)\n"
  "v_mfma_f32_16x16x32_bf16 v[0:3], v[136:139], v[80:83], v[0:3]\n"
  "ds_read_b64_tr_b16 v[76:77], v88 offset:28672\n"
  "ds_read_b64_tr_b16 v[78:79], v88 offset:29184\n"
  "s_setprio 2\n"
  "s_waitcnt vmcnt(12)\n"
  "v_mfma_f32_16x16x32_bf16 v[4:7], v[140:143], v[80:83], v[4:7]\n"
  "s_waitcnt lgkmcnt(6)\n"
  "v_mfma_f32_16x16x32_bf16 v[8:11], v[136:139], v[64:67], v[8:11]\n"
  "ds_read_b64_tr_b16 v[80:81], v89 offset:28672\n"
  "ds_read_b64_tr_b16 v[82:83], v89 offset:29184\n"
  "v_mfma_f32_16x16x32_bf16 v[12:15], v[140:143], v[64:67], v[12:15]\n"
  "s_waitcnt lgkmcnt(6)\n"
  "v_mfma_f32_16x16x32_bf16 v[16:19], v[136:139], v[68:71], v[16:19]\n"
  "ds_read_b64_tr_b16 v[64:65], v90 offset:28672\n"
  "ds_read_b64_tr_b16 v[66:67], v90 offset:29184\n"
  "buffer_load_dwordx4 v[184:187], v101, s[20:23], s44 offen offset:2048 sc1\n"
  "v_mfma_f32_16x16x32_bf16 v[20:23], v[140:143], v[68:71], v[20:23]\n"
  "s_add_u32 m0, s28, 0\n"
  "s_mov_b32 s43, 1179648\n"
  "buffer_load_dwordx4 v94, s[16:19], s43 offen lds\n"
  "s_waitcnt lgkmcnt(6)\n"
  "v_mfma_f32_16x16x32_bf16 v[24:27], v[136:139], v[72:75], v[24:27]\n"
  "ds_read_b64_tr_b16 v[68:69], v91 offset:28672\n"
  "ds_read_b64_tr_b16 v[70:71], v91 offset:29184\n"
  "v_mfma_f32_16x16x32_bf16 v[28:31], v[140:143], v[72:75], v[28:31]\n"
  "s_waitcnt lgkmcnt(6)\n"
  "v_mfma_f32_16x16x32_bf16 v[32:35], v[76:79], v[136:139], v[32:35]\n"
  "ds_read_b64_tr_b16 v[72:73], v88 offset:32768\n"
  "ds_read_b64_tr_b16 v[74:75], v88 offset:33280\n"
  "s_setprio 1\n"
  "v_mfma_f32_16x16x32_bf16 v[36:39], v[76:79], v[140:143], v[36:39]\n"
  "s_waitcnt lgkmcnt(6)\n"
  "v_mfma_f32_16x16x32_bf16 v[40:43], v[80:83], v[136:139], v[40:43]\n"
  "ds_read_b64_tr_b16 v[76:77], v89 offset:32768\n"
  "ds_read_b64_tr_b16 v[78:79], v89 offset:33280\n"
  "buffer_load_dwordx4 v[188:191], v101, s[20:23], s44 offen offset:3072 sc1\n"
  "v_mfma_f32_16x16x32_bf16 v[44:47], v[80:83], v[140:143], v[44:47]\n"
  "s_waitcnt lgkmcnt(6)\n"
  "v_mfma_f32_16x16x32_bf16 v[48:51], v[64:67], v[136:139], v[48:51]\n"
  "ds_read_b64_tr_b16 v[80:81], v90 offset:32768\n"
  "ds_read_b64_tr_b16 v[82:83], v90 offset:33280\n"
  "v_mfma_f32_16x16x32_bf16 v[52:55], v[64:67], v[140:143], v[52:55]\n"
  "s_waitcnt lgkmcnt(6)\n"
  "v_mfma_f32_16x16x32_bf16 v[56:59], v[68:71], v[136:139], v[56:59]\n"
  "ds_read_b64_tr_b16 v[64:65], v91 offset:32768\n"
  "ds_read_b64_tr_b16 v[66:67], v91 offset:33280\n"
  "v_mfma_f32_16x16x32_bf16 v[60:63], v[68:71], v[140:143], v[60:63]\n"
  "s_waitcnt vmcnt(14) lgkmcnt(6)\n"
  "v_mfma_f32_16x16x32_bf16 v[0:3], v[144:147], v[72:75], v[0:3]\n"
  "ds_read_b64_tr_b16 v[68:69], v88 offset:40960\n"
  "ds_read_b64_tr_b16 v[70:71], v88 offset:41472\n"
  "s_add_u32 s44, s29, 20480\n"
  "buffer_load_dwordx4 v[192:195], v101, s[20:23], s44 offen offset:0 sc1\n"
  "s_setprio 0\n"
  "s_waitcnt vmcnt(14)\n"
  "v_mfma_f32_16x16x32_bf16 v[4:7], v[148:151], v[72:75], v[4:7]\n"
  "s_waitcnt lgkmcnt(6)\n"
  "v_mfma_f32_16x16x32_bf16 v[8:11], v[144:147], v[76:79], v[8:11]\n"
  "ds_read_b64_tr_b16 v[72:73], v89 offset:40960\n"
  "ds_read_b64_tr_b16 v[74:75], v89 offset:41472\n"
  "v_mfma_f32_16x16x32_bf16 v[12:15], v[148:151], v[76:79], v[12:15]\n"
  "s_waitcnt lgkmcnt(6)\n"
  "v_mfma_f32_16x16x32_bf16 v[16:19], v[144:147], v[80:83], v[16:19]\n"
  "ds_read_b64_tr_b16 v[76:77], v90 offset:40960\n"
  "ds_read_b64_tr_b16 v[78:79], v90 offset:41472\n"
  "v_mfma_f32_16x16x32_bf16 v[20:23], v[148:151], v[80:83], v[20:23]\n"
  "s_add_u32 m0, s28, 8192\n"
  "s_mov_b32 s43, 1769472\n"
  "buffer_load_dwordx4 v94, s[16:19], s43 offen lds\n"
  "s_waitcnt lgkmcnt(6)\n"
  "v_mfma_f32_16x16x32_bf16 v[24:27], v[144:147], v[64:67], v[24:27]\n"
  "ds_read_b64_tr_b16 v[80:81], v91 offset:40960\n"
  "ds_read_b64_tr_b16 v[82:83], v91 offset:41472\n"
  "buffer_load_dwordx4 v[196:199], v101, s[20:23], s44 offen offset:1024 sc1\n"
  "v_mfma_f32_16x16x32_bf16 v[28:31], v[148:151], v[64:67], v[28:31]\n"
  "s_waitcnt lgkmcnt(6)\n"
  "v_mfma_f32_16x16x32_bf16 v[32:35], v[68:71], v[144:147], v[32:35]\n"
  "s_barrier\n"
  "ds_read_b64_tr_b16 v[64:65], v88 offset:36864\n"
  "ds_read_b64_tr_b16 v[66:67], v88 offset:37376\n"
  "s_setprio 3\n"
  "v_mfma_f32_16x16x32_bf16 v[36:39], v[68:71], v[148:151], v[36:39]\n"
  "s_waitcnt lgkmcnt(6)\n"
  "v_mfma_f32_16x16x32_bf16 v[40:43], v[72:75], v[144:147], v[40:43]\n"
  "ds_read_b64_tr_b16 v[68:69], v89 offset:36864\n"
  "ds_read_b64_tr_b16 v[70:71], v89 offset:37376\n"
  "v_mfma_f32_16x16x32_bf16 v[44:47], v[72:75], v[148:151], v[44:47]\n"
  "s_waitcnt lgkmcnt(6)\n"
  "v_mfma_f32_16x16x32_bf16 v[48:51], v[76:79], v[144:147], v[48:51]\n"
  "ds_read_b64_tr_b16 v[72:73], v90 offset:36864\n"
  "ds_read_b64_tr_b16 v[74:75], v90 offset:37376\n"
  "buffer_load_dwordx4 v[200:203], v101, s[20:23], s44 offen offset:2048 sc1\n"
  "v_mfma_f32_16x16x32_bf16 v[52:55], v[76:79], v[148:151], v[52:55]\n"
  "s_waitcnt lgkmcnt(6)\n"
  "v_mfma_f32_16x16x32_bf16 v[56:59], v[80:83], v[144:147], v[56:59]\n"
  "ds_read_b64_tr_b16 v[76:77], v91 offset:36864\n"
  "ds_read_b64_tr_b16 v[78:79], v91 offset:37376\n"
  "v_mfma_f32_16x16x32_bf16 v[60:63], v[80:83], v[148:151], v[60:63]\n"
  "s_waitcnt vmcnt(16) lgkmcnt(6)\n"
  "v_mfma_f32_16x16x32_bf16 v[0:3], v[152:155], v[64:67], v[0:3]\n"
  "ds_read_b64_tr_b16 v[80:81], v88 offset:45056\n"
  "ds_read_b64_tr_b16 v[82:83], v88 offset:45568\n"
  "s_setprio 2\n"
  "s_waitcnt vmcnt(15)\n"
  "v_mfma_f32_16x16x32_bf16 v[4:7], v[156:159], v[64:67], v[4:7]\n"
  "s_waitcnt lgkmcnt(6)\n"
  "v_mfma_f32_16x16x32_bf16 v[8:11], v[152:155], v[68:71], v[8:11]\n"
  "ds_read_b64_tr_b16 v[64:65], v89 offset:45056\n"
  "ds_read_b64_tr_b16 v[66:67], v89 offset:45568\n"
  "buffer_load_dwordx4 v[204:207], v101, s[20:23], s44 offen offset:3072 sc1\n"
  "v_mfma_f32_16x16x32_bf16 v[12:15], v[156:159], v[68:71], v[12:15]\n"
  "s_waitcnt lgkmcnt(6)\n"
  "v_mfma_f32_16x16x32_bf16 v[16:19], v[152:155], v[72:75], v[16:19]\n"
  "ds_read_b64_tr_b16 v[68:69], v90 offset:45056\n"
  "ds_read_b64_tr_b16 v[70:71], v90 offset:45568\n"
  "v_mfma_f32_16x16x32_bf16 v[20:23], v[156:159], v[72:75], v[20:23]\n"
  "s_add_u32 m0, s28, 16384\n"
  "s_mov_b32 s43, 2359296\n"
  "buffer_load_dwordx4 v94, s[16:19], s43 offen lds\n"
  "s_waitcnt lgkmcnt(6)\n"
  "v_mfma_f32_16x16x32_bf16 v[24:27], v[152:155], v[76:79], v[24:27]\n"
  "ds_read_b64_tr_b16 v[72:73], v91 offset:45056\n"
  "ds_read_b64_tr_b16 v[74:75], v91 offset:45568\n"
  "v_mfma_f32_16x16x32_bf16 v[28:31], v[156:159], v[76:79], v[28:31]\n"
  "s_waitcnt lgkmcnt(6)\n"
  "v_mfma_f32_16x16x32_bf16 v[32:35], v[80:83], v[152:155], v[32:35]\n"
  "ds_read_b64_tr_b16 v[76:77], v88 offset:49152\n"
  "ds_read_b64_tr_b16 v[78:79], v88 offset:49664\n"
  "s_add_u32 s44, s29, 24576\n"
  "buffer_load_dwordx4 v[208:211], v101, s[20:23], s44 offen offset:0 sc1\n"
  "s_setprio 1\n"
  "v_mfma_f32_16x16x32_bf16 v[36:39], v[80:83], v[156:159], v[36:39]\n"
  "s_waitcnt lgkmcnt(6)\n"
  "v_mfma_f32_16x16x32_bf16 v[40:43], v[64:67], v[152:155], v[40:43]\n"
  "ds_read_b64_tr_b16 v[80:81], v89 offset:49152\n"
  "ds_read_b64_tr_b16 v[82:83], v89 offset:49664\n"
  "v_mfma_f32_16x16x32_bf16 v[44:47], v[64:67], v[156:159], v[44:47]\n"
  "s_waitcnt lgkmcnt(6)\n"
  "v_mfma_f32_16x16x32_bf16 v[48:51], v[68:71], v[152:155], v[48:51]\n"
  "ds_read_b64_tr_b16 v[64:65], v90 offset:49152\n"
  "ds_read_b64_tr_b16 v[66:67], v90 offset:49664\n"
  "v_mfma_f32_16x16x32_bf16 v[52:55], v[68:71], v[156:159], v[52:55]\n"
  "s_waitcnt lgkmcnt(6)\n"
  "v_mfma_f32_16x16x32_bf16 v[56:59], v[72:75], v[152:155], v[56:59]\n"
  "ds_read_b64_tr_b16 v[68:69], v91 offset:49152\n"
  "ds_read_b64_tr_b16 v[70:71], v91 offset:49664\n"
  "buffer_load_dwordx4 v[212:215], v101, s[20:23], s44 offen offset:1024 sc1\n"
  "v_mfma_f32_16x16x32_bf16 v[60:63], v[72:75], v[156:159], v[60:63]\n"
  "s_waitcnt vmcnt(17) lgkmcnt(6)\n"
  "v_mfma_f32_16x16x32_bf16 v[0:3], v[160:163], v[76:79], v[0:3]\n"
  "ds_read_b64_tr_b16 v[72:73], v88 offset:57344\n"
  "ds_read_b64_tr_b16 v[74:75], v88 offset:57856\n"
  "s_setprio 0\n"
  "s_waitcnt vmcnt(16)\n"
  "v_mfma_f32_16x16x32_bf16 v[4:7], v[164:167], v[76:79], v[4:7]\n"
  "s_waitcnt lgkmcnt(6)\n"
  "v_mfma_f32_16x16x32_bf16 v[8:11], v[160:163], v[80:83], v[8:11]\n"
  "ds_read_b64_tr_b16 v[76:77], v89 offset:57344\n"
  "ds_read_b64_tr_b16 v[78:79], v89 offset:57856\n"
  "v_mfma_f32_16x16x32_bf16 v[12:15], v[164:167], v[80:83], v[12:15]\n"
  "s_waitcnt lgkmcnt(6)\n"
  "v_mfma_f32_16x16x32_bf16 v[16:19], v[160:163], v[64:67], v[16:19]\n"
  "ds_read_b64_tr_b16 v[80:81], v90 offset:57344\n"
  "ds_read_b64_tr_b16 v[82:83], v90 offset:57856\n"
  "buffer_load_dwordx4 v[216:219], v101, s[20:23], s44 offen offset:2048 sc1\n"
  "v_mfma_f32_16x16x32_bf16 v[20:23], v[164:167], v[64:67], v[20:23]\n"
  "s_add_u32 m0, s28, 24576\n"
  "s_mov_b32 s43, 2949120\n"
  "buffer_load_dwordx4 v94, s[16:19], s43 offen lds\n"
  "s_waitcnt lgkmcnt(6)\n"
  "v_mfma_f32_16x16x32_bf16 v[24:27], v[160:163], v[68:71], v[24:27]\n"
  "ds_read_b64_tr_b16 v[64:65], v91 offset:57344\n"
  "ds_read_b64_tr_b16 v[66:67], v91 offset:57856\n"
  "v_mfma_f32_16x16x32_bf16 v[28:31], v[164:167], v[68:71], v[28:31]\n"
  "s_waitcnt lgkmcnt(6)\n"
  "v_mfma_f32_16x16x32_bf16 v[32:35], v[72:75], v[160:163], v[32:35]\n"
  "s_barrier\n"
  "ds_read_b64_tr_b16 v[68:69], v88 offset:53248\n"
  "ds_read_b64_tr_b16 v[70:71], v88 offset:53760\n"
  "s_setprio 3\n"
  "v_mfma_f32_16x16x32_bf16 v[36:39], v[72:75], v[164:167], v[36:39]\n"
  "s_waitcnt lgkmcnt(6)\n"
  "v_mfma_f32_16x16x32_bf16 v[40:43], v[76:79], v[160:163], v[40:43]\n"
  "ds_read_b64_tr_b16 v[72:73], v89 offset:53248\n"
  "ds_read_b64_tr_b16 v[74:75], v89 offset:53760\n"
  "buffer_load_dwordx4 v[220:223], v101, s[20:23], s44 offen offset:3072 sc1\n"
  "v_mfma_f32_16x16x32_bf16 v[44:47], v[76:79], v[164:167], v[44:47]\n"
  "s_waitcnt lgkmcnt(6)\n"
  "v_mfma_f32_16x16x32_bf16 v[48:51], v[80:83], v[160:163], v[48:51]\n"
  "ds_read_b64_tr_b16 v[76:77], v90 offset:53248\n"
  "ds_read_b64_tr_b16 v[78:79], v90 offset:53760\n"
  "v_mfma_f32_16x16x32_bf16 v[52:55], v[80:83], v[164:167], v[52:55]\n"
  "s_waitcnt lgkmcnt(6)\n"
  "v_mfma_f32_16x16x32_bf16 v[56:59], v[64:67], v[160:163], v[56:59]\n"
  "ds_read_b64_tr_b16 v[80:81], v91 offset:53248\n"
  "ds_read_b64_tr_b16 v[82:83], v91 offset:53760\n"
  "v_mfma_f32_16x16x32_bf16 v[60:63], v[64:67], v[164:167], v[60:63]\n"
  "s_waitcnt vmcnt(18) lgkmcnt(6)\n"
  "v_mfma_f32_16x16x32_bf16 v[0:3], v[168:171], v[68:71], v[0:3]\n"
  "ds_read_b64_tr_b16 v[64:65], v88 offset:61440\n"
  "ds_read_b64_tr_b16 v[66:67], v88 offset:61952\n"
  "s_add_u32 s44, s29, 28672\n"
  "buffer_load_dwordx4 v[224:227], v101, s[20:23], s44 offen offset:0 sc1\n"
  "s_setprio 2\n"
  "s_waitcnt vmcnt(18)\n"
  "v_mfma_f32_16x16x32_bf16 v[4:7], v[172:175], v[68:71], v[4:7]\n"
  "s_waitcnt lgkmcnt(6)\n"
  "v_mfma_f32_16x16x32_bf16 v[8:11], v[168:171], v[72:75], v[8:11]\n"
  "ds_read_b64_tr_b16 v[68:69], v89 offset:61440\n"
  "ds_read_b64_tr_b16 v[70:71], v89 offset:61952\n"
  "v_mfma_f32_16x16x32_bf16 v[12:15], v[172:175], v[72:75], v[12:15]\n"
  "s_waitcnt lgkmcnt(6)\n"
  "v_mfma_f32_16x16x32_bf16 v[16:19], v[168:171], v[76:79], v[16:19]\n"
  "ds_read_b64_tr_b16 v[72:73], v90 offset:61440\n"
  "ds_read_b64_tr_b16 v[74:75], v90 offset:61952\n"
  "v_mfma_f32_16x16x32_bf16 v[20:23], v[172:175], v[76:79], v[20:23]\n"
  "s_add_u32 m0, s28, 32768\n"
  "s_mov_b32 s43, 3538944\n"
  "buffer_load_dwordx4 v94, s[16:19], s43 offen lds\n"
  "s_waitcnt lgkmcnt(6)\n"
  "v_mfma_f32_16x16x32_bf16 v[24:27], v[168:171], v[80:83], v[24:27]\n"
  "ds_read_b64_tr_b16 v[76:77], v91 offset:61440\n"
  "ds_read_b64_tr_b16 v[78:79], v91 offset:61952\n"
  "buffer_load_dwordx4 v[228:231], v101, s[20:23], s44 offen offset:1024 sc1\n"
  "v_mfma_f32_16x16x32_bf16 v[28:31], v[172:175], v[80:83], v[28:31]\n"
  "s_waitcnt lgkmcnt(6)\n"
  "v_mfma_f32_16x16x32_bf16 v[32:35], v[64:67], v[168:171], v[32:35]\n"
  "v_add_u32 v88, 0x10000, v88\n"
  "ds_read_b64_tr_b16 v[80:81], v88 offset:0\n"
  "ds_read_b64_tr_b16 v[82:83], v88 offset:512\n"
  "s_setprio 1\n"
  "v_mfma_f32_16x16x32_bf16 v[36:39], v[64:67], v[172:175], v[36:39]\n"
  "s_waitcnt lgkmcnt(6)\n"
  "v_mfma_f32_16x16x32_bf16 v[40:43], v[68:71], v[168:171], v[40:43]\n"
  "v_add_u32 v89, 0x10000, v89\n"
  "ds_read_b64_tr_b16 v[64:65], v89 offset:0\n"
  "ds_read_b64_tr_b16 v[66:67], v89 offset:512\n"
  "v_mfma_f32_16x16x32_bf16 v[44:47], v[68:71], v[172:175], v[44:47]\n"
  "s_waitcnt lgkmcnt(6)\n"
  "v_mfma_f32_16x16x32_bf16 v[48:51], v[72:75], v[168:171], v[48:51]\n"
  "v_add_u32 v90, 0x10000, v90\n"
  "ds_read_b64_tr_b16 v[68:69], v90 offset:0\n"
  "ds_read_b64_tr_b16 v[70:71], v90 offset:512\n"
  "buffer_load_dwordx4 v[232:235], v101, s[20:23], s44 offen offset:2048 sc1\n"
  "v_mfma_f32_16x16x32_bf16 v[52:55], v[72:75], v[172:175], v[52:55]\n"
  "s_waitcnt lgkmcnt(6)\n"
  "v_mfma_f32_16x16x32_bf16 v[56:59], v[76:79], v[168:171], v[56:59]\n"
  "v_add_u32 v91, 0x10000, v91\n"
  "ds_read_b64_tr_b16 v[72:73], v91 offset:0\n"
  "ds_read_b64_tr_b16 v[74:75], v91 offset:512\n"
  "v_mfma_f32_16x16x32_bf16 v[60:63], v[76:79], v[172:175], v[60:63]\n"
  "s_waitcnt vmcnt(19) lgkmcnt(6)\n"
  "v_mfma_f32_16x16x32_bf16 v[0:3], v[176:179], v[80:83], v[0:3]\n"
  "ds_read_b64_tr_b16 v[76:77], v88 offset:8192\n"
  "ds_read_b64_tr_b16 v[78:79], v88 offset:8704\n"
  "s_setprio 0\n"
  "s_waitcnt vmcnt(18)\n"
  "v_mfma_f32_16x16x32_bf16 v[4:7], v[180:183], v[80:83], v[4:7]\n"
  "s_waitcnt lgkmcnt(6)\n"
  "v_mfma_f32_16x16x32_bf16 v[8:11], v[176:179], v[64:67], v[8:11]\n"
  "ds_read_b64_tr_b16 v[80:81], v89 offset:8192\n"
  "ds_read_b64_tr_b16 v[82:83], v89 offset:8704\n"
  "buffer_load_dwordx4 v[236:239], v101, s[20:23], s44 offen offset:3072 sc1\n"
  "v_mfma_f32_16x16x32_bf16 v[12:15], v[180:183], v[64:67], v[12:15]\n"
  "s_waitcnt lgkmcnt(6)\n"
  "v_mfma_f32_16x16x32_bf16 v[16:19], v[176:179], v[68:71], v[16:19]\n"
  "ds_read_b64_tr_b16 v[64:65], v90 offset:8192\n"
  "ds_read_b64_tr_b16 v[66:67], v90 offset:8704\n"
  "v_mfma_f32_16x16x32_bf16 v[20:23], v[180:183], v[68:71], v[20:23]\n"
  "s_add_u32 m0, s28, 40960\n"
  "s_mov_b32 s43, 4128768\n"
  "buffer_load_dwordx4 v94, s[16:19], s43 offen lds\n"
  "s_waitcnt lgkmcnt(6)\n"
  "v_mfma_f32_16x16x32_bf16 v[24:27], v[176:179], v[72:75], v[24:27]\n"
  "ds_read_b64_tr_b16 v[68:69], v91 offset:8192\n"
  "ds_read_b64_tr_b16 v[70:71], v91 offset:8704\n"
  "v_mfma_f32_16x16x32_bf16 v[28:31], v[180:183], v[72:75], v[28:31]\n"
  "s_waitcnt lgkmcnt(6)\n"
  "v_mfma_f32_16x16x32_bf16 v[32:35], v[76:79], v[176:179], v[32:35]\n"
  "s_barrier\n"
  "ds_read_b64_tr_b16 v[72:73], v88 offset:4096\n"
  "ds_read_b64_tr_b16 v[74:75], v88 offset:4608\n"
  "s_add_u32 s44, s29, 32768\n"
  "buffer_load_dwordx4 v[240:243], v101, s[20:23], s44 offen offset:0 sc1\n"
  "s_setprio 3\n"
  "v_mfma_f32_16x16x32_bf16 v[36:39], v[76:79], v[180:183], v[36:39]\n"
  "s_waitcnt lgkmcnt(6)\n"
  "v_mfma_f32_16x16x32_bf16 v[40:43], v[80:83], v[176:179], v[40:43]\n"
  "ds_read_b64_tr_b16 v[76:77], v89 offset:4096\n"
  "ds_read_b64_tr_b16 v[78:79], v89 offset:4608\n"
  "v_mfma_f32_16x16x32_bf16 v[44:47], v[80:83], v[180:183], v[44:47]\n"
  "s_waitcnt lgkmcnt(6)\n"
  "v_mfma_f32_16x16x32_bf16 v[48:51], v[64:67], v[176:179], v[48:51]\n"
  "ds_read_b64_tr_b16 v[80:81], v90 offset:4096\n"
  "ds_read_b64_tr_b16 v[82:83], v90 offset:4608\n"
  "v_mfma_f32_16x16x32_bf16 v[52:55], v[64:67], v[180:183], v[52:55]\n"
  "s_waitcnt lgkmcnt(6)\n"
  "v_mfma_f32_16x16x32_bf16 v[56:59], v[68:71], v[176:179], v[56:59]\n"
  "ds_read_b64_tr_b16 v[64:65], v91 offset:4096\n"
  "ds_read_b64_tr_b16 v[66:67], v91 offset:4608\n"
  "buffer_load_dwordx4 v[244:247], v101, s[20:23], s44 offen offset:1024 sc1\n"
  "v_mfma_f32_16x16x32_bf16 v[60:63], v[68:71], v[180:183], v[60:63]\n"
  "s_waitcnt vmcnt(21) lgkmcnt(6)\n"
  "v_mfma_f32_16x16x32_bf16 v[0:3], v[184:187], v[72:75], v[0:3]\n"
  "ds_read_b64_tr_b16 v[68:69], v88 offset:12288\n"
  "ds_read_b64_tr_b16 v[70:71], v88 offset:12800\n"
  "s_setprio 2\n"
  "s_waitcnt vmcnt(19)\n"
  "v_mfma_f32_16x16x32_bf16 v[4:7], v[188:191], v[72:75], v[4:7]\n"
  "s_waitcnt lgkmcnt(6)\n"
  "v_mfma_f32_16x16x32_bf16 v[8:11], v[184:187], v[76:79], v[8:11]\n"
  "ds_read_b64_tr_b16 v[72:73], v89 offset:12288\n"
  "ds_read_b64_tr_b16 v[74:75], v89 offset:12800\n"
  "v_mfma_f32_16x16x32_bf16 v[12:15], v[188:191], v[76:79], v[12:15]\n"
  "s_waitcnt lgkmcnt(6)\n"
  "v_mfma_f32_16x16x32_bf16 v[16:19], v[184:187], v[80:83], v[16:19]\n"
  "ds_read_b64_tr_b16 v[76:77], v90 offset:12288\n"
  "ds_read_b64_tr_b16 v[78:79], v90 offset:12800\n"
  "buffer_load_dwordx4 v[248:251], v101, s[20:23], s44 offen offset:2048 sc1\n"
  "v_mfma_f32_16x16x32_bf16 v[20:23], v[188:191], v[80:83], v[20:23]\n"
  "s_add_u32 m0, s28, 49152\n"
  "s_mov_b32 s43, 4718592\n"
  "buffer_load_dwordx4 v94, s[16:19], s43 offen lds\n"
  "s_waitcnt lgkmcnt(6)\n"
  "v_mfma_f32_16x16x32_bf16 v[24:27], v[184:187], v[64:67], v[24:27]\n"
  "ds_read_b64_tr_b16 v[80:81], v91 offset:12288\n"
  "ds_read_b64_tr_b16 v[82:83], v91 offset:12800\n"
  "v_mfma_f32_16x16x32_bf16 v[28:31], v[188:191], v[64:67], v[28:31]\n"
  "s_waitcnt lgkmcnt(6)\n"
  "v_mfma_f32_16x16x32_bf16 v[32:35], v[68:71], v[184:187], v[32:35]\n"
  "ds_read_b64_tr_b16 v[64:65], v88 offset:16384\n"
  "ds_read_b64_tr_b16 v[66:67], v88 offset:16896\n"
  "s_setprio 1\n"
  "v_mfma_f32_16x16x32_bf16 v[36:39], v[68:71], v[188:191], v[36:39]\n"
  "s_waitcnt lgkmcnt(6)\n"
  "v_mfma_f32_16x16x32_bf16 v[40:43], v[72:75], v[184:187], v[40:43]\n"
  "ds_read_b64_tr_b16 v[68:69], v89 offset:16384\n"
  "ds_read_b64_tr_b16 v[70:71], v89 offset:16896\n"
  "buffer_load_dwordx4 v[252:255], v101, s[20:23], s44 offen offset:3072 sc1\n"
  "v_mfma_f32_16x16x32_bf16 v[44:47], v[72:75], v[188:191], v[44:47]\n"
  "s_waitcnt lgkmcnt(6)\n"
  "v_mfma_f32_16x16x32_bf16 v[48:51], v[76:79], v[184:187], v[48:51]\n"
  "ds_read_b64_tr_b16 v[72:73], v90 offset:16384\n"
  "ds_read_b64_tr_b16 v[74:75], v90 offset:16896\n"
  "v_mfma_f32_16x16x32_bf16 v[52:55], v[76:79], v[188:191], v[52:55]\n"
  "s_waitcnt lgkmcnt(6)\n"
  "v_mfma_f32_16x16x32_bf16 v[56:59], v[80:83], v[184:187], v[56:59]\n"
  "ds_read_b64_tr_b16 v[76:77], v91 offset:16384\n"
  "ds_read_b64_tr_b16 v[78:79], v91 offset:16896\n"
  "v_mfma_f32_16x16x32_bf16 v[60:63], v[80:83], v[188:191], v[60:63]\n"
  "s_waitcnt vmcnt(21) lgkmcnt(6)\n"
  "v_mfma_f32_16x16x32_bf16 v[0:3], v[192:195], v[64:67], v[0:3]\n"
  "ds_read_b64_tr_b16 v[80:81], v88 offset:24576\n"
  "ds_read_b64_tr_b16 v[82:83], v88 offset:25088\n"
  "s_setprio 0\n"
  "s_waitcnt vmcnt(19)\n"
  "v_mfma_f32_16x16x32_bf16 v[4:7], v[196:199], v[64:67], v[4:7]\n"
  "s_waitcnt lgkmcnt(6)\n"
  "v_mfma_f32_16x16x32_bf16 v[8:11], v[192:195], v[68:71], v[8:11]\n"
  "ds_read_b64_tr_b16 v[64:65], v89 offset:24576\n"
  "ds_read_b64_tr_b16 v[66:67], v89 offset:25088\n"
  "v_mfma_f32_16x16x32_bf16 v[12:15], v[196:199], v[68:71], v[12:15]\n"
  "s_waitcnt lgkmcnt(6)\n"
  "v_mfma_f32_16x16x32_bf16 v[16:19], v[192:195], v[72:75], v[16:19]\n"
  "ds_read_b64_tr_b16 v[68:69], v90 offset:24576\n"
  "ds_read_b64_tr_b16 v[70:71], v90 offset:25088\n"
  "v_mfma_f32_16x16x32_bf16 v[20:23], v[196:199], v[72:75], v[20:23]\n"
  "s_add_u32 m0, s28, 57344\n"
  "s_mov_b32 s43, 128\n"
  "buffer_load_dwordx4 v94, s[12:15], s43 offen lds\n"
  "s_waitcnt lgkmcnt(6)\n"
  "v_mfma_f32_16x16x32_bf16 v[24:27], v[192:195], v[76:79], v[24:27]\n"
  "ds_read_b64_tr_b16 v[72:73], v91 offset:24576\n"
  "ds_read_b64_tr_b16 v[74:75], v91 offset:25088\n"
  "v_mfma_f32_16x16x32_bf16 v[28:31], v[196:199], v[76:79], v[28:31]\n"
  "s_waitcnt lgkmcnt(6)\n"
  "v_mfma_f32_16x16x32_bf16 v[32:35], v[80:83], v[192:195], v[32:35]\n"
  "s_barrier\n"
  "ds_read_b64_tr_b16 v[76:77], v88 offset:20480\n"
  "ds_read_b64_tr_b16 v[78:79], v88 offset:20992\n"
  "s_setprio 3\n"
  "v_mfma_f32_16x16x32_bf16 v[36:39], v[80:83], v[196:199], v[36:39]\n"
  "s_waitcnt lgkmcnt(6)\n"
  "v_mfma_f32_16x16x32_bf16 v[40:43], v[64:67], v[192:195], v[40:43]\n"
  "ds_read_b64_tr_b16 v[80:81], v89 offset:20480\n"
  "ds_read_b64_tr_b16 v[82:83], v89 offset:20992\n"
  "v_mfma_f32_16x16x32_bf16 v[44:47], v[64:67], v[196:199], v[44:47]\n"
  "s_waitcnt lgkmcnt(6)\n"
  "v_mfma_f32_16x16x32_bf16 v[48:51], v[68:71], v[192:195], v[48:51]\n"
  "ds_read_b64_tr_b16 v[64:65], v90 offset:20480\n"
  "ds_read_b64_tr_b16 v[66:67], v90 offset:20992\n"
  "v_mfma_f32_16x16x32_bf16 v[52:55], v[68:71], v[196:199], v[52:55]\n"
  "s_waitcnt lgkmcnt(6)\n"
  "v_mfma_f32_16x16x32_bf16 v[56:59], v[72:75], v[192:195], v[56:59]\n"
  "ds_read_b64_tr_b16 v[68:69], v91 offset:20480\n"
  "ds_read_b64_tr_b16 v[70:71], v91 offset:20992\n"
  "v_mfma_f32_16x16x32_bf16 v[60:63], v[72:75], v[196:199], v[60:63]\n"
  "s_add_u32 m0, s28, 65536\n"
  "s_mov_b32 s43, 589952\n"
  "buffer_load_dwordx4 v94, s[12:15], s43 offen lds\n"
  "s_waitcnt vmcnt(20) lgkmcnt(6)\n"
  "v_mfma_f32_16x16x32_bf16 v[0:3], v[200:203], v[76:79], v[0:3]\n"
  "ds_read_b64_tr_b16 v[72:73], v88 offset:28672\n"
  "ds_read_b64_tr_b16 v[74:75], v88 offset:29184\n"
  "s_setprio 2\n"
  "s_waitcnt vmcnt(19)\n"
  "v_mfma_f32_16x16x32_bf16 v[4:7], v[204:207], v[76:79], v[4:7]\n"
  "s_waitcnt lgkmcnt(6)\n"
  "v_mfma_f32_16x16x32_bf16 v[8:11], v[200:203], v[80:83], v[8:11]\n"
  "ds_read_b64_tr_b16 v[76:77], v89 offset:28672\n"
  "ds_read_b64_tr_b16 v[78:79], v89 offset:29184\n"
  "v_mfma_f32_16x16x32_bf16 v[12:15], v[204:207], v[80:83], v[12:15]\n"
  "s_waitcnt lgkmcnt(6)\n"
  "v_mfma_f32_16x16x32_bf16 v[16:19], v[200:203], v[64:67], v[16:19]\n"
  "ds_read_b64_tr_b16 v[80:81], v90 offset:28672\n"
  "ds_read_b64_tr_b16 v[82:83], v90 offset:29184\n"
  "v_mfma_f32_16x16x32_bf16 v[20:23], v[204:207], v[64:67], v[20:23]\n"
  "s_waitcnt lgkmcnt(6)\n"
  "v_mfma_f32_16x16x32_bf16 v[24:27], v[200:203], v[68:71], v[24:27]\n"
  "ds_read_b64_tr_b16 v[64:65], v91 offset:28672\n"
  "ds_read_b64_tr_b16 v[66:67], v91 offset:29184\n"
  "v_mfma_f32_16x16x32_bf16 v[28:31], v[204:207], v[68:71], v[28:31]\n"
  "s_waitcnt lgkmcnt(6)\n"
  "v_mfma_f32_16x16x32_bf16 v[32:35], v[72:75], v[200:203], v[32:35]\n"
  "ds_read_b64_tr_b16 v[68:69], v88 offset:32768\n"
  "ds_read_b64_tr_b16 v[70:71], v88 offset:33280\n"
  "s_setprio 1\n"
  "v_mfma_f32_16x16x32_bf16 v[36:39], v[72:75], v[204:207], v[36:39]\n"
  "s_add_u32 m0, s28, 73728\n"
  "s_mov_b32 s43, 1179776\n"
  "buffer_load_dwordx4 v94, s[12:15], s43 offen lds\n"
  "s_waitcnt lgkmcnt(6)\n"
  "v_mfma_f32_16x16x32_bf16 v[40:43], v[76:79], v[200:203], v[40:43]\n"
  "ds_read_b64_tr_b16 v[72:73], v89 offset:32768\n"
  "ds_read_b64_tr_b16 v[74:75], v89 offset:33280\n"
  "v_mfma_f32_16x16x32_bf16 v[44:47], v[76:79], v[204:207], v[44:47]\n"
  "s_waitcnt lgkmcnt(6)\n"
  "v_mfma_f32_16x16x32_bf16 v[48:51], v[80:83], v[200:203], v[48:51]\n"
  "ds_read_b64_tr_b16 v[76:77], v90 offset:32768\n"
  "ds_read_b64_tr_b16 v[78:79], v90 offset:33280\n"
  "v_mfma_f32_16x16x32_bf16 v[52:55], v[80:83], v[204:207], v[52:55]\n"
  "s_waitcnt lgkmcnt(6)\n"
  "v_mfma_f32_16x16x32_bf16 v[56:59], v[64:67], v[200:203], v[56:59]\n"
  "ds_read_b64_tr_b16 v[80:81], v91 offset:32768\n"
  "ds_read_b64_tr_b16 v[82:83], v91 offset:33280\n"
  "v_mfma_f32_16x16x32_bf16 v[60:63], v[64:67], v[204:207], v[60:63]\n"
  "s_waitcnt vmcnt(18) lgkmcnt(6)\n"
  "v_mfma_f32_16x16x32_bf16 v[0:3], v[208:211], v[68:71], v[0:3]\n"
  "ds_read_b64_tr_b16 v[64:65], v88 offset:40960\n"
  "ds_read_b64_tr_b16 v[66:67], v88 offset:41472\n"
  "s_setprio 0\n"
  "s_waitcnt vmcnt(17)\n"
  "v_mfma_f32_16x16x32_bf16 v[4:7], v[212:215], v[68:71], v[4:7]\n"
  "s_waitcnt lgkmcnt(6)\n"
  "v_mfma_f32_16x16x32_bf16 v[8:11], v[208:211], v[72:75], v[8:11]\n"
  "ds_read_b64_tr_b16 v[68:69], v89 offset:40960\n"
  "ds_read_b64_tr_b16 v[70:71], v89 offset:41472\n"
  "v_mfma_f32_16x16x32_bf16 v[12:15], v[212:215], v[72:75], v[12:15]\n"
  "s_waitcnt lgkmcnt(6)\n"
  "v_mfma_f32_16x16x32_bf16 v[16:19], v[208:211], v[76:79], v[16:19]\n"
  "ds_read_b64_tr_b16 v[72:73], v90 offset:40960\n"
  "ds_read_b64_tr_b16 v[74:75], v90 offset:41472\n"
  "buffer_load_dword v87, v84, s[36:39], 0 offen\n"
  "v_mfma_f32_16x16x32_bf16 v[20:23], v[212:215], v[76:79], v[20:23]\n"
  "s_waitcnt lgkmcnt(6)\n"
  "v_mfma_f32_16x16x32_bf16 v[24:27], v[208:211], v[80:83], v[24:27]\n"
  "ds_read_b64_tr_b16 v[76:77], v91 offset:40960\n"
  "ds_read_b64_tr_b16 v[78:79], v91 offset:41472\n"
  "v_mfma_f32_16x16x32_bf16 v[28:31], v[212:215], v[80:83], v[28:31]\n"
  "s_waitcnt lgkmcnt(6)\n"
  "v_mfma_f32_16x16x32_bf16 v[32:35], v[64:67], v[208:211], v[32:35]\n"
  "s_barrier\n"
  "ds_read_b64_tr_b16 v[80:81], v88 offset:36864\n"
  "ds_read_b64_tr_b16 v[82:83], v88 offset:37376\n"
  "s_setprio 3\n"
  "v_mfma_f32_16x16x32_bf16 v[36:39], v[64:67], v[212:215], v[36:39]\n"
  "s_add_u32 m0, s28, 81920\n"
  "s_mov_b32 s43, 1769600\n"
  "buffer_load_dwordx4 v94, s[12:15], s43 offen lds\n"
  "s_waitcnt lgkmcnt(6)\n"
  "v_mfma_f32_16x16x32_bf16 v[40:43], v[68:71], v[208:211], v[40:43]\n"
  "ds_read_b64_tr_b16 v[64:65], v89 offset:36864\n"
  "ds_read_b64_tr_b16 v[66:67], v89 offset:37376\n"
  "v_mfma_f32_16x16x32_bf16 v[44:47], v[68:71], v[212:215], v[44:47]\n"
  "s_waitcnt lgkmcnt(6)\n"
  "v_mfma_f32_16x16x32_bf16 v[48:51], v[72:75], v[208:211], v[48:51]\n"
  "ds_read_b64_tr_b16 v[68:69], v90 offset:36864\n"
  "ds_read_b64_tr_b16 v[70:71], v90 offset:37376\n"
  "v_mfma_f32_16x16x32_bf16 v[52:55], v[72:75], v[212:215], v[52:55]\n"
  "s_waitcnt lgkmcnt(6)\n"
  "v_mfma_f32_16x16x32_bf16 v[56:59], v[76:79], v[208:211], v[56:59]\n"
  "ds_read_b64_tr_b16 v[72:73], v91 offset:36864\n"
  "ds_read_b64_tr_b16 v[74:75], v91 offset:37376\n"
  "v_mfma_f32_16x16x32_bf16 v[60:63], v[76:79], v[212:215], v[60:63]\n"
  "s_waitcnt vmcnt(18) lgkmcnt(6)\n"
  "v_mfma_f32_16x16x32_bf16 v[0:3], v[216:219], v[80:83], v[0:3]\n"
  "ds_read_b64_tr_b16 v[76:77], v88 offset:45056\n"
  "ds_read_b64_tr_b16 v[78:79], v88 offset:45568\n"
  "v_lshrrev_b32 v93, 16, v111\n"
  "s_setprio 2\n"
  "s_waitcnt vmcnt(16)\n"
  "v_mfma_f32_16x16x32_bf16 v[4:7], v[220:223], v[80:83], v[4:7]\n"
  "s_waitcnt lgkmcnt(6)\n"
  "v_mfma_f32_16x16x32_bf16 v[8:11], v[216:219], v[64:67], v[8:11]\n"
  "ds_read_b64_tr_b16 v[80:81], v89 offset:45056\n"
  "ds_read_b64_tr_b16 v[82:83], v89 offset:45568\n"
  "v_mfma_f32_16x16x32_bf16 v[12:15], v[220:223], v[64:67], v[12:15]\n"
  "s_add_u32 m0, s28, 90112\n"
  "s_mov_b32 s43, 2359424\n"
  "buffer_load_dwordx4 v94, s[12:15], s43 offen lds\n"
  "s_waitcnt lgkmcnt(6)\n"
  "v_mfma_f32_16x16x32_bf16 v[16:19], v[216:219], v[68:71], v[16:19]\n"
  "ds_read_b64_tr_b16 v[64:65], v90 offset:45056\n"
  "ds_read_b64_tr_b16 v[66:67], v90 offset:45568\n"
  "v_mfma_f32_16x16x32_bf16 v[20:23], v[220:223], v[68:71], v[20:23]\n"
  "s_waitcnt lgkmcnt(6)\n"
  "v_mfma_f32_16x16x32_bf16 v[24:27], v[216:219], v[72:75], v[24:27]\n"
  "ds_read_b64_tr_b16 v[68:69], v91 offset:45056\n"
  "ds_read_b64_tr_b16 v[70:71], v91 offset:45568\n"
  "v_mfma_f32_16x16x32_bf16 v[28:31], v[220:223], v[72:75], v[28:31]\n"
  "s_waitcnt lgkmcnt(6)\n"
  "v_mfma_f32_16x16x32_bf16 v[32:35], v[76:79], v[216:219], v[32:35]\n"
  "ds_read_b64_tr_b16 v[72:73], v88 offset:49152\n"
  "ds_read_b64_tr_b16 v[74:75], v88 offset:49664\n"
  "buffer_load_dword v87, v84, s[36:39], s35 offen\n"
  "s_setprio 1\n"
  "v_mfma_f32_16x16x32_bf16 v[36:39], v[76:79], v[220:223], v[36:39]\n"
  "s_waitcnt lgkmcnt(6)\n"
  "v_mfma_f32_16x16x32_bf16 v[40:43], v[80:83], v[216:219], v[40:43]\n"
  "ds_read_b64_tr_b16 v[76:77], v89 offset:49152\n"
  "ds_read_b64_tr_b16 v[78:79], v89 offset:49664\n"
  "v_mfma_f32_16x16x32_bf16 v[44:47], v[80:83], v[220:223], v[44:47]\n"
  "s_waitcnt lgkmcnt(6)\n"
  "v_mfma_f32_16x16x32_bf16 v[48:51], v[64:67], v[216:219], v[48:51]\n"
  "ds_read_b64_tr_b16 v[80:81], v90 offset:49152\n"
  "ds_read_b64_tr_b16 v[82:83], v90 offset:49664\n"
  "v_mfma_f32_16x16x32_bf16 v[52:55], v[64:67], v[220:223], v[52:55]\n"
  "s_waitcnt lgkmcnt(6)\n"
  "v_mfma_f32_16x16x32_bf16 v[56:59], v[68:71], v[216:219], v[56:59]\n"
  "ds_read_b64_tr_b16 v[64:65], v91 offset:49152\n"
  "ds_read_b64_tr_b16 v[66:67], v91 offset:49664\n"
  "v_mfma_f32_16x16x32_bf16 v[60:63], v[68:71], v[220:223], v[60:63]\n"
  "s_waitcnt vmcnt(17) lgkmcnt(6)\n"
  "v_mfma_f32_16x16x32_bf16 v[0:3], v[224:227], v[72:75], v[0:3]\n"
  "ds_read_b64_tr_b16 v[68:69], v88 offset:53248\n"
  "ds_read_b64_tr_b16 v[70:71], v88 offset:53760\n"
  "s_setprio 0\n"
  "s_waitcnt vmcnt(15)\n"
  "v_mfma_f32_16x16x32_bf16 v[4:7], v[228:231], v[72:75], v[4:7]\n"
  "s_waitcnt lgkmcnt(6)\n"
  "v_mfma_f32_16x16x32_bf16 v[8:11], v[224:227], v[76:79], v[8:11]\n"
  "ds_read_b64_tr_b16 v[72:73], v89 offset:53248\n"
  "ds_read_b64_tr_b16 v[74:75], v89 offset:53760\n"
  "v_mfma_f32_16x16x32_bf16 v[12:15], v[228:231], v[76:79], v[12:15]\n"
  "s_waitcnt lgkmcnt(6)\n"
  "v_mfma_f32_16x16x32_bf16 v[16:19], v[224:227], v[80:83], v[16:19]\n"
  "ds_read_b64_tr_b16 v[76:77], v90 offset:53248\n"
  "ds_read_b64_tr_b16 v[78:79], v90 offset:53760\n"
  "s_add_u32 s43, s28, 131072\n"
  "v_add_u32 v86, s43, v100\n"
  "v_mfma_f32_16x16x32_bf16 v[20:23], v[228:231], v[80:83], v[20:23]\n"
  "s_waitcnt lgkmcnt(6)\n"
  "v_mfma_f32_16x16x32_bf16 v[24:27], v[224:227], v[64:67], v[24:27]\n"
  "ds_read_b64_tr_b16 v[80:81], v91 offset:53248\n"
  "ds_read_b64_tr_b16 v[82:83], v91 offset:53760\n"
  "v_mfma_f32_16x16x32_bf16 v[28:31], v[228:231], v[64:67], v[28:31]\n"
  "s_waitcnt vmcnt(14) lgkmcnt(6)\n"
  "v_mfma_f32_16x16x32_bf16 v[0:3], v[232:235], v[68:71], v[0:3]\n"
  "s_barrier\n"
  "v_add_u32 v88, 0x10000, v88\n"
  "ds_read_b64_tr_b16 v[64:65], v88 offset:0\n"
  "ds_read_b64_tr_b16 v[66:67], v88 offset:512\n"
  "s_setprio 3\n"
  "s_waitcnt vmcnt(13)\n"
  "v_mfma_f32_16x16x32_bf16 v[4:7], v[236:239], v[68:71], v[4:7]\n"
  "s_add_u32 m0, s28, 98304\n"
  "s_mov_b32 s43, 2949248\n"
  "buffer_load_dwordx4 v94, s[12:15], s43 offen lds\n"
  "s_waitcnt lgkmcnt(6)\n"
  "v_mfma_f32_16x16x32_bf16 v[8:11], v[232:235], v[72:75], v[8:11]\n"
  "v_add_u32 v89, 0x10000, v89\n"
  "ds_read_b64_tr_b16 v[68:69], v89 offset:0\n"
  "ds_read_b64_tr_b16 v[70:71], v89 offset:512\n"
  "v_mfma_f32_16x16x32_bf16 v[12:15], v[236:239], v[72:75], v[12:15]\n"
  "s_waitcnt lgkmcnt(6)\n"
  "v_mfma_f32_16x16x32_bf16 v[16:19], v[232:235], v[76:79], v[16:19]\n"
  "v_add_u32 v90, 0x10000, v90\n"
  "ds_read_b64_tr_b16 v[72:73], v90 offset:0\n"
  "ds_read_b64_tr_b16 v[74:75], v90 offset:512\n"
  "v_mfma_f32_16x16x32_bf16 v[20:23], v[236:239], v[76:79], v[20:23]\n"
  "s_waitcnt lgkmcnt(6)\n"
  "v_mfma_f32_16x16x32_bf16 v[24:27], v[232:235], v[80:83], v[24:27]\n"
  "v_add_u32 v91, 0x10000, v91\n"
  "ds_read_b64_tr_b16 v[76:77], v91 offset:0\n"
  "ds_read_b64_tr_b16 v[78:79], v91 offset:512\n"
  "v_mfma_f32_16x16x32_bf16 v[28:31], v[236:239], v[80:83], v[28:31]\n"
  "s_waitcnt vmcnt(12) lgkmcnt(6)\n"
  "v_mfma_f32_16x16x32_bf16 v[0:3], v[240:243], v[64:67], v[0:3]\n"
  "ds_read_b64_tr_b16 v[80:81], v88 offset:4096\n"
  "ds_read_b64_tr_b16 v[82:83], v88 offset:4608\n"
  "s_waitcnt vmcnt(11)\n"
  "v_mfma_f32_16x16x32_bf16 v[4:7], v[244:247], v[64:67], v[4:7]\n"
  "s_waitcnt lgkmcnt(6)\n"
  "v_mfma_f32_16x16x32_bf16 v[8:11], v[240:243], v[68:71], v[8:11]\n"
  "ds_read_b64_tr_b16 v[64:65], v89 offset:4096\n"
  "ds_read_b64_tr_b16 v[66:67], v89 offset:4608\n"
  "v_mfma_f32_16x16x32_bf16 v[12:15], v[244:247], v[68:71], v[12:15]\n"
  "s_add_u32 m0, s28, 106496\n"
  "s_mov_b32 s43, 3539072\n"
  "buffer_load_dwordx4 v94, s[12:15], s43 offen lds\n"
  "s_waitcnt lgkmcnt(6)\n"
  "v_mfma_f32_16x16x32_bf16 v[16:19], v[240:243], v[72:75], v[16:19]\n"
  "ds_read_b64_tr_b16 v[68:69], v90 offset:4096\n"
  "ds_read_b64_tr_b16 v[70:71], v90 offset:4608\n"
  "s_setprio 2\n"
  "v_mfma_f32_16x16x32_bf16 v[20:23], v[244:247], v[72:75], v[20:23]\n"
  "s_waitcnt lgkmcnt(6)\n"
  "v_mfma_f32_16x16x32_bf16 v[24:27], v[240:243], v[76:79], v[24:27]\n"
  "ds_read_b64_tr_b16 v[72:73], v91 offset:4096\n"
  "ds_read_b64_tr_b16 v[74:75], v91 offset:4608\n"
  "v_mfma_f32_16x16x32_bf16 v[28:31], v[244:247], v[76:79], v[28:31]\n"
  "s_waitcnt vmcnt(11) lgkmcnt(6)\n"
  "v_mfma_f32_16x16x32_bf16 v[0:3], v[248:251], v[80:83], v[0:3]\n"
  "v_add_u32 v88, 0xffffe000, v88\n"
  "ds_read_b64_tr_b16 v[76:77], v88 offset:0\n"
  "ds_read_b64_tr_b16 v[78:79], v88 offset:512\n"
  "s_waitcnt vmcnt(9)\n"
  "v_mfma_f32_16x16x32_bf16 v[4:7], v[252:255], v[80:83], v[4:7]\n"
  "s_waitcnt lgkmcnt(6)\n"
  "v_mfma_f32_16x16x32_bf16 v[8:11], v[248:251], v[64:67], v[8:11]\n"
  "v_add_u32 v89, 0xffffe000, v89\n"
  "ds_read_b64_tr_b16 v[80:81], v89 offset:0\n"
  "ds_read_b64_tr_b16 v[82:83], v89 offset:512\n"
  "v_mfma_f32_16x16x32_bf16 v[12:15], v[252:255], v[64:67], v[12:15]\n"
  "s_waitcnt lgkmcnt(6)\n"
  "v_mfma_f32_16x16x32_bf16 v[16:19], v[248:251], v[68:71], v[16:19]\n"
  "v_add_u32 v90, 0xffffe000, v90\n"
  "ds_read_b64_tr_b16 v[64:65], v90 offset:0\n"
  "ds_read_b64_tr_b16 v[66:67], v90 offset:512\n"
  "v_mfma_f32_16x16x32_bf16 v[20:23], v[252:255], v[68:71], v[20:23]\n"
  "s_waitcnt lgkmcnt(6)\n"
  "v_mfma_f32_16x16x32_bf16 v[24:27], v[248:251], v[72:75], v[24:27]\n"
  "v_add_u32 v91, 0xffffe000, v91\n"
  "ds_read_b64_tr_b16 v[68:69], v91 offset:0\n"
  "ds_read_b64_tr_b16 v[70:71], v91 offset:512\n"
  "v_mfma_f32_16x16x32_bf16 v[28:31], v[252:255], v[72:75], v[28:31]\n"
  "s_waitcnt lgkmcnt(6)\n"
  "v_mfma_f32_16x16x32_bf16 v[32:35], v[76:79], v[224:227], v[32:35]\n"
  "ds_read_b64_tr_b16 v[72:73], v88 offset:4096\n"
  "ds_read_b64_tr_b16 v[74:75], v88 offset:4608\n"
  "LJOIN_%=:\n"
  "s_setprio 1\n"
  "v_mfma_f32_16x16x32_bf16 v[36:39], v[76:79], v[228:231], v[36:39]\n"
  "s_waitcnt lgkmcnt(6)\n"
  "v_mfma_f32_16x16x32_bf16 v[40:43], v[80:83], v[224:227], v[40:43]\n"
  "ds_read_b64_tr_b16 v[76:77], v89 offset:4096\n"
  "ds_read_b64_tr_b16 v[78:79], v89 offset:4608\n"
  "v_mul_f32 v16, s40, v16\n"
  "v_mul_f32 v17, s40, v17\n"
  "v_mfma_f32_16x16x32_bf16 v[44:47], v[80:83], v[228:231], v[44:47]\n"
  "v_mul_f32 v18, s40, v18\n"
  "v_mul_f32 v19, s40, v19\n"
  "s_waitcnt lgkmcnt(6)\n"
  "v_mfma_f32_16x16x32_bf16 v[48:51], v[64:67], v[224:227], v[48:51]\n"
  "ds_read_b64_tr_b16 v[80:81], v90 offset:4096\n"
  "ds_read_b64_tr_b16 v[82:83], v90 offset:4608\n"
  "v_mul_f32 v20, s40, v20\n"
  "v_mfma_f32_16x16x32_bf16 v[52:55], v[64:67], v[228:231], v[52:55]\n"
  "v_mul_f32 v21, s40, v21\n"
  "v_mul_f32 v22, s40, v22\n"
  "s_waitcnt lgkmcnt(6)\n"
  "v_mfma_f32_16x16x32_bf16 v[56:59], v[68:71], v[224:227], v[56:59]\n"
  "ds_read_b64_tr_b16 v[64:65], v91 offset:4096\n"
  "ds_read_b64_tr_b16 v[66:67], v91 offset:4608\n"
  "v_mul_f32 v23, s40, v23\n"
  "v_cvt_pk_bf16_f32 v16, v16, v17\n"
  "v_mfma_f32_16x16x32_bf16 v[60:63], v[68:71], v[228:231], v[60:63]\n"
  "v_cvt_pk_bf16_f32 v17, v18, v19\n"
  "s_waitcnt lgkmcnt(6)\n"
  "v_mfma_f32_16x16x32_bf16 v[32:35], v[72:75], v[232:235], v[32:35]\n"
  "ds_read_b64_tr_b16 v[68:69], v88 offset:16384\n"
  "ds_read_b64_tr_b16 v[70:71], v88 offset:16896\n"
  "v_cvt_pk_bf16_f32 v18, v20, v21\n"
  "v_cvt_pk_bf16_f32 v19, v22, v23\n"
  "v_mfma_f32_16x16x32_bf16 v[36:39], v[72:75], v[236:239], v[36:39]\n"
  "v_mul_f32 v0, s40, v0\n"
  "v_mul_f32 v1, s40, v1\n"
  "s_waitcnt lgkmcnt(6)\n"
  "v_mfma_f32_16x16x32_bf16 v[40:43], v[76:79], v[232:235], v[40:43]\n"
  "ds_read_b64_tr_b16 v[72:73], v89 offset:16384\n"
  "ds_read_b64_tr_b16 v[74:75], v89 offset:16896\n"
  "v_mul_f32 v2, s40, v2\n"
  "v_mfma_f32_16x16x32_bf16 v[44:47], v[76:79], v[236:239], v[44:47]\n"
  "v_mul_f32 v3, s40, v3\n"
  "v_mul_f32 v4, s40, v4\n"
  "s_waitcnt lgkmcnt(6)\n"
  "v_mfma_f32_16x16x32_bf16 v[48:51], v[80:83], v[232:235], v[48:51]\n"
  "ds_read_b64_tr_b16 v[76:77], v90 offset:16384\n"
  "ds_read_b64_tr_b16 v[78:79], v90 offset:16896\n"
  "v_mul_f32 v5, s40, v5\n"
  "v_mul_f32 v6, s40, v6\n"
  "s_setprio 0\n"
  "v_mfma_f32_16x16x32_bf16 v[52:55], v[80:83], v[236:239], v[52:55]\n"
  "v_mul_f32 v7, s40, v7\n"
  "s_waitcnt lgkmcnt(6)\n"
  "v_mfma_f32_16x16x32_bf16 v[56:59], v[64:67], v[232:235], v[56:59]\n"
  "ds_read_b64_tr_b16 v[80:81], v91 offset:16384\n"
  "ds_read_b64_tr_b16 v[82:83], v91 offset:16896\n"
  "v_cvt_pk_bf16_f32 v20, v0, v1\n"
  "v_cvt_pk_bf16_f32 v21, v2, v3\n"
  "v_mfma_f32_16x16x32_bf16 v[60:63], v[64:67], v[236:239], v[60:63]\n"
  "v_cvt_pk_bf16_f32 v22, v4, v5\n"
  "v_cvt_pk_bf16_f32 v23, v6, v7\n"
  "s_waitcnt lgkmcnt(6)\n"
  "v_mfma_f32_16x16x32_bf16 v[32:35], v[68:71], v[240:243], v[32:35]\n"
  "ds_read_b64_tr_b16 v[64:65], v88 offset:20480\n"
  "ds_read_b64_tr_b16 v[66:67], v88 offset:20992\n"
  "v_mul_f32 v24, s40, v24\n"
  "buffer_store_dwordx4 v[16:19], v98, s[20:23], 0 offen sc1\n"
  "v_mfma_f32_16x16x32_bf16 v[36:39], v[68:71], v[244:247], v[36:39]\n"
  "v_mul_f32 v25, s40, v25\n"
  "v_mul_f32 v26, s40, v26\n"
  "s_waitcnt lgkmcnt(6)\n"
  "v_mfma_f32_16x16x32_bf16 v[40:43], v[72:75], v[240:243], v[40:43]\n"
  "ds_read_b64_tr_b16 v[68:69], v89 offset:20480\n"
  "ds_read_b64_tr_b16 v[70:71], v89 offset:20992\n"
  "v_mul_f32 v27, s40, v27\n"
  "v_mul_f32 v28, s40, v28\n"
  "v_mfma_f32_16x16x32_bf16 v[44:47], v[72:75], v[244:247], v[44:47]\n"
  "v_mul_f32 v29, s40, v29\n"
  "s_waitcnt lgkmcnt(6)\n"
  "v_mfma_f32_16x16x32_bf16 v[48:51], v[76:79], v[240:243], v[48:51]\n"
  "ds_read_b64_tr_b16 v[72:73], v90 offset:20480\n"
  "ds_read_b64_tr_b16 v[74:75], v90 offset:20992\n"
  "v_mul_f32 v30, s40, v30\n"
  "v_mul_f32 v31, s40, v31\n"
  "v_mfma_f32_16x16x32_bf16 v[52:55], v[76:79], v[244:247], v[52:55]\n"
  "v_cvt_pk_bf16_f32 v24, v24, v25\n"
  "v_cvt_pk_bf16_f32 v25, v26, v27\n"
  "s_waitcnt lgkmcnt(6)\n"
  "v_mfma_f32_16x16x32_bf16 v[56:59], v[80:83], v[240:243], v[56:59]\n"
  "ds_read_b64_tr_b16 v[76:77], v91 offset:20480\n"
  "ds_read_b64_tr_b16 v[78:79], v91 offset:20992\n"
  "v_cvt_pk_bf16_f32 v26, v28, v29\n"
  "v_mfma_f32_16x16x32_bf16 v[60:63], v[80:83], v[244:247], v[60:63]\n"
  "v_cvt_pk_bf16_f32 v27, v30, v31\n"
  "v_mul_f32 v8, s40, v8\n"
  "s_waitcnt lgkmcnt(6)\n"
  "v_mfma_f32_16x16x32_bf16 v[32:35], v[64:67], v[248:251], v[32:35]\n"
  "s_barrier\n"
  "ds_read_b64_tr_b16 v[80:81], v88 offset:24576\n"
  "ds_read_b64_tr_b16 v[82:83], v88 offset:25088\n"
  "v_mul_f32 v9, s40, v9\n"
  "v_mul_f32 v10, s40, v10\n"
  "s_setprio 3\n"
  "v_mfma_f32_16x16x32_bf16 v[36:39], v[64:67], v[252:255], v[36:39]\n"
  "s_add_u32 m0, s28, 114688\n"
  "s_mov_b32 s43, 4128896\n"
  "buffer_load_dwordx4 v94, s[12:15], s43 offen lds\n"
  "v_mul_f32 v11, s40, v11\n"
  "s_waitcnt lgkmcnt(6)\n"
  "v_mfma_f32_16x16x32_bf16 v[40:43], v[68:71], v[248:251], v[40:43]\n"
  "ds_read_b64_tr_b16 v[64:65], v89 offset:24576\n"
  "ds_read_b64_tr_b16 v[66:67], v89 offset:25088\n"
  "v_mul_f32 v12, s40, v12\n"
  "v_mul_f32 v13, s40, v13\n"
  "v_mfma_f32_16x16x32_bf16 v[44:47], v[68:71], v[252:255], v[44:47]\n"
  "v_mul_f32 v14, s40, v14\n"
  "v_mul_f32 v15, s40, v15\n"
  "s_waitcnt lgkmcnt(6)\n"
  "v_mfma_f32_16x16x32_bf16 v[48:51], v[72:75], v[248:251], v[48:51]\n"
  "ds_read_b64_tr_b16 v[68:69], v88 offset:28672\n"
  "ds_read_b64_tr_b16 v[70:71], v88 offset:29184\n"
  "v_cvt_pk_bf16_f32 v28, v8, v9\n"
  "v_mov_b32 v101, 0xbdd2d3e8\n"
  "s_setprio 2\n"
  "v_mfma_f32_16x16x32_bf16 v[52:55], v[72:75], v[252:255], v[52:55]\n"
  "v_cvt_pk_bf16_f32 v29, v10, v11\n"
  "v_cvt_pk_bf16_f32 v30, v12, v13\n"
  "s_waitcnt lgkmcnt(6)\n"
  "v_mfma_f32_16x16x32_bf16 v[56:59], v[76:79], v[248:251], v[56:59]\n"
  "ds_read_b64_tr_b16 v[72:73], v89 offset:28672\n"
  "ds_read_b64_tr_b16 v[74:75], v89 offset:29184\n"
  "v_cvt_pk_bf16_f32 v31, v14, v15\n"
  "v_mfma_f32_16x16x32_bf16 v[60:63], v[76:79], v[252:255], v[60:63]\n"
  "s_waitcnt lgkmcnt(6)\n"
  "v_mfma_f32_16x16x32_bf16 v[0:3], v[80:83], v[112:115], 0\n"
  "ds_read_b64_tr_b16 v[76:77], v88 offset:32768\n"
  "ds_read_b64_tr_b16 v[78:79], v88 offset:33280\n"
  "s_setprio 1\n"
  "v_mfma_f32_16x16x32_bf16 v[4:7], v[80:83], v[116:119], 0\n"
  "s_waitcnt lgkmcnt(6)\n"
  "v_mfma_f32_16x16x32_bf16 v[8:11], v[64:67], v[112:115], 0\n"
  "ds_read_b64_tr_b16 v[80:81], v89 offset:32768\n"
  "ds_read_b64_tr_b16 v[82:83], v89 offset:33280\n"
  "v_mul_f32 v102, v32, v32\n"
  "v_mul_f32 v103, v33, v33\n"
  "v_mfma_f32_16x16x32_bf16 v[12:15], v[64:67], v[116:119], 0\n"
  "s_add_u32 m0, s28, 122880\n"
  "s_mov_b32 s43, 4718720\n"
  "buffer_load_dwordx4 v94, s[12:15], s43 offen lds\n"
  "v_mul_f32 v104, v34, v34\n"
  "s_waitcnt lgkmcnt(6)\n"
  "v_mfma_f32_16x16x32_bf16 v[0:3], v[68:71], v[120:123], v[0:3]\n"
  "ds_read_b64_tr_b16 v[64:65], v88 offset:36864\n"
  "ds_read_b64_tr_b16 v[66:67], v88 offset:37376\n"
  "v_mul_f32 v105, v35, v35\n"
  "s_setprio 0\n"
  "v_mfma_f32_16x16x32_bf16 v[4:7], v[68:71], v[124:127], v[4:7]\n"
  "v_fmaak_f32 v102, v102, v101, 0xc0135761\n"
  "v_fmaak_f32 v103, v103, v101, 0xc0135761\n"
  "s_waitcnt lgkmcnt(6)\n"
  "v_mfma_f32_16x16x32_bf16 v[8:11], v[72:75], v[120:123], v[8:11]\n"
  "ds_read_b64_tr_b16 v[68:69], v89 offset:36864\n"
  "ds_read_b64_tr_b16 v[70:71], v89 offset:37376\n"
  "v_fmaak_f32 v104, v104, v101, 0xc0135761\n"
  "v_mfma_f32_16x16x32_bf16 v[12:15], v[72:75], v[124:127], v[12:15]\n"
  "v_fmaak_f32 v105, v105, v101, 0xc0135761\n"
  "s_waitcnt lgkmcnt(6)\n"
  "v_mfma_f32_16x16x32_bf16 v[0:3], v[76:79], v[128:131], v[0:3]\n"
  "s_barrier\n"
  "v_add_u32 v88, 0xfffe2000, v88\n"
  "ds_read_b64_tr_b16 v[72:73], v88 offset:0\n"
  "ds_read_b64_tr_b16 v[74:75], v88 offset:512\n"
  "v_mul_f32 v102, v102, v32\n"
  "s_setprio 3\n"
  "v_mfma_f32_16x16x32_bf16 v[4:7], v[76:79], v[132:135], v[4:7]\n"
  "s_mov_b32 s43, 0\n"
  "s_add_u32 m0, s28, 131072\n"
  "s_nop 0\n"
  "buffer_load_dwordx4 v84, s[36:39], s43 offen lds\n"
  "v_mul_f32 v103, v103, v33\n"
  "v_mul_f32 v104, v104, v34\n"
  "s_waitcnt lgkmcnt(6)\n"
  "v_mfma_f32_16x16x32_bf16 v[8:11], v[80:83], v[128:131], v[8:11]\n"
  "v_add_u32 v89, 0xfffe2000, v89\n"
  "ds_read_b64_tr_b16 v[76:77], v89 offset:0\n"
  "ds_read_b64_tr_b16 v[78:79], v89 offset:512\n"
  "v_mul_f32 v105, v105, v35\n"
  "v_mfma_f32_16x16x32_bf16 v[12:15], v[80:83], v[132:135], v[12:15]\n"
  "v_exp_f32 v102, v102\n"
  "s_waitcnt lgkmcnt(6)\n"
  "v_mfma_f32_16x16x32_bf16 v[0:3], v[64:67], v[136:139], v[0:3]\n"
  "ds_read_b64_tr_b16 v[80:81], v88 offset:4096\n"
  "ds_read_b64_tr_b16 v[82:83], v88 offset:4608\n"
  "buffer_load_dword v87, v85, s[16:19], 0 offen\n"
  "buffer_store_dwordx4 v[20:23], v96, s[20:23], 0 offen sc1\n"
  "v_exp_f32 v103, v103\n"
  "v_mfma_f32_16x16x32_bf16 v[4:7], v[64:67], v[140:143], v[4:7]\n"
  "s_add_u32 s43, s35, 0\n"
  "s_add_u32 m0, s28, 139264\n"
  "s_nop 0\n"
  "buffer_load_dwordx4 v84, s[36:39], s43 offen lds\n"
  "v_exp_f32 v104, v104\n"
  "v_exp_f32 v105, v105\n"
  "s_waitcnt lgkmcnt(6)\n"
  "v_mfma_f32_16x16x32_bf16 v[8:11], v[68:71], v[136:139], v[8:11]\n"
  "ds_read_b64_tr_b16 v[64:65], v89 offset:4096\n"
  "ds_read_b64_tr_b16 v[66:67], v89 offset:4608\n"
  "v_add_f32 v102, 1.0, v102\n"
  "v_mfma_f32_16x16x32_bf16 v[12:15], v[68:71], v[140:143], v[12:15]\n"
  "v_add_f32 v103, 1.0, v103\n"
  "s_waitcnt lgkmcnt(6)\n"
  "v_mfma_f32_16x16x32_bf16 v[0:3], v[72:75], v[144:147], v[0:3]\n"
  "ds_read_b64_tr_b16 v[68:69], v88 offset:8192\n"
  "ds_read_b64_tr_b16 v[70:71], v88 offset:8704\n"
  "v_add_f32 v104, 1.0, v104\n"
  "v_mfma_f32_16x16x32_bf16 v[4:7], v[72:75], v[148:151], v[4:7]\n"
  "v_add_f32 v105, 1.0, v105\n"
  "v_rcp_f32 v102, v102\n"
  "s_waitcnt lgkmcnt(6)\n"
  "v_mfma_f32_16x16x32_bf16 v[8:11], v[76:79], v[144:147], v[8:11]\n"
  "ds_read_b64_tr_b16 v[72:73], v89 offset:8192\n"
  "ds_read_b64_tr_b16 v[74:75], v89 offset:8704\n"
  "v_rcp_f32 v103, v103\n"
  "v_mfma_f32_16x16x32_bf16 v[12:15], v[76:79], v[148:151], v[12:15]\n"
  "v_rcp_f32 v104, v104\n"
  "s_waitcnt lgkmcnt(6)\n"
  "v_mfma_f32_16x16x32_bf16 v[0:3], v[80:83], v[152:155], v[0:3]\n"
  "ds_read_b64_tr_b16 v[76:77], v88 offset:12288\n"
  "ds_read_b64_tr_b16 v[78:79], v88 offset:12800\n"
  "v_rcp_f32 v105, v105\n"
  "v_mul_f32 v32, v32, v102\n"
  "v_mfma_f32_16x16x32_bf16 v[4:7], v[80:83], v[156:159], v[4:7]\n"
  "v_mul_f32 v33, v33, v103\n"
  "s_waitcnt lgkmcnt(6)\n"
  "v_mfma_f32_16x16x32_bf16 v[8:11], v[64:67], v[152:155], v[8:11]\n"
  "ds_read_b64_tr_b16 v[80:81], v89 offset:12288\n"
  "ds_read_b64_tr_b16 v[82:83], v89 offset:12800\n"
  "v_mul_f32 v34, v34, v104\n"
  "s_setprio 2\n"
  "v_mfma_f32_16x16x32_bf16 v[12:15], v[64:67], v[156:159], v[12:15]\n"
  "v_mul_f32 v35, v35, v105\n"
  "s_waitcnt lgkmcnt(6)\n"
  "v_mfma_f32_16x16x32_bf16 v[0:3], v[68:71], v[160:163], v[0:3]\n"
  "ds_read_b64_tr_b16 v[64:65], v88 offset:16384\n"
  "ds_read_b64_tr_b16 v[66:67], v88 offset:16896\n"
  "s_mov_b32 exec_lo, 0\n"
  "buffer_load_dword v87, v85, s[12:15], 0 offen\n"
  "s_mov_b32 exec_lo, -1\n"
  "v_mul_f32 v106, v40, v40\n"
  "v_mul_f32 v107, v41, v41\n"
  "v_mfma_f32_16x16x32_bf16 v[4:7], v[68:71], v[164:167], v[4:7]\n"
  "v_mul_f32 v108, v42, v42\n"
  "s_waitcnt lgkmcnt(6)\n"
  "v_mfma_f32_16x16x32_bf16 v[8:11], v[72:75], v[160:163], v[8:11]\n"
  "ds_read_b64_tr_b16 v[68:69], v89 offset:16384\n"
  "ds_read_b64_tr_b16 v[70:71], v89 offset:16896\n"
  "v_mul_f32 v109, v43, v43\n"
  "v_mfma_f32_16x16x32_bf16 v[12:15], v[72:75], v[164:167], v[12:15]\n"
  "v_fmaak_f32 v106, v106, v101, 0xc0135761\n"
  "s_waitcnt lgkmcnt(6)\n"
  "v_mfma_f32_16x16x32_bf16 v[0:3], v[76:79], v[168:171], v[0:3]\n"
  "ds_read_b64_tr_b16 v[72:73], v88 offset:20480\n"
  "ds_read_b64_tr_b16 v[74:75], v88 offset:20992\n"
  "v_fmaak_f32 v107, v107, v101, 0xc0135761\n"
  "v_fmaak_f32 v108, v108, v101, 0xc0135761\n"
  "v_mfma_f32_16x16x32_bf16 v[4:7], v[76:79], v[172:175], v[4:7]\n"
  "v_fmaak_f32 v109, v109, v101, 0xc0135761\n"
  "s_waitcnt lgkmcnt(6)\n"
  "v_mfma_f32_16x16x32_bf16 v[8:11], v[80:83], v[168:171], v[8:11]\n"
  "ds_read_b64_tr_b16 v[76:77], v89 offset:20480\n"
  "ds_read_b64_tr_b16 v[78:79], v89 offset:20992\n"
  "v_mul_f32 v106, v106, v40\n"
  "v_mfma_f32_16x16x32_bf16 v[12:15], v[80:83], v[172:175], v[12:15]\n"
  "v_mul_f32 v107, v107, v41\n"
  "s_waitcnt lgkmcnt(6)\n"
  "v_mfma_f32_16x16x32_bf16 v[0:3], v[64:67], v[176:179], v[0:3]\n"
  "ds_read_b64_tr_b16 v[80:81], v88 offset:24576\n"
  "ds_read_b64_tr_b16 v[82:83], v88 offset:25088\n"
  "v_mul_f32 v108, v108, v42\n"
  "v_mul_f32 v109, v109, v43\n"
  "v_mfma_f32_16x16x32_bf16 v[4:7], v[64:67], v[180:183], v[4:7]\n"
  "v_exp_f32 v106, v106\n"
  "s_waitcnt lgkmcnt(6)\n"
  "v_mfma_f32_16x16x32_bf16 v[8:11], v[68:71], v[176:179], v[8:11]\n"
  "ds_read_b64_tr_b16 v[64:65], v89 offset:24576\n"
  "ds_read_b64_tr_b16 v[66:67], v89 offset:25088\n"
  "v_exp_f32 v107, v107\n"
  "v_mfma_f32_16x16x32_bf16 v[12:15], v[68:71], v[180:183], v[12:15]\n"
  "v_exp_f32 v108, v108\n"
  "v_exp_f32 v109, v109\n"
  "s_waitcnt lgkmcnt(6)\n"
  "v_mfma_f32_16x16x32_bf16 v[0:3], v[72:75], v[184:187], v[0:3]\n"
  "ds_read_b64_tr_b16 v[68:69], v88 offset:28672\n"
  "ds_read_b64_tr_b16 v[70:71], v88 offset:29184\n"
  "buffer_store_dwordx4 v[24:27], v99, s[20:23], 0 offen sc1\n"
  "v_add_f32 v106, 1.0, v106\n"
  "s_setprio 1\n"
  "v_mfma_f32_16x16x32_bf16 v[4:7], v[72:75], v[188:191], v[4:7]\n"
  "v_add_f32 v107, 1.0, v107\n"
  "s_waitcnt lgkmcnt(6)\n"
  "v_mfma_f32_16x16x32_bf16 v[8:11], v[76:79], v[184:187], v[8:11]\n"
  "ds_read_b64_tr_b16 v[72:73], v89 offset:28672\n"
  "ds_read_b64_tr_b16 v[74:75], v89 offset:29184\n"
  "v_add_f32 v108, 1.0, v108\n"
  "v_mfma_f32_16x16x32_bf16 v[12:15], v[76:79], v[188:191], v[12:15]\n"
  "v_add_f32 v109, 1.0, v109\n"
  "v_rcp_f32 v106, v106\n"
  "s_waitcnt lgkmcnt(6)\n"
  "v_mfma_f32_16x16x32_bf16 v[0:3], v[80:83], v[192:195], v[0:3]\n"
  "ds_read_b64_tr_b16 v[76:77], v88 offset:32768\n"
  "ds_read_b64_tr_b16 v[78:79], v88 offset:33280\n"
  "v_rcp_f32 v107, v107\n"
  "v_mfma_f32_16x16x32_bf16 v[4:7], v[80:83], v[196:199], v[4:7]\n"
  "v_rcp_f32 v108, v108\n"
  "s_waitcnt lgkmcnt(6)\n"
  "v_mfma_f32_16x16x32_bf16 v[8:11], v[64:67], v[192:195], v[8:11]\n"
  "ds_read_b64_tr_b16 v[80:81], v89 offset:32768\n"
  "ds_read_b64_tr_b16 v[82:83], v89 offset:33280\n"
  "v_rcp_f32 v109, v109\n"
  "v_mfma_f32_16x16x32_bf16 v[12:15], v[64:67], v[196:199], v[12:15]\n"
  "v_mul_f32 v40, v40, v106\n"
  "v_mul_f32 v41, v41, v107\n"
  "s_waitcnt lgkmcnt(6)\n"
  "v_mfma_f32_16x16x32_bf16 v[0:3], v[68:71], v[200:203], v[0:3]\n"
  "ds_read_b64_tr_b16 v[64:65], v88 offset:36864\n"
  "ds_read_b64_tr_b16 v[66:67], v88 offset:37376\n"
  "v_mul_f32 v42, v42, v108\n"
  "v_mfma_f32_16x16x32_bf16 v[4:7], v[68:71], v[204:207], v[4:7]\n"
  "v_mul_f32 v43, v43, v109\n"
  "s_waitcnt lgkmcnt(6)\n"
  "v_mfma_f32_16x16x32_bf16 v[8:11], v[72:75], v[200:203], v[8:11]\n"
  "ds_read_b64_tr_b16 v[68:69], v89 offset:36864\n"
  "ds_read_b64_tr_b16 v[70:71], v89 offset:37376\n"
  "v_mul_f32 v102, v36, v36\n"
  "v_mfma_f32_16x16x32_bf16 v[12:15], v[72:75], v[204:207], v[12:15]\n"
  "v_mul_f32 v103, v37, v37\n"
  "v_mul_f32 v104, v38, v38\n"
  "s_waitcnt lgkmcnt(6)\n"
  "v_mfma_f32_16x16x32_bf16 v[0:3], v[76:79], v[208:211], v[0:3]\n"
  "ds_read_b64_tr_b16 v[72:73], v88 offset:40960\n"
  "ds_read_b64_tr_b16 v[74:75], v88 offset:41472\n"
  "v_mul_f32 v105, v39, v39\n"
  "v_mfma_f32_16x16x32_bf16 v[4:7], v[76:79], v[212:215], v[4:7]\n"
  "v_fmaak_f32 v102, v102, v101, 0xc0135761\n"
  "s_waitcnt lgkmcnt(6)\n"
  "v_mfma_f32_16x16x32_bf16 v[8:11], v[80:83], v[208:211], v[8:11]\n"
  "ds_read_b64_tr_b16 v[76:77], v89 offset:40960\n"
  "ds_read_b64_tr_b16 v[78:79], v89 offset:41472\n"
  "v_fmaak_f32 v103, v103, v101, 0xc0135761\n"
  "v_fmaak_f32 v104, v104, v101, 0xc0135761\n"
  "s_setprio 0\n"
  "v_mfma_f32_16x16x32_bf16 v[12:15], v[80:83], v[212:215], v[12:15]\n"
  "v_fmaak_f32 v105, v105, v101, 0xc0135761\n"
  "s_waitcnt lgkmcnt(6)\n"
  "v_mfma_f32_16x16x32_bf16 v[0:3], v[64:67], v[216:219], v[0:3]\n"
  "ds_read_b64_tr_b16 v[80:81], v88 offset:45056\n"
  "ds_read_b64_tr_b16 v[82:83], v88 offset:45568\n"
  "v_mul_f32 v102, v102, v36\n"
  "v_mfma_f32_16x16x32_bf16 v[4:7], v[64:67], v[220:223], v[4:7]\n"
  "v_mul_f32 v103, v103, v37\n"
  "s_waitcnt lgkmcnt(6)\n"
  "v_mfma_f32_16x16x32_bf16 v[8:11], v[68:71], v[216:219], v[8:11]\n"
  "ds_read_b64_tr_b16 v[64:65], v89 offset:45056\n"
  "ds_read_b64_tr_b16 v[66:67], v89 offset:45568\n"
  "v_mul_f32 v104, v104, v38\n"
  "v_mul_f32 v105, v105, v39\n"
  "v_mfma_f32_16x16x32_bf16 v[12:15], v[68:71], v[220:223], v[12:15]\n"
  "v_exp_f32 v102, v102\n"
  "s_waitcnt lgkmcnt(6)\n"
  "v_mfma_f32_16x16x32_bf16 v[0:3], v[72:75], v[224:227], v[0:3]\n"
  "ds_read_b64_tr_b16 v[68:69], v88 offset:49152\n"
  "ds_read_b64_tr_b16 v[70:71], v88 offset:49664\n"
  "v_exp_f32 v103, v103\n"
  "v_mfma_f32_16x16x32_bf16 v[4:7], v[72:75], v[228:231], v[4:7]\n"
  "v_exp_f32 v104, v104\n"
  "s_waitcnt lgkmcnt(6)\n"
  "v_mfma_f32_16x16x32_bf16 v[8:11], v[76:79], v[224:227], v[8:11]\n"
  "ds_read_b64_tr_b16 v[72:73], v89 offset:49152\n"
  "ds_read_b64_tr_b16 v[74:75], v89 offset:49664\n"
  "v_exp_f32 v105, v105\n"
  "v_add_f32 v102, 1.0, v102\n"
  "v_mfma_f32_16x16x32_bf16 v[12:15], v[76:79], v[228:231], v[12:15]\n"
  "v_add_f32 v103, 1.0, v103\n"
  "s_waitcnt lgkmcnt(6)\n"
  "v_mfma_f32_16x16x32_bf16 v[0:3], v[80:83], v[232:235], v[0:3]\n"
  "ds_read_b64_tr_b16 v[76:77], v88 offset:53248\n"
  "ds_read_b64_tr_b16 v[78:79], v88 offset:53760\n"
  "buffer_store_dwordx4 v[28:31], v97, s[20:23], 0 offen sc1\n"
  "v_add_f32 v104, 1.0, v104\n"
  "v_mfma_f32_16x16x32_bf16 v[4:7], v[80:83], v[236:239], v[4:7]\n"
  "v_add_f32 v105, 1.0, v105\n"
  "s_waitcnt lgkmcnt(6)\n"
  "v_mfma_f32_16x16x32_bf16 v[8:11], v[64:67], v[232:235], v[8:11]\n"
  "ds_read_b64_tr_b16 v[80:81], v89 offset:53248\n"
  "ds_read_b64_tr_b16 v[82:83], v89 offset:53760\n"
  "v_rcp_f32 v102, v102\n"
  "v_rcp_f32 v103, v103\n"
  "v_mfma_f32_16x16x32_bf16 v[12:15], v[64:67], v[236:239], v[12:15]\n"
  "v_rcp_f32 v104, v104\n"
  "s_waitcnt lgkmcnt(6)\n"
  "v_mfma_f32_16x16x32_bf16 v[0:3], v[68:71], v[240:243], v[0:3]\n"
  "s_waitcnt vmcnt(7)\n"
  "s_barrier\n"
  "ds_read_b64_tr_b16 v[64:65], v88 offset:57344\n"
  "ds_read_b64_tr_b16 v[66:67], v88 offset:57856\n"
  "v_rcp_f32 v105, v105\n"
  "s_setprio 3\n"
  "v_mfma_f32_16x16x32_bf16 v[4:7], v[68:71], v[244:247], v[4:7]\n"
  "v_mul_f32 v36, v36, v102\n"
  "v_mul_f32 v37, v37, v103\n"
  "s_waitcnt lgkmcnt(6)\n"
  "v_mfma_f32_16x16x32_bf16 v[8:11], v[72:75], v[240:243], v[8:11]\n"
  "ds_read_b64_tr_b16 v[68:69], v89 offset:57344\n"
  "ds_read_b64_tr_b16 v[70:71], v89 offset:57856\n"
  "v_mul_f32 v38, v38, v104\n"
  "v_mfma_f32_16x16x32_bf16 v[12:15], v[72:75], v[244:247], v[12:15]\n"
  "v_mul_f32 v39, v39, v105\n"
  "s_waitcnt lgkmcnt(6)\n"
  "v_mfma_f32_16x16x32_bf16 v[0:3], v[76:79], v[248:251], v[0:3]\n"
  "ds_read_b64_tr_b16 v[72:73], v88 offset:61440\n"
  "ds_read_b64_tr_b16 v[74:75], v88 offset:61952\n"
  "v_mul_f32 v106, v44, v44\n"
  "v_mfma_f32_16x16x32_bf16 v[4:7], v[76:79], v[252:255], v[4:7]\n"
  "v_mul_f32 v107, v45, v45\n"
  "v_mul_f32 v108, v46, v46\n"
  "s_waitcnt lgkmcnt(6)\n"
  "v_mfma_f32_16x16x32_bf16 v[8:11], v[80:83], v[248:251], v[8:11]\n"
  "ds_read_b64_tr_b16 v[76:77], v89 offset:61440\n"
  "ds_read_b64_tr_b16 v[78:79], v89 offset:61952\n"
  "v_mul_f32 v109, v47, v47\n"
  "v_mfma_f32_16x16x32_bf16 v[12:15], v[80:83], v[252:255], v[12:15]\n"
  "v_fmaak_f32 v106, v106, v101, 0xc0135761\n"
  "s_waitcnt lgkmcnt(6)\n"
  "v_mfma_f32_16x16x32_bf16 v[16:19], v[64:67], v[112:115], 0\n"
  "v_add_u32 v88, 0x10000, v88\n"
  "ds_read_b64_tr_b16 v[80:81], v88 offset:0\n"
  "ds_read_b64_tr_b16 v[82:83], v88 offset:512\n"
  "v_fmaak_f32 v107, v107, v101, 0xc0135761\n"
  "v_mfma_f32_16x16x32_bf16 v[20:23], v[64:67], v[116:119], 0\n"
  "v_fmaak_f32 v108, v108, v101, 0xc0135761\n"
  "v_fmaak_f32 v109, v109, v101, 0xc0135761\n"
  "s_waitcnt lgkmcnt(6)\n"
  "v_mfma_f32_16x16x32_bf16 v[24:27], v[68:71], v[112:115], 0\n"
  "v_add_u32 v89, 0x10000, v89\n"
  "ds_read_b64_tr_b16 v[64:65], v89 offset:0\n"
  "ds_read_b64_tr_b16 v[66:67], v89 offset:512\n"
  "v_mul_f32 v106, v106, v44\n"
  "v_mfma_f32_16x16x32_bf16 v[28:31], v[68:71], v[116:119], 0\n"
  "s_waitcnt vmcnt(3)\n"
  "ds_read_b64_tr_b16 v[96:97], v86 offset:0\n"
  "ds_read_b64_tr_b16 v[98:99], v86 offset:8192\n"
  "v_mul_f32 v107, v107, v45\n"
  "s_waitcnt lgkmcnt(8)\n"
  "v_mfma_f32_16x16x32_bf16 v[16:19], v[72:75], v[120:123], v[16:19]\n"
  "ds_read_b64_tr_b16 v[68:69], v88 offset:4096\n"
  "ds_read_b64_tr_b16 v[70:71], v88 offset:4608\n"
  "v_mul_f32 v108, v108, v46\n"
  "v_mfma_f32_16x16x32_bf16 v[20:23], v[72:75], v[124:127], v[20:23]\n"
  "v_mul_f32 v109, v109, v47\n"
  "v_exp_f32 v106, v106\n"
  "s_waitcnt lgkmcnt(8)\n"
  "v_mfma_f32_16x16x32_bf16 v[24:27], v[76:79], v[120:123], v[24:27]\n"
  "ds_read_b64_tr_b16 v[72:73], v89 offset:4096\n"
  "ds_read_b64_tr_b16 v[74:75], v89 offset:4608\n"
  "v_exp_f32 v107, v107\n"
  "v_mfma_f32_16x16x32_bf16 v[28:31], v[76:79], v[124:127], v[28:31]\n"
  "v_exp_f32 v108, v108\n"
  "s_waitcnt lgkmcnt(8)\n"
  "v_mfma_f32_16x16x32_bf16 v[16:19], v[80:83], v[128:131], v[16:19]\n"
  "ds_read_b64_tr_b16 v[76:77], v88 offset:8192\n"
  "ds_read_b64_tr_b16 v[78:79], v88 offset:8704\n"
  "v_exp_f32 v109, v109\n"
  "v_add_f32 v106, 1.0, v106\n"
  "v_mfma_f32_16x16x32_bf16 v[20:23], v[80:83], v[132:135], v[20:23]\n"
  "v_add_f32 v107, 1.0, v107\n"
  "s_waitcnt lgkmcnt(8)\n"
  "v_mfma_f32_16x16x32_bf16 v[24:27], v[64:67], v[128:131], v[24:27]\n"
  "ds_read_b64_tr_b16 v[80:81], v89 offset:8192\n"
  "ds_read_b64_tr_b16 v[82:83], v89 offset:8704\n"
  "v_add_f32 v108, 1.0, v108\n"
  "v_mfma_f32_16x16x32_bf16 v[28:31], v[64:67], v[132:135], v[28:31]\n"
  "v_add_f32 v109, 1.0, v109\n"
  "s_waitcnt lgkmcnt(6)\n"
  "v_mfma_f32_16x16x32_bf16 v[16:19], v[68:71], v[136:139], v[16:19]\n"
  "ds_read_b64_tr_b16 v[64:65], v88 offset:12288\n"
  "ds_read_b64_tr_b16 v[66:67], v88 offset:12800\n"
  "s_mov_b32 s22, s76\n"
  "buffer_store_dwordx4 v[96:99], v100, s[20:23], s71 offen offset:0 sc1\n"
  "s_mov_b32 s22, 39133312\n"
  "v_rcp_f32 v106, v106\n"
  "v_rcp_f32 v107, v107\n"
  "v_mfma_f32_16x16x32_bf16 v[20:23], v[68:71], v[140:143], v[20:23]\n"
  "v_rcp_f32 v108, v108\n"
  "s_waitcnt lgkmcnt(6)\n"
  "v_mfma_f32_16x16x32_bf16 v[24:27], v[72:75], v[136:139], v[24:27]\n"
  "ds_read_b64_tr_b16 v[68:69], v89 offset:12288\n"
  "ds_read_b64_tr_b16 v[70:71], v89 offset:12800\n"
  "v_rcp_f32 v109, v109\n"
  "v_mfma_f32_16x16x32_bf16 v[28:31], v[72:75], v[140:143], v[28:31]\n"
  "v_mul_f32 v44, v44, v106\n"
  "s_waitcnt lgkmcnt(6)\n"
  "v_mfma_f32_16x16x32_bf16 v[16:19], v[76:79], v[144:147], v[16:19]\n"
  "ds_read_b64_tr_b16 v[72:73], v88 offset:16384\n"
  "ds_read_b64_tr_b16 v[74:75], v88 offset:16896\n"
  "v_mul_f32 v45, v45, v107\n"
  "v_mul_f32 v46, v46, v108\n"
  "v_mfma_f32_16x16x32_bf16 v[20:23], v[76:79], v[148:151], v[20:23]\n"
  "v_mul_f32 v47, v47, v109\n"
  "s_waitcnt lgkmcnt(6)\n"
  "v_mfma_f32_16x16x32_bf16 v[24:27], v[80:83], v[144:147], v[24:27]\n"
  "ds_read_b64_tr_b16 v[76:77], v89 offset:16384\n"
  "ds_read_b64_tr_b16 v[78:79], v89 offset:16896\n"
  "v_mul_f32 v0, v0, v32\n"
  "v_mfma_f32_16x16x32_bf16 v[28:31], v[80:83], v[148:151], v[28:31]\n"
  "v_mul_f32 v1, v1, v33\n"
  "s_waitcnt lgkmcnt(6)\n"
  "v_mfma_f32_16x16x32_bf16 v[16:19], v[64:67], v[152:155], v[16:19]\n"
  "ds_read_b64_tr_b16 v[80:81], v88 offset:20480\n"
  "ds_read_b64_tr_b16 v[82:83], v88 offset:20992\n"
  "v_mul_f32 v2, v2, v34\n"
  "v_mul_f32 v3, v3, v35\n"
  "v_mfma_f32_16x16x32_bf16 v[20:23], v[64:67], v[156:159], v[20:23]\n"
  "v_mul_f32 v8, v8, v40\n"
  "s_waitcnt lgkmcnt(6)\n"
  "v_mfma_f32_16x16x32_bf16 v[24:27], v[68:71], v[152:155], v[24:27]\n"
  "ds_read_b64_tr_b16 v[64:65], v89 offset:20480\n"
  "ds_read_b64_tr_b16 v[66:67], v89 offset:20992\n"
  "v_mul_f32 v9, v9, v41\n"
  "v_mfma_f32_16x16x32_bf16 v[28:31], v[68:71], v[156:159], v[28:31]\n"
  "v_mul_f32 v10, v10, v42\n"
  "v_mul_f32 v11, v11, v43\n"
  "s_waitcnt lgkmcnt(6)\n"
  "v_mfma_f32_16x16x32_bf16 v[16:19], v[72:75], v[160:163], v[16:19]\n"
  "ds_read_b64_tr_b16 v[68:69], v88 offset:24576\n"
  "ds_read_b64_tr_b16 v[70:71], v88 offset:25088\n"
  "v_cvt_pk_bf16_f32 v32, v0, v1\n"
  "v_mfma_f32_16x16x32_bf16 v[20:23], v[72:75], v[164:167], v[20:23]\n"
  "v_cvt_pk_bf16_f32 v33, v2, v3\n"
  "s_waitcnt lgkmcnt(6)\n"
  "v_mfma_f32_16x16x32_bf16 v[24:27], v[76:79], v[160:163], v[24:27]\n"
  "ds_read_b64_tr_b16 v[72:73], v89 offset:24576\n"
  "ds_read_b64_tr_b16 v[74:75], v89 offset:25088\n"
  "v_cvt_pk_bf16_f32 v34, v8, v9\n"
  "v_mfma_f32_16x16x32_bf16 v[28:31], v[76:79], v[164:167], v[28:31]\n"
  "ds_read_b64_tr_b16 v[96:97], v86 offset:8\n"
  "ds_read_b64_tr_b16 v[98:99], v86 offset:8200\n"
  "v_cvt_pk_bf16_f32 v35, v10, v11\n"
  "v_mul_f32 v4, v4, v36\n"
  "s_waitcnt lgkmcnt(8)\n"
  "v_mfma_f32_16x16x32_bf16 v[16:19], v[80:83], v[168:171], v[16:19]\n"
  "ds_read_b64_tr_b16 v[76:77], v88 offset:28672\n"
  "ds_read_b64_tr_b16 v[78:79], v88 offset:29184\n"
  "v_mul_f32 v5, v5, v37\n"
  "v_mfma_f32_16x16x32_bf16 v[20:23], v[80:83], v[172:175], v[20:23]\n"
  "v_mul_f32 v6, v6, v38\n"
  "s_waitcnt lgkmcnt(8)\n"
  "v_mfma_f32_16x16x32_bf16 v[24:27], v[64:67], v[168:171], v[24:27]\n"
  "ds_read_b64_tr_b16 v[80:81], v89 offset:28672\n"
  "ds_read_b64_tr_b16 v[82:83], v89 offset:29184\n"
  "v_mul_f32 v7, v7, v39\n"
  "v_mfma_f32_16x16x32_bf16 v[28:31], v[64:67], v[172:175], v[28:31]\n"
  "v_mul_f32 v12, v12, v44\n"
  "v_mul_f32 v13, v13, v45\n"
  "s_setprio 2\n"
  "s_waitcnt lgkmcnt(8)\n"
  "v_mfma_f32_16x16x32_bf16 v[16:19], v[68:71], v[176:179], v[16:19]\n"
  "ds_read_b64_tr_b16 v[64:65], v88 offset:32768\n"
  "ds_read_b64_tr_b16 v[66:67], v88 offset:33280\n"
  "v_mul_f32 v14, v14, v46\n"
  "v_mfma_f32_16x16x32_bf16 v[20:23], v[68:71], v[180:183], v[20:23]\n"
  "v_mul_f32 v15, v15, v47\n"
  "s_waitcnt lgkmcnt(8)\n"
  "v_mfma_f32_16x16x32_bf16 v[24:27], v[72:75], v[176:179], v[24:27]\n"
  "ds_read_b64_tr_b16 v[68:69], v89 offset:32768\n"
  "ds_read_b64_tr_b16 v[70:71], v89 offset:33280\n"
  "v_cvt_pk_bf16_f32 v36, v4, v5\n"
  "v_mfma_f32_16x16x32_bf16 v[28:31], v[72:75], v[180:183], v[28:31]\n"
  "v_cvt_pk_bf16_f32 v37, v6, v7\n"
  "v_cvt_pk_bf16_f32 v38, v12, v13\n"
  "s_waitcnt lgkmcnt(6)\n"
  "v_mfma_f32_16x16x32_bf16 v[16:19], v[76:79], v[184:187], v[16:19]\n"
  "ds_read_b64_tr_b16 v[72:73], v88 offset:36864\n"
  "ds_read_b64_tr_b16 v[74:75], v88 offset:37376\n"
  "s_mov_b32 s22, s76\n"
  "buffer_store_dwordx4 v[96:99], v100, s[20:23], s71 offen offset:1024 sc1\n"
  "s_mov_b32 s22, 39133312\n"
  "v_cvt_pk_bf16_f32 v39, v14, v15\n"
  "v_mfma_f32_16x16x32_bf16 v[20:23], v[76:79], v[188:191], v[20:23]\n"
  "v_mul_f32 v102, v48, v48\n"
  "s_waitcnt lgkmcnt(6)\n"
  "v_mfma_f32_16x16x32_bf16 v[24:27], v[80:83], v[184:187], v[24:27]\n"
  "ds_read_b64_tr_b16 v[76:77], v89 offset:36864\n"
  "ds_read_b64_tr_b16 v[78:79], v89 offset:37376\n"
  "v_mul_f32 v103, v49, v49\n"
  "v_mfma_f32_16x16x32_bf16 v[28:31], v[80:83], v[188:191], v[28:31]\n"
  "v_mul_f32 v104, v50, v50\n"
  "v_mul_f32 v105, v51, v51\n"
  "s_waitcnt lgkmcnt(6)\n"
  "v_mfma_f32_16x16x32_bf16 v[16:19], v[64:67], v[192:195], v[16:19]\n"
  "ds_read_b64_tr_b16 v[80:81], v88 offset:40960\n"
  "ds_read_b64_tr_b16 v[82:83], v88 offset:41472\n"
  "s_mov_b32 s43, 64\n"
  "s_add_u32 m0, s28, 131072\n"
  "s_nop 0\n"
  "buffer_load_dwordx4 v84, s[36:39], s43 offen lds\n"
  "s_waitcnt vmcnt(3)\n"
  "v_mov_b32 v87, s47\n"
  "s_mov_b64 exec, 1\n"
  "buffer_store_dword v87, off, s[20:23], s69 offset:128 sc1\n"
  "s_mov_b64 exec, -1\n"
  "v_fmaak_f32 v102, v102, v101, 0xc0135761\n"
  "v_mfma_f32_16x16x32_bf16 v[20:23], v[64:67], v[196:199], v[20:23]\n"
  "v_fmaak_f32 v103, v103, v101, 0xc0135761\n"
  "s_waitcnt lgkmcnt(6)\n"
  "v_mfma_f32_16x16x32_bf16 v[24:27], v[68:71], v[192:195], v[24:27]\n"
  "ds_read_b64_tr_b16 v[64:65], v89 offset:40960\n"
  "ds_read_b64_tr_b16 v[66:67], v89 offset:41472\n"
  "v_fmaak_f32 v104, v104, v101, 0xc0135761\n"
  "v_fmaak_f32 v105, v105, v101, 0xc0135761\n"
  "v_mfma_f32_16x16x32_bf16 v[28:31], v[68:71], v[196:199], v[28:31]\n"
  "v_mul_f32 v102, v102, v48\n"
  "s_waitcnt lgkmcnt(6)\n"
  "v_mfma_f32_16x16x32_bf16 v[16:19], v[72:75], v[200:203], v[16:19]\n"
  "ds_read_b64_tr_b16 v[68:69], v88 offset:45056\n"
  "ds_read_b64_tr_b16 v[70:71], v88 offset:45568\n"
  "s_add_u32 s43, s35, 64\n"
  "s_add_u32 m0, s28, 139264\n"
  "s_nop 0\n"
  "buffer_load_dwordx4 v84, s[36:39], s43 offen lds\n"
  "v_mul_f32 v103, v103, v49\n"
  "v_mfma_f32_16x16x32_bf16 v[20:23], v[72:75], v[204:207], v[20:23]\n"
  "v_mul_f32 v104, v104, v50\n"
  "s_waitcnt lgkmcnt(6)\n"
  "v_mfma_f32_16x16x32_bf16 v[24:27], v[76:79], v[200:203], v[24:27]\n"
  "ds_read_b64_tr_b16 v[72:73], v89 offset:45056\n"
  "ds_read_b64_tr_b16 v[74:75], v89 offset:45568\n"
  "v_mul_f32 v105, v105, v51\n"
  "v_exp_f32 v102, v102\n"
  "v_mfma_f32_16x16x32_bf16 v[28:31], v[76:79], v[204:207], v[28:31]\n"
  "v_exp_f32 v103, v103\n"
  "s_waitcnt lgkmcnt(6)\n"
  "v_mfma_f32_16x16x32_bf16 v[16:19], v[80:83], v[208:211], v[16:19]\n"
  "ds_read_b64_tr_b16 v[76:77], v88 offset:49152\n"
  "ds_read_b64_tr_b16 v[78:79], v88 offset:49664\n"
  "v_exp_f32 v104, v104\n"
  "v_mfma_f32_16x16x32_bf16 v[20:23], v[80:83], v[212:215], v[20:23]\n"
  "v_exp_f32 v105, v105\n"
  "s_waitcnt lgkmcnt(6)\n"
  "v_mfma_f32_16x16x32_bf16 v[24:27], v[64:67], v[208:211], v[24:27]\n"
  "ds_read_b64_tr_b16 v[80:81], v89 offset:49152\n"
  "ds_read_b64_tr_b16 v[82:83], v89 offset:49664\n"
  "v_add_f32 v102, 1.0, v102\n"
  "v_add_f32 v103, 1.0, v103\n"
  "v_mfma_f32_16x16x32_bf16 v[28:31], v[64:67], v[212:215], v[28:31]\n"
  "v_add_f32 v104, 1.0, v104\n"
  "s_waitcnt lgkmcnt(6)\n"
  "v_mfma_f32_16x16x32_bf16 v[16:19], v[68:71], v[216:219], v[16:19]\n"
  "ds_read_b64_tr_b16 v[64:65], v88 offset:53248\n"
  "ds_read_b64_tr_b16 v[66:67], v88 offset:53760\n"
  "v_add_f32 v105, 1.0, v105\n"
  "v_mfma_f32_16x16x32_bf16 v[20:23], v[68:71], v[220:223], v[20:23]\n"
  "v_rcp_f32 v102, v102\n"
  "s_waitcnt lgkmcnt(6)\n"
  "v_mfma_f32_16x16x32_bf16 v[24:27], v[72:75], v[216:219], v[24:27]\n"
  "ds_read_b64_tr_b16 v[68:69], v89 offset:53248\n"
  "ds_read_b64_tr_b16 v[70:71], v89 offset:53760\n"
  "v_rcp_f32 v103, v103\n"
  "v_rcp_f32 v104, v104\n"
  "v_mfma_f32_16x16x32_bf16 v[28:31], v[72:75], v[220:223], v[28:31]\n"
  "v_rcp_f32 v105, v105\n"
  "s_waitcnt lgkmcnt(6)\n"
  "v_mfma_f32_16x16x32_bf16 v[16:19], v[76:79], v[224:227], v[16:19]\n"
  "ds_read_b64_tr_b16 v[72:73], v88 offset:57344\n"
  "ds_read_b64_tr_b16 v[74:75], v88 offset:57856\n"
  "v_mul_f32 v48, v48, v102\n"
  "v_mfma_f32_16x16x32_bf16 v[20:23], v[76:79], v[228:231], v[20:23]\n"
  "v_mul_f32 v49, v49, v103\n"
  "v_mul_f32 v50, v50, v104\n"
  "s_waitcnt lgkmcnt(6)\n"
  "v_mfma_f32_16x16x32_bf16 v[24:27], v[80:83], v[224:227], v[24:27]\n"
  "ds_read_b64_tr_b16 v[76:77], v89 offset:57344\n"
  "ds_read_b64_tr_b16 v[78:79], v89 offset:57856\n"
  "v_mul_f32 v51, v51, v105\n"
  "v_mfma_f32_16x16x32_bf16 v[28:31], v[80:83], v[228:231], v[28:31]\n"
  "v_mul_f32 v106, v56, v56\n"
  "s_waitcnt lgkmcnt(6)\n"
  "v_mfma_f32_16x16x32_bf16 v[16:19], v[64:67], v[232:235], v[16:19]\n"
  "ds_read_b64_tr_b16 v[80:81], v88 offset:61440\n"
  "ds_read_b64_tr_b16 v[82:83], v88 offset:61952\n"
  "v_mul_f32 v107, v57, v57\n"
  "s_add_u32 s44, s30, 0\n"
  "buffer_store_dwordx4 v[32:35], v100, s[20:23], s44 offen offset:0 sc1\n"
  "v_mfma_f32_16x16x32_bf16 v[20:23], v[64:67], v[236:239], v[20:23]\n"
  "v_mul_f32 v108, v58, v58\n"
  "v_mul_f32 v109, v59, v59\n"
  "s_waitcnt lgkmcnt(6)\n"
  "v_mfma_f32_16x16x32_bf16 v[24:27], v[68:71], v[232:235], v[24:27]\n"
  "ds_read_b64_tr_b16 v[64:65], v89 offset:61440\n"
  "ds_read_b64_tr_b16 v[66:67], v89 offset:61952\n"
  "v_fmaak_f32 v106, v106, v101, 0xc0135761\n"
  "v_mfma_f32_16x16x32_bf16 v[28:31], v[68:71], v[236:239], v[28:31]\n"
  "v_fmaak_f32 v107, v107, v101, 0xc0135761\n"
  "s_waitcnt lgkmcnt(6)\n"
  "v_mfma_f32_16x16x32_bf16 v[16:19], v[72:75], v[240:243], v[16:19]\n"
  "ds_read_b64_tr_b16 v[68:69], v90 offset:24576\n"
  "ds_read_b64_tr_b16 v[70:71], v90 offset:25088\n"
  "v_fmaak_f32 v108, v108, v101, 0xc0135761\n"
  "v_mfma_f32_16x16x32_bf16 v[20:23], v[72:75], v[244:247], v[20:23]\n"
  "v_fmaak_f32 v109, v109, v101, 0xc0135761\n"
  "v_mul_f32 v106, v106, v56\n"
  "s_waitcnt lgkmcnt(6)\n"
  "v_mfma_f32_16x16x32_bf16 v[24:27], v[76:79], v[240:243], v[24:27]\n"
  "ds_read_b64_tr_b16 v[72:73], v91 offset:24576\n"
  "ds_read_b64_tr_b16 v[74:75], v91 offset:25088\n"
  "v_mul_f32 v107, v107, v57\n"
  "v_mfma_f32_16x16x32_bf16 v[28:31], v[76:79], v[244:247], v[28:31]\n"
  "v_mul_f32 v108, v108, v58\n"
  "s_waitcnt lgkmcnt(6)\n"
  "v_mfma_f32_16x16x32_bf16 v[16:19], v[80:83], v[248:251], v[16:19]\n"
  "ds_read_b64_tr_b16 v[76:77], v90 offset:28672\n"
  "ds_read_b64_tr_b16 v[78:79], v90 offset:29184\n"
  "v_mul_f32 v109, v109, v59\n"
  "v_mfma_f32_16x16x32_bf16 v[20:23], v[80:83], v[252:255], v[20:23]\n"
  "v_exp_f32 v106, v106\n"
  "v_exp_f32 v107, v107\n"
  "s_waitcnt lgkmcnt(6)\n"
  "v_mfma_f32_16x16x32_bf16 v[24:27], v[64:67], v[248:251], v[24:27]\n"
  "ds_read_b64_tr_b16 v[80:81], v91 offset:28672\n"
  "ds_read_b64_tr_b16 v[82:83], v91 offset:29184\n"
  "v_exp_f32 v108, v108\n"
  "s_setprio 1\n"
  "v_mfma_f32_16x16x32_bf16 v[28:31], v[64:67], v[252:255], v[28:31]\n"
  "v_exp_f32 v109, v109\n"
  "s_waitcnt lgkmcnt(6)\n"
  "v_mfma_f32_16x16x32_bf16 v[0:3], v[68:71], v[112:115], 0\n"
  "ds_read_b64_tr_b16 v[64:65], v90 offset:32768\n"
  "ds_read_b64_tr_b16 v[66:67], v90 offset:33280\n"
  "v_add_f32 v106, 1.0, v106\n"
  "v_add_f32 v107, 1.0, v107\n"
  "v_mfma_f32_16x16x32_bf16 v[4:7], v[68:71], v[116:119], 0\n"
  "v_add_f32 v108, 1.0, v108\n"
  "s_waitcnt lgkmcnt(6)\n"
  "v_mfma_f32_16x16x32_bf16 v[8:11], v[72:75], v[112:115], 0\n"
  "ds_read_b64_tr_b16 v[68:69], v91 offset:32768\n"
  "ds_read_b64_tr_b16 v[70:71], v91 offset:33280\n"
  "v_add_f32 v109, 1.0, v109\n"
  "v_mfma_f32_16x16x32_bf16 v[12:15], v[72:75], v[116:119], 0\n"
  "v_rcp_f32 v106, v106\n"
  "s_waitcnt lgkmcnt(6)\n"
  "v_mfma_f32_16x16x32_bf16 v[0:3], v[76:79], v[120:123], v[0:3]\n"
  "ds_read_b64_tr_b16 v[72:73], v90 offset:36864\n"
  "ds_read_b64_tr_b16 v[74:75], v90 offset:37376\n"
  "v_rcp_f32 v107, v107\n"
  "v_rcp_f32 v108, v108\n"
  "v_mfma_f32_16x16x32_bf16 v[4:7], v[76:79], v[124:127], v[4:7]\n"
  "v_rcp_f32 v109, v109\n"
  "s_waitcnt lgkmcnt(6)\n"
  "v_mfma_f32_16x16x32_bf16 v[8:11], v[80:83], v[120:123], v[8:11]\n"
  "ds_read_b64_tr_b16 v[76:77], v91 offset:36864\n"
  "ds_read_b64_tr_b16 v[78:79], v91 offset:37376\n"
  "v_mul_f32 v56, v56, v106\n"
  "v_mfma_f32_16x16x32_bf16 v[12:15], v[80:83], v[124:127], v[12:15]\n"
  "v_mul_f32 v57, v57, v107\n"
  "s_waitcnt lgkmcnt(6)\n"
  "v_mfma_f32_16x16x32_bf16 v[0:3], v[64:67], v[128:131], v[0:3]\n"
  "v_add_u32 v90, 0xfffe2000, v90\n"
  "ds_read_b64_tr_b16 v[80:81], v90 offset:0\n"
  "ds_read_b64_tr_b16 v[82:83], v90 offset:512\n"
  "v_mul_f32 v58, v58, v108\n"
  "v_mul_f32 v59, v59, v109\n"
  "v_mfma_f32_16x16x32_bf16 v[4:7], v[64:67], v[132:135], v[4:7]\n"
  "v_mul_f32 v102, v52, v52\n"
  "s_waitcnt lgkmcnt(6)\n"
  "v_mfma_f32_16x16x32_bf16 v[8:11], v[68:71], v[128:131], v[8:11]\n"
  "v_add_u32 v91, 0xfffe2000, v91\n"
  "ds_read_b64_tr_b16 v[64:65], v91 offset:0\n"
  "ds_read_b64_tr_b16 v[66:67], v91 offset:512\n"
  "v_mul_f32 v103, v53, v53\n"
  "v_mfma_f32_16x16x32_bf16 v[12:15], v[68:71], v[132:135], v[12:15]\n"
  "v_mul_f32 v104, v54, v54\n"
  "s_waitcnt lgkmcnt(6)\n"
  "v_mfma_f32_16x16x32_bf16 v[0:3], v[72:75], v[136:139], v[0:3]\n"
  "ds_read_b64_tr_b16 v[68:69], v90 offset:4096\n"
  "ds_read_b64_tr_b16 v[70:71], v90 offset:4608\n"
  "v_mul_f32 v105, v55, v55\n"
  "v_fmaak_f32 v102, v102, v101, 0xc0135761\n"
  "s_add_u32 s44, s30, 0\n"
  "buffer_store_dwordx4 v[36:39], v100, s[20:23], s44 offen offset:1024 sc1\n"
  "v_mfma_f32_16x16x32_bf16 v[4:7], v[72:75], v[140:143], v[4:7]\n"
  "v_fmaak_f32 v103, v103, v101, 0xc0135761\n"
  "s_waitcnt lgkmcnt(6)\n"
  "v_mfma_f32_16x16x32_bf16 v[8:11], v[76:79], v[136:139], v[8:11]\n"
  "ds_read_b64_tr_b16 v[72:73], v91 offset:4096\n"
  "ds_read_b64_tr_b16 v[74:75], v91 offset:4608\n"
  "v_fmaak_f32 v104, v104, v101, 0xc0135761\n"
  "v_mfma_f32_16x16x32_bf16 v[12:15], v[76:79], v[140:143], v[12:15]\n"
  "v_fmaak_f32 v105, v105, v101, 0xc0135761\n"
  "v_mul_f32 v102, v102, v52\n"
  "s_waitcnt lgkmcnt(6)\n"
  "v_mfma_f32_16x16x32_bf16 v[0:3], v[80:83], v[144:147], v[0:3]\n"
  "ds_read_b64_tr_b16 v[76:77], v90 offset:8192\n"
  "ds_read_b64_tr_b16 v[78:79], v90 offset:8704\n"
  "v_mul_f32 v103, v103, v53\n"
  "v_mfma_f32_16x16x32_bf16 v[4:7], v[80:83], v[148:151], v[4:7]\n"
  "v_mul_f32 v104, v104, v54\n"
  "s_waitcnt lgkmcnt(6)\n"
  "v_mfma_f32_16x16x32_bf16 v[8:11], v[64:67], v[144:147], v[8:11]\n"
  "ds_read_b64_tr_b16 v[80:81], v91 offset:8192\n"
  "ds_read_b64_tr_b16 v[82:83], v91 offset:8704\n"
  "v_mul_f32 v105, v105, v55\n"
  "v_mfma_f32_16x16x32_bf16 v[12:15], v[64:67], v[148:151], v[12:15]\n"
  "v_exp_f32 v102, v102\n"
  "v_exp_f32 v103, v103\n"
  "s_waitcnt lgkmcnt(6)\n"
  "v_mfma_f32_16x16x32_bf16 v[0:3], v[68:71], v[152:155], v[0:3]\n"
  "ds_read_b64_tr_b16 v[64:65], v90 offset:12288\n"
  "ds_read_b64_tr_b16 v[66:67], v90 offset:12800\n"
  "v_exp_f32 v104, v104\n"
  "v_mfma_f32_16x16x32_bf16 v[4:7], v[68:71], v[156:159], v[4:7]\n"
  "v_exp_f32 v105, v105\n"
  "s_waitcnt lgkmcnt(6)\n"
  "v_mfma_f32_16x16x32_bf16 v[8:11], v[72:75], v[152:155], v[8:11]\n"
  "ds_read_b64_tr_b16 v[68:69], v91 offset:12288\n"
  "ds_read_b64_tr_b16 v[70:71], v91 offset:12800\n"
  "v_add_f32 v102, 1.0, v102\n"
  "v_mfma_f32_16x16x32_bf16 v[12:15], v[72:75], v[156:159], v[12:15]\n"
  "v_add_f32 v103, 1.0, v103\n"
  "v_add_f32 v104, 1.0, v104\n"
  "s_waitcnt lgkmcnt(6)\n"
  "v_mfma_f32_16x16x32_bf16 v[0:3], v[76:79], v[160:163], v[0:3]\n"
  "ds_read_b64_tr_b16 v[72:73], v90 offset:16384\n"
  "ds_read_b64_tr_b16 v[74:75], v90 offset:16896\n"
  "v_add_f32 v105, 1.0, v105\n"
  "v_mfma_f32_16x16x32_bf16 v[4:7], v[76:79], v[164:167], v[4:7]\n"
  "v_rcp_f32 v102, v102\n"
  "s_waitcnt lgkmcnt(6)\n"
  "v_mfma_f32_16x16x32_bf16 v[8:11], v[80:83], v[160:163], v[8:11]\n"
  "ds_read_b64_tr_b16 v[76:77], v91 offset:16384\n"
  "ds_read_b64_tr_b16 v[78:79], v91 offset:16896\n"
  "v_rcp_f32 v103, v103\n"
  "v_mfma_f32_16x16x32_bf16 v[12:15], v[80:83], v[164:167], v[12:15]\n"
  "s_waitcnt vmcnt(2)\n"
  "ds_read_b64_tr_b16 v[96:97], v86 offset:0\n"
  "ds_read_b64_tr_b16 v[98:99], v86 offset:8192\n"
  "v_rcp_f32 v104, v104\n"
  "v_rcp_f32 v105, v105\n"
  "s_waitcnt lgkmcnt(8)\n"
  "v_mfma_f32_16x16x32_bf16 v[0:3], v[64:67], v[168:171], v[0:3]\n"
  "ds_read_b64_tr_b16 v[80:81], v90 offset:20480\n"
  "ds_read_b64_tr_b16 v[82:83], v90 offset:20992\n"
  "v_mul_f32 v52, v52, v102\n"
  "v_mfma_f32_16x16x32_bf16 v[4:7], v[64:67], v[172:175], v[4:7]\n"
  "v_mul_f32 v53, v53, v103\n"
  "s_waitcnt lgkmcnt(8)\n"
  "v_mfma_f32_16x16x32_bf16 v[8:11], v[68:71], v[168:171], v[8:11]\n"
  "ds_read_b64_tr_b16 v[64:65], v91 offset:20480\n"
  "ds_read_b64_tr_b16 v[66:67], v91 offset:20992\n"
  "v_mul_f32 v54, v54, v104\n"
  "v_mul_f32 v55, v55, v105\n"
  "v_mfma_f32_16x16x32_bf16 v[12:15], v[68:71], v[172:175], v[12:15]\n"
  "v_mul_f32 v106, v60, v60\n"
  "s_waitcnt lgkmcnt(8)\n"
  "v_mfma_f32_16x16x32_bf16 v[0:3], v[72:75], v[176:179], v[0:3]\n"
  "ds_read_b64_tr_b16 v[68:69], v90 offset:24576\n"
  "ds_read_b64_tr_b16 v[70:71], v90 offset:25088\n"
  "v_mul_f32 v107, v61, v61\n"
  "v_mfma_f32_16x16x32_bf16 v[4:7], v[72:75], v[180:183], v[4:7]\n"
  "v_mul_f32 v108, v62, v62\n"
  "s_waitcnt lgkmcnt(8)\n"
  "v_mfma_f32_16x16x32_bf16 v[8:11], v[76:79], v[176:179], v[8:11]\n"
  "ds_read_b64_tr_b16 v[72:73], v91 offset:24576\n"
  "ds_read_b64_tr_b16 v[74:75], v91 offset:25088\n"
  "v_mul_f32 v109, v63, v63\n"
  "v_fmaak_f32 v106, v106, v101, 0xc0135761\n"
  "v_mfma_f32_16x16x32_bf16 v[12:15], v[76:79], v[180:183], v[12:15]\n"
  "v_fmaak_f32 v107, v107, v101, 0xc0135761\n"
  "s_waitcnt lgkmcnt(6)\n"
  "v_mfma_f32_16x16x32_bf16 v[0:3], v[80:83], v[184:187], v[0:3]\n"
  "ds_read_b64_tr_b16 v[76:77], v90 offset:28672\n"
  "ds_read_b64_tr_b16 v[78:79], v90 offset:29184\n"
  "s_mov_b32 s22, s76\n"
  "buffer_store_dwordx4 v[96:99], v100, s[20:23], s71 offen offset:2048 sc1\n"
  "s_mov_b32 s22, 39133312\n"
  "v_fmaak_f32 v108, v108, v101, 0xc0135761\n"
  "v_mfma_f32_16x16x32_bf16 v[4:7], v[80:83], v[188:191], v[4:7]\n"
  "v_fmaak_f32 v109, v109, v101, 0xc0135761\n"
  "s_setprio 0\n"
  "s_waitcnt lgkmcnt(6)\n"
  "v_mfma_f32_16x16x32_bf16 v[8:11], v[64:67], v[184:187], v[8:11]\n"
  "ds_read_b64_tr_b16 v[80:81], v91 offset:28672\n"
  "ds_read_b64_tr_b16 v[82:83], v91 offset:29184\n"
  "v_mul_f32 v106, v106, v60\n"
  "v_mul_f32 v107, v107, v61\n"
  "v_mfma_f32_16x16x32_bf16 v[12:15], v[64:67], v[188:191], v[12:15]\n"
  "v_mul_f32 v108, v108, v62\n"
  "s_waitcnt lgkmcnt(6)\n"
  "v_mfma_f32_16x16x32_bf16 v[0:3], v[68:71], v[192:195], v[0:3]\n"
  "ds_read_b64_tr_b16 v[64:65], v90 offset:32768\n"
  "ds_read_b64_tr_b16 v[66:67], v90 offset:33280\n"
  "v_mul_f32 v109, v109, v63\n"
  "v_mfma_f32_16x16x32_bf16 v[4:7], v[68:71], v[196:199], v[4:7]\n"
  "v_exp_f32 v106, v106\n"
  "s_waitcnt lgkmcnt(6)\n"
  "v_mfma_f32_16x16x32_bf16 v[8:11], v[72:75], v[192:195], v[8:11]\n"
  "ds_read_b64_tr_b16 v[68:69], v91 offset:32768\n"
  "ds_read_b64_tr_b16 v[70:71], v91 offset:33280\n"
  "v_exp_f32 v107, v107\n"
  "v_exp_f32 v108, v108\n"
  "v_mfma_f32_16x16x32_bf16 v[12:15], v[72:75], v[196:199], v[12:15]\n"
  "v_exp_f32 v109, v109\n"
  "s_waitcnt lgkmcnt(6)\n"
  "v_mfma_f32_16x16x32_bf16 v[0:3], v[76:79], v[200:203], v[0:3]\n"
  "ds_read_b64_tr_b16 v[72:73], v90 offset:36864\n"
  "ds_read_b64_tr_b16 v[74:75], v90 offset:37376\n"
  "v_add_f32 v106, 1.0, v106\n"
  "v_mfma_f32_16x16x32_bf16 v[4:7], v[76:79], v[204:207], v[4:7]\n"
  "v_add_f32 v107, 1.0, v107\n"
  "v_add_f32 v108, 1.0, v108\n"
  "s_waitcnt lgkmcnt(6)\n"
  "v_mfma_f32_16x16x32_bf16 v[8:11], v[80:83], v[200:203], v[8:11]\n"
  "ds_read_b64_tr_b16 v[76:77], v91 offset:36864\n"
  "ds_read_b64_tr_b16 v[78:79], v91 offset:37376\n"
  "v_add_f32 v109, 1.0, v109\n"
  "v_mfma_f32_16x16x32_bf16 v[12:15], v[80:83], v[204:207], v[12:15]\n"
  "v_rcp_f32 v106, v106\n"
  "s_waitcnt lgkmcnt(6)\n"
  "v_mfma_f32_16x16x32_bf16 v[0:3], v[64:67], v[208:211], v[0:3]\n"
  "ds_read_b64_tr_b16 v[80:81], v90 offset:40960\n"
  "ds_read_b64_tr_b16 v[82:83], v90 offset:41472\n"
  "v_rcp_f32 v107, v107\n"
  "v_mfma_f32_16x16x32_bf16 v[4:7], v[64:67], v[212:215], v[4:7]\n"
  "v_rcp_f32 v108, v108\n"
  "v_rcp_f32 v109, v109\n"
  "s_waitcnt lgkmcnt(6)\n"
  "v_mfma_f32_16x16x32_bf16 v[8:11], v[68:71], v[208:211], v[8:11]\n"
  "ds_read_b64_tr_b16 v[64:65], v91 offset:40960\n"
  "ds_read_b64_tr_b16 v[66:67], v91 offset:41472\n"
  "v_mul_f32 v60, v60, v106\n"
  "v_mfma_f32_16x16x32_bf16 v[12:15], v[68:71], v[212:215], v[12:15]\n"
  "ds_read_b64_tr_b16 v[96:97], v86 offset:8\n"
  "ds_read_b64_tr_b16 v[98:99], v86 offset:8200\n"
  "v_mul_f32 v61, v61, v107\n"
  "s_waitcnt lgkmcnt(8)\n"
  "v_mfma_f32_16x16x32_bf16 v[0:3], v[72:75], v[216:219], v[0:3]\n"
  "ds_read_b64_tr_b16 v[68:69], v90 offset:45056\n"
  "ds_read_b64_tr_b16 v[70:71], v90 offset:45568\n"
  "v_mul_f32 v62, v62, v108\n"
  "v_mfma_f32_16x16x32_bf16 v[4:7], v[72:75], v[220:223], v[4:7]\n"
  "v_mul_f32 v63, v63, v109\n"
  "v_mul_f32 v102, v16, v16\n"
  "s_waitcnt lgkmcnt(8)\n"
  "v_mfma_f32_16x16x32_bf16 v[8:11], v[76:79], v[216:219], v[8:11]\n"
  "ds_read_b64_tr_b16 v[72:73], v91 offset:45056\n"
  "ds_read_b64_tr_b16 v[74:75], v91 offset:45568\n"
  "v_mul_f32 v103, v17, v17\n"
  "v_mfma_f32_16x16x32_bf16 v[12:15], v[76:79], v[220:223], v[12:15]\n"
  "v_mul_f32 v104, v18, v18\n"
  "s_waitcnt lgkmcnt(8)\n"
  "v_mfma_f32_16x16x32_bf16 v[0:3], v[80:83], v[224:227], v[0:3]\n"
  "ds_read_b64_tr_b16 v[76:77], v90 offset:49152\n"
  "ds_read_b64_tr_b16 v[78:79], v90 offset:49664\n"
  "v_mul_f32 v105, v19, v19\n"
  "v_mfma_f32_16x16x32_bf16 v[4:7], v[80:83], v[228:231], v[4:7]\n"
  "v_fmaak_f32 v102, v102, v101, 0xc0135761\n"
  "v_fmaak_f32 v103, v103, v101, 0xc0135761\n"
  "s_waitcnt lgkmcnt(8)\n"
  "v_mfma_f32_16x16x32_bf16 v[8:11], v[64:67], v[224:227], v[8:11]\n"
  "ds_read_b64_tr_b16 v[80:81], v91 offset:49152\n"
  "ds_read_b64_tr_b16 v[82:83], v91 offset:49664\n"
  "v_fmaak_f32 v104, v104, v101, 0xc0135761\n"
  "v_mfma_f32_16x16x32_bf16 v[12:15], v[64:67], v[228:231], v[12:15]\n"
  "v_fmaak_f32 v105, v105, v101, 0xc0135761\n"
  "s_waitcnt lgkmcnt(6)\n"
  "v_mfma_f32_16x16x32_bf16 v[0:3], v[68:71], v[232:235], v[0:3]\n"
  "ds_read_b64_tr_b16 v[64:65], v90 offset:53248\n"
  "ds_read_b64_tr_b16 v[66:67], v90 offset:53760\n"
  "s_mov_b32 s22, s76\n"
  "buffer_store_dwordx4 v[96:99], v100, s[20:23], s71 offen offset:3072 sc1\n"
  "s_mov_b32 s22, 39133312\n"
  "s_mov_b64 s[36:37], s[20:21]\n"
  "v_mul_f32 v102, v102, v16\n"
  "v_mul_f32 v103, v103, v17\n"
  "v_mfma_f32_16x16x32_bf16 v[4:7], v[68:71], v[236:239], v[4:7]\n"
  "v_mul_f32 v104, v104, v18\n"
  "s_waitcnt lgkmcnt(6)\n"
  "v_mfma_f32_16x16x32_bf16 v[8:11], v[72:75], v[232:235], v[8:11]\n"
  "ds_read_b64_tr_b16 v[68:69], v91 offset:53248\n"
  "ds_read_b64_tr_b16 v[70:71], v91 offset:53760\n"
  "v_mul_f32 v105, v105, v19\n"
  "v_mfma_f32_16x16x32_bf16 v[12:15], v[72:75], v[236:239], v[12:15]\n"
  "v_exp_f32 v102, v102\n"
  "s_waitcnt lgkmcnt(6)\n"
  "v_mfma_f32_16x16x32_bf16 v[0:3], v[76:79], v[240:243], v[0:3]\n"
  "ds_read_b64_tr_b16 v[72:73], v90 offset:57344\n"
  "ds_read_b64_tr_b16 v[74:75], v90 offset:57856\n"
  "v_exp_f32 v103, v103\n"
  "v_exp_f32 v104, v104\n"
  "v_mfma_f32_16x16x32_bf16 v[4:7], v[76:79], v[244:247], v[4:7]\n"
  "v_exp_f32 v105, v105\n"
  "s_waitcnt lgkmcnt(6)\n"
  "v_mfma_f32_16x16x32_bf16 v[8:11], v[80:83], v[240:243], v[8:11]\n"
  "ds_read_b64_tr_b16 v[76:77], v91 offset:57344\n"
  "ds_read_b64_tr_b16 v[78:79], v91 offset:57856\n"
  "s_add_u32 m0, s28, 131072\n"
  "s_mov_b32 s43, 128\n"
  "buffer_load_dwordx4 v94, s[16:19], s43 offen lds\n"
  "v_add_f32 v102, 1.0, v102\n"
  "v_mfma_f32_16x16x32_bf16 v[12:15], v[80:83], v[244:247], v[12:15]\n"
  "v_add_f32 v103, 1.0, v103\n"
  "s_waitcnt lgkmcnt(6)\n"
  "v_mfma_f32_16x16x32_bf16 v[0:3], v[64:67], v[248:251], v[0:3]\n"
  "ds_read_b64_tr_b16 v[80:81], v90 offset:61440\n"
  "ds_read_b64_tr_b16 v[82:83], v90 offset:61952\n"
  "v_add_f32 v104, 1.0, v104\n"
  "v_add_f32 v105, 1.0, v105\n"
  "v_mfma_f32_16x16x32_bf16 v[4:7], v[64:67], v[252:255], v[4:7]\n"
  "v_rcp_f32 v102, v102\n"
  "s_waitcnt lgkmcnt(6)\n"
  "v_mfma_f32_16x16x32_bf16 v[8:11], v[68:71], v[248:251], v[8:11]\n"
  "ds_read_b64_tr_b16 v[64:65], v91 offset:61440\n"
  "ds_read_b64_tr_b16 v[66:67], v91 offset:61952\n"
  "v_rcp_f32 v103, v103\n"
  "v_mfma_f32_16x16x32_bf16 v[12:15], v[68:71], v[252:255], v[12:15]\n"
  "v_rcp_f32 v104, v104\n"
  "s_waitcnt lgkmcnt(6)\n"
  "v_mfma_f32_16x16x32_bf16 v[32:35], v[72:75], v[112:115], 0\n"
  "v_add_u32 v90, 0x10000, v90\n"
  "ds_read_b64_tr_b16 v[68:69], v90 offset:0\n"
  "ds_read_b64_tr_b16 v[70:71], v90 offset:512\n"
  "s_add_u32 m0, s28, 139264\n"
  "s_mov_b32 s43, 589952\n"
  "buffer_load_dwordx4 v94, s[16:19], s43 offen lds\n"
  "v_rcp_f32 v105, v105\n"
  "v_mul_f32 v16, v16, v102\n"
  "v_mfma_f32_16x16x32_bf16 v[36:39], v[72:75], v[116:119], 0\n"
  "v_mul_f32 v17, v17, v103\n"
  "s_waitcnt lgkmcnt(6)\n"
  "v_mfma_f32_16x16x32_bf16 v[40:43], v[76:79], v[112:115], 0\n"
  "v_add_u32 v91, 0x10000, v91\n"
  "ds_read_b64_tr_b16 v[72:73], v91 offset:0\n"
  "ds_read_b64_tr_b16 v[74:75], v91 offset:512\n"
  "v_mul_f32 v18, v18, v104\n"
  "v_mfma_f32_16x16x32_bf16 v[44:47], v[76:79], v[116:119], 0\n"
  "v_mul_f32 v19, v19, v105\n"
  "s_waitcnt lgkmcnt(6)\n"
  "v_mfma_f32_16x16x32_bf16 v[32:35], v[80:83], v[120:123], v[32:35]\n"
  "s_barrier\n"
  "ds_read_b64_tr_b16 v[76:77], v90 offset:4096\n"
  "ds_read_b64_tr_b16 v[78:79], v90 offset:4608\n"
  "v_mul_f32 v0, v0, v48\n"
  "v_mul_f32 v1, v1, v49\n"
  "s_setprio 3\n"
  "v_mfma_f32_16x16x32_bf16 v[36:39], v[80:83], v[124:127], v[36:39]\n"
  "s_add_u32 m0, s28, 147456\n"
  "s_mov_b32 s43, 1179776\n"
  "buffer_load_dwordx4 v94, s[16:19], s43 offen lds\n"
  "v_mul_f32 v2, v2, v50\n"
  "s_waitcnt lgkmcnt(6)\n"
  "v_mfma_f32_16x16x32_bf16 v[40:43], v[64:67], v[120:123], v[40:43]\n"
  "ds_read_b64_tr_b16 v[80:81], v91 offset:4096\n"
  "ds_read_b64_tr_b16 v[82:83], v91 offset:4608\n"
  "v_mul_f32 v3, v3, v51\n"
  "v_mfma_f32_16x16x32_bf16 v[44:47], v[64:67], v[124:127], v[44:47]\n"
  "v_mul_f32 v8, v8, v56\n"
  "s_waitcnt lgkmcnt(6)\n"
  "v_mfma_f32_16x16x32_bf16 v[32:35], v[68:71], v[128:131], v[32:35]\n"
  "ds_read_b64_tr_b16 v[64:65], v90 offset:8192\n"
  "ds_read_b64_tr_b16 v[66:67], v90 offset:8704\n"
  "v_mul_f32 v9, v9, v57\n"
  "v_mul_f32 v10, v10, v58\n"
  "v_mfma_f32_16x16x32_bf16 v[36:39], v[68:71], v[132:135], v[36:39]\n"
  "v_mul_f32 v11, v11, v59\n"
  "s_waitcnt lgkmcnt(6)\n"
  "v_mfma_f32_16x16x32_bf16 v[40:43], v[72:75], v[128:131], v[40:43]\n"
  "ds_read_b64_tr_b16 v[68:69], v91 offset:8192\n"
  "ds_read_b64_tr_b16 v[70:71], v91 offset:8704\n"
  "v_cvt_pk_bf16_f32 v48, v0, v1\n"
  "v_mfma_f32_16x16x32_bf16 v[44:47], v[72:75], v[132:135], v[44:47]\n"
  "v_cvt_pk_bf16_f32 v49, v2, v3\n"
  "s_waitcnt lgkmcnt(6)\n"
  "v_mfma_f32_16x16x32_bf16 v[32:35], v[76:79], v[136:139], v[32:35]\n"
  "ds_read_b64_tr_b16 v[72:73], v90 offset:12288\n"
  "ds_read_b64_tr_b16 v[74:75], v90 offset:12800\n"
  "s_add_u32 m0, s28, 155648\n"
  "s_mov_b32 s43, 1769600\n"
  "buffer_load_dwordx4 v94, s[16:19], s43 offen lds\n"
  "v_cvt_pk_bf16_f32 v50, v8, v9\n"
  "v_mfma_f32_16x16x32_bf16 v[36:39], v[76:79], v[140:143], v[36:39]\n"
  "v_cvt_pk_bf16_f32 v51, v10, v11\n"
  "v_mul_f32 v4, v4, v52\n"
  "s_waitcnt lgkmcnt(6)\n"
  "v_mfma_f32_16x16x32_bf16 v[40:43], v[80:83], v[136:139], v[40:43]\n"
  "ds_read_b64_tr_b16 v[76:77], v91 offset:12288\n"
  "ds_read_b64_tr_b16 v[78:79], v91 offset:12800\n"
  "v_mul_f32 v5, v5, v53\n"
  "v_mfma_f32_16x16x32_bf16 v[44:47], v[80:83], v[140:143], v[44:47]\n"
  "v_mul_f32 v6, v6, v54\n"
  "s_waitcnt lgkmcnt(6)\n"
  "v_mfma_f32_16x16x32_bf16 v[32:35], v[64:67], v[144:147], v[32:35]\n"
  "ds_read_b64_tr_b16 v[80:81], v90 offset:16384\n"
  "ds_read_b64_tr_b16 v[82:83], v90 offset:16896\n"
  "v_mul_f32 v7, v7, v55\n"
  "s_add_u32 s44, s30, 4096\n"
  "buffer_store_dwordx4 v[48:51], v100, s[20:23], s44 offen offset:0 sc1\n"
  "v_mfma_f32_16x16x32_bf16 v[36:39], v[64:67], v[148:151], v[36:39]\n"
  "v_mul_f32 v12, v12, v60\n"
  "v_mul_f32 v13, v13, v61\n"
  "s_waitcnt lgkmcnt(6)\n"
  "v_mfma_f32_16x16x32_bf16 v[40:43], v[68:71], v[144:147], v[40:43]\n"
  "ds_read_b64_tr_b16 v[64:65], v91 offset:16384\n"
  "ds_read_b64_tr_b16 v[66:67], v91 offset:16896\n"
  "v_mul_f32 v14, v14, v62\n"
  "v_mfma_f32_16x16x32_bf16 v[44:47], v[68:71], v[148:151], v[44:47]\n"
  "v_mul_f32 v15, v15, v63\n"
  "s_setprio 2\n"
  "s_waitcnt lgkmcnt(6)\n"
  "v_mfma_f32_16x16x32_bf16 v[32:35], v[72:75], v[152:155], v[32:35]\n"
  "ds_read_b64_tr_b16 v[68:69], v90 offset:20480\n"
  "ds_read_b64_tr_b16 v[70:71], v90 offset:20992\n"
  "s_add_u32 m0, s28, 40960\n"
  "s_mov_b32 s43, 2359424\n"
  "buffer_load_dwordx4 v94, s[16:19], s43 offen lds\n"
  "v_cvt_pk_bf16_f32 v52, v4, v5\n"
  "v_and_b32 v96, 0xffff, v110\n"
  "v_mfma_f32_16x16x32_bf16 v[36:39], v[72:75], v[156:159], v[36:39]\n"
  "v_cvt_pk_bf16_f32 v53, v6, v7\n"
  "s_waitcnt lgkmcnt(6)\n"
  "v_mfma_f32_16x16x32_bf16 v[40:43], v[76:79], v[152:155], v[40:43]\n"
  "ds_read_b64_tr_b16 v[72:73], v91 offset:20480\n"
  "ds_read_b64_tr_b16 v[74:75], v91 offset:20992\n"
  "v_cvt_pk_bf16_f32 v54, v12, v13\n"
  "v_cvt_pk_bf16_f32 v55, v14, v15\n"
  "v_lshlrev_b32 v96, 2, v96\n"
  "v_mfma_f32_16x16x32_bf16 v[44:47], v[76:79], v[156:159], v[44:47]\n"
  "v_mul_f32 v106, v24, v24\n"
  "s_waitcnt lgkmcnt(6)\n"
  "v_mfma_f32_16x16x32_bf16 v[32:35], v[80:83], v[160:163], v[32:35]\n"
  "ds_read_b64_tr_b16 v[76:77], v90 offset:24576\n"
  "ds_read_b64_tr_b16 v[78:79], v90 offset:25088\n"
  "v_mul_f32 v107, v25, v25\n"
  "v_lshrrev_b32 v98, 16, v110\n"
  "v_mfma_f32_16x16x32_bf16 v[36:39], v[80:83], v[164:167], v[36:39]\n"
  "v_mul_f32 v108, v26, v26\n"
  "s_waitcnt lgkmcnt(6)\n"
  "v_mfma_f32_16x16x32_bf16 v[40:43], v[64:67], v[160:163], v[40:43]\n"
  "ds_read_b64_tr_b16 v[80:81], v91 offset:24576\n"
  "ds_read_b64_tr_b16 v[82:83], v91 offset:25088\n"
  "v_mul_f32 v109, v27, v27\n"
  "v_fmaak_f32 v106, v106, v101, 0xc0135761\n"
  "v_lshlrev_b32 v98, 2, v98\n"
  "v_mfma_f32_16x16x32_bf16 v[44:47], v[64:67], v[164:167], v[44:47]\n"
  "v_fmaak_f32 v107, v107, v101, 0xc0135761\n"
  "s_waitcnt lgkmcnt(6)\n"
  "v_mfma_f32_16x16x32_bf16 v[32:35], v[68:71], v[168:171], v[32:35]\n"
  "ds_read_b64_tr_b16 v[64:65], v90 offset:28672\n"
  "ds_read_b64_tr_b16 v[66:67], v90 offset:29184\n"
  "s_add_u32 m0, s28, 49152\n"
  "s_mov_b32 s43, 2949248\n"
  "buffer_load_dwordx4 v94, s[16:19], s43 offen lds\n"
  "v_fmaak_f32 v108, v108, v101, 0xc0135761\n"
  "v_mfma_f32_16x16x32_bf16 v[36:39], v[68:71], v[172:175], v[36:39]\n"
  "v_fmaak_f32 v109, v109, v101, 0xc0135761\n"
  "s_waitcnt lgkmcnt(6)\n"
  "v_mfma_f32_16x16x32_bf16 v[40:43], v[72:75], v[168:171], v[40:43]\n"
  "ds_read_b64_tr_b16 v[68:69], v91 offset:28672\n"
  "ds_read_b64_tr_b16 v[70:71], v91 offset:29184\n"
  "v_mul_f32 v106, v106, v24\n"
  "v_mfma_f32_16x16x32_bf16 v[44:47], v[72:75], v[172:175], v[44:47]\n"
  "v_mul_f32 v107, v107, v25\n"
  "v_mul_f32 v108, v108, v26\n"
  "s_waitcnt lgkmcnt(6)\n"
  "v_mfma_f32_16x16x32_bf16 v[32:35], v[76:79], v[176:179], v[32:35]\n"
  "ds_read_b64_tr_b16 v[72:73], v90 offset:32768\n"
  "ds_read_b64_tr_b16 v[74:75], v90 offset:33280\n"
  "v_mul_f32 v109, v109, v27\n"
  "v_mfma_f32_16x16x32_bf16 v[36:39], v[76:79], v[180:183], v[36:39]\n"
  "v_exp_f32 v106, v106\n"
  "s_waitcnt lgkmcnt(6)\n"
  "v_mfma_f32_16x16x32_bf16 v[40:43], v[80:83], v[176:179], v[40:43]\n"
  "ds_read_b64_tr_b16 v[76:77], v91 offset:32768\n"
  "ds_read_b64_tr_b16 v[78:79], v91 offset:33280\n"
  "v_exp_f32 v107, v107\n"
  "s_setprio 1\n"
  "v_mfma_f32_16x16x32_bf16 v[44:47], v[80:83], v[180:183], v[44:47]\n"
  "v_exp_f32 v108, v108\n"
  "v_exp_f32 v109, v109\n"
  "s_waitcnt lgkmcnt(6)\n"
  "v_mfma_f32_16x16x32_bf16 v[32:35], v[64:67], v[184:187], v[32:35]\n"
  "ds_read_b64_tr_b16 v[80:81], v90 offset:36864\n"
  "ds_read_b64_tr_b16 v[82:83], v90 offset:37376\n"
  "v_add_f32 v106, 1.0, v106\n"
  "v_mfma_f32_16x16x32_bf16 v[36:39], v[64:67], v[188:191], v[36:39]\n"
  "v_add_f32 v107, 1.0, v107\n"
  "s_waitcnt lgkmcnt(6)\n"
  "v_mfma_f32_16x16x32_bf16 v[40:43], v[68:71], v[184:187], v[40:43]\n"
  "ds_read_b64_tr_b16 v[64:65], v91 offset:36864\n"
  "ds_read_b64_tr_b16 v[66:67], v91 offset:37376\n"
  "v_add_f32 v108, 1.0, v108\n"
  "v_mfma_f32_16x16x32_bf16 v[44:47], v[68:71], v[188:191], v[44:47]\n"
  "v_add_f32 v109, 1.0, v109\n"
  "s_waitcnt lgkmcnt(6)\n"
  "v_mfma_f32_16x16x32_bf16 v[32:35], v[72:75], v[192:195], v[32:35]\n"
  "ds_read_b64_tr_b16 v[68:69], v90 offset:40960\n"
  "ds_read_b64_tr_b16 v[70:71], v90 offset:41472\n"
  "s_add_u32 m0, s28, 0\n"
  "s_add_u32 s43, s41, 0\n"
  "buffer_load_dwordx4 v95, s[12:15], s43 offen lds\n"
  "v_rcp_f32 v106, v106\n"
  "v_rcp_f32 v107, v107\n"
  "v_mfma_f32_16x16x32_bf16 v[36:39], v[72:75], v[196:199], v[36:39]\n"
  "v_rcp_f32 v108, v108\n"
  "s_waitcnt lgkmcnt(6)\n"
  "v_mfma_f32_16x16x32_bf16 v[40:43], v[76:79], v[192:195], v[40:43]\n"
  "ds_read_b64_tr_b16 v[72:73], v91 offset:40960\n"
  "ds_read_b64_tr_b16 v[74:75], v91 offset:41472\n"
  "v_rcp_f32 v109, v109\n"
  "v_mfma_f32_16x16x32_bf16 v[44:47], v[76:79], v[196:199], v[44:47]\n"
  "v_mul_f32 v24, v24, v106\n"
  "s_waitcnt lgkmcnt(6)\n"
  "v_mfma_f32_16x16x32_bf16 v[32:35], v[80:83], v[200:203], v[32:35]\n"
  "ds_read_b64_tr_b16 v[76:77], v90 offset:45056\n"
  "ds_read_b64_tr_b16 v[78:79], v90 offset:45568\n"
  "v_mul_f32 v25, v25, v107\n"
  "v_mul_f32 v26, v26, v108\n"
  "s_add_u32 s44, s30, 4096\n"
  "buffer_store_dwordx4 v[52:55], v100, s[20:23], s44 offen offset:1024 sc1\n"
  "v_mfma_f32_16x16x32_bf16 v[36:39], v[80:83], v[204:207], v[36:39]\n"
  "v_mul_f32 v27, v27, v109\n"
  "s_waitcnt lgkmcnt(6)\n"
  "v_mfma_f32_16x16x32_bf16 v[40:43], v[64:67], v[200:203], v[40:43]\n"
  "ds_read_b64_tr_b16 v[80:81], v91 offset:45056\n"
  "ds_read_b64_tr_b16 v[82:83], v91 offset:45568\n"
  "v_mul_f32 v102, v20, v20\n"
  "buffer_load_dword v97, v96, s[20:23], s80 offen sc1\n"
  "v_mfma_f32_16x16x32_bf16 v[44:47], v[64:67], v[204:207], v[44:47]\n"
  "v_mul_f32 v103, v21, v21\n"
  "s_waitcnt lgkmcnt(6)\n"
  "v_mfma_f32_16x16x32_bf16 v[32:35], v[68:71], v[208:211], v[32:35]\n"
  "ds_read_b64_tr_b16 v[64:65], v90 offset:49152\n"
  "ds_read_b64_tr_b16 v[66:67], v90 offset:49664\n"
  "v_mul_f32 v104, v22, v22\n"
  "v_mfma_f32_16x16x32_bf16 v[36:39], v[68:71], v[212:215], v[36:39]\n"
  "v_mul_f32 v105, v23, v23\n"
  "v_fmaak_f32 v102, v102, v101, 0xc0135761\n"
  "s_setprio 0\n"
  "s_waitcnt lgkmcnt(6)\n"
  "v_mfma_f32_16x16x32_bf16 v[40:43], v[72:75], v[208:211], v[40:43]\n"
  "ds_read_b64_tr_b16 v[68:69], v91 offset:49152\n"
  "ds_read_b64_tr_b16 v[70:71], v91 offset:49664\n"
  "v_fmaak_f32 v103, v103, v101, 0xc0135761\n"
  "v_mfma_f32_16x16x32_bf16 v[44:47], v[72:75], v[212:215], v[44:47]\n"
  "v_fmaak_f32 v104, v104, v101, 0xc0135761\n"
  "s_waitcnt lgkmcnt(6)\n"
  "v_mfma_f32_16x16x32_bf16 v[32:35], v[76:79], v[216:219], v[32:35]\n"
  "ds_read_b64_tr_b16 v[72:73], v90 offset:53248\n"
  "ds_read_b64_tr_b16 v[74:75], v90 offset:53760\n"
  "v_fmaak_f32 v105, v105, v101, 0xc0135761\n"
  "v_mfma_f32_16x16x32_bf16 v[36:39], v[76:79], v[220:223], v[36:39]\n"
  "v_mul_f32 v102, v102, v20\n"
  "v_mul_f32 v103, v103, v21\n"
  "s_waitcnt lgkmcnt(6)\n"
  "v_mfma_f32_16x16x32_bf16 v[40:43], v[80:83], v[216:219], v[40:43]\n"
  "ds_read_b64_tr_b16 v[76:77], v91 offset:53248\n"
  "ds_read_b64_tr_b16 v[78:79], v91 offset:53760\n"
  "v_mul_f32 v104, v104, v22\n"
  "v_mfma_f32_16x16x32_bf16 v[44:47], v[80:83], v[220:223], v[44:47]\n"
  "v_mul_f32 v105, v105, v23\n"
  "s_waitcnt lgkmcnt(6)\n"
  "v_mfma_f32_16x16x32_bf16 v[32:35], v[64:67], v[224:227], v[32:35]\n"
  "ds_read_b64_tr_b16 v[80:81], v90 offset:57344\n"
  "ds_read_b64_tr_b16 v[82:83], v90 offset:57856\n"
  "v_exp_f32 v102, v102\n"
  "v_mfma_f32_16x16x32_bf16 v[36:39], v[64:67], v[228:231], v[36:39]\n"
  "v_exp_f32 v103, v103\n"
  "s_waitcnt lgkmcnt(6)\n"
  "v_mfma_f32_16x16x32_bf16 v[40:43], v[68:71], v[224:227], v[40:43]\n"
  "ds_read_b64_tr_b16 v[64:65], v91 offset:57344\n"
  "ds_read_b64_tr_b16 v[66:67], v91 offset:57856\n"
  "v_exp_f32 v104, v104\n"
  "v_exp_f32 v105, v105\n"
  "v_mfma_f32_16x16x32_bf16 v[44:47], v[68:71], v[228:231], v[44:47]\n"
  "v_add_f32 v102, 1.0, v102\n"
  "s_waitcnt lgkmcnt(6)\n"
  "v_mfma_f32_16x16x32_bf16 v[32:35], v[72:75], v[232:235], v[32:35]\n"
  "ds_read_b64_tr_b16 v[68:69], v90 offset:61440\n"
  "ds_read_b64_tr_b16 v[70:71], v90 offset:61952\n"
  "s_add_u32 m0, s28, 8192\n"
  "s_add_u32 s43, s41, 2359296\n"
  "buffer_load_dwordx4 v95, s[12:15], s43 offen lds\n"
  "v_add_f32 v103, 1.0, v103\n"
  "v_mfma_f32_16x16x32_bf16 v[36:39], v[72:75], v[236:239], v[36:39]\n"
  "v_add_f32 v104, 1.0, v104\n"
  "s_waitcnt lgkmcnt(6)\n"
  "v_mfma_f32_16x16x32_bf16 v[40:43], v[76:79], v[232:235], v[40:43]\n"
  "ds_read_b64_tr_b16 v[72:73], v91 offset:61440\n"
  "ds_read_b64_tr_b16 v[74:75], v91 offset:61952\n"
  "v_add_f32 v105, 1.0, v105\n"
  "v_rcp_f32 v102, v102\n"
  "v_mfma_f32_16x16x32_bf16 v[44:47], v[76:79], v[236:239], v[44:47]\n"
  "v_rcp_f32 v103, v103\n"
  "s_waitcnt lgkmcnt(6)\n"
  "v_mfma_f32_16x16x32_bf16 v[32:35], v[80:83], v[240:243], v[32:35]\n"
  "s_waitcnt vmcnt(9)\n"
  "s_barrier\n"
  "v_add_u32 v88, 0x10000, v88\n"
  "ds_read_b64_tr_b16 v[76:77], v88 offset:0\n"
  "ds_read_b64_tr_b16 v[78:79], v88 offset:512\n"
  "v_rcp_f32 v104, v104\n"
  "s_setprio 3\n"
  "v_mfma_f32_16x16x32_bf16 v[36:39], v[80:83], v[244:247], v[36:39]\n"
  "v_rcp_f32 v105, v105\n"
  "s_waitcnt lgkmcnt(6)\n"
  "v_mfma_f32_16x16x32_bf16 v[40:43], v[64:67], v[240:243], v[40:43]\n"
  "v_add_u32 v89, 0x10000, v89\n"
  "ds_read_b64_tr_b16 v[80:81], v89 offset:0\n"
  "ds_read_b64_tr_b16 v[82:83], v89 offset:512\n"
  "v_mul_f32 v20, v20, v102\n"
  "v_mfma_f32_16x16x32_bf16 v[44:47], v[64:67], v[244:247], v[44:47]\n"
  "v_mul_f32 v21, v21, v103\n"
  "v_mul_f32 v22, v22, v104\n"
  "s_waitcnt lgkmcnt(6)\n"
  "v_mfma_f32_16x16x32_bf16 v[32:35], v[68:71], v[248:251], v[32:35]\n"
  "v_add_u32 v90, 0x10000, v90\n"
  "ds_read_b64_tr_b16 v[64:65], v90 offset:0\n"
  "ds_read_b64_tr_b16 v[66:67], v90 offset:512\n"
  "v_mul_f32 v23, v23, v105\n"
  "s_setprio 2\n"
  "v_mfma_f32_16x16x32_bf16 v[36:39], v[68:71], v[252:255], v[36:39]\n"
  "v_mul_f32 v106, v28, v28\n"
  "s_waitcnt lgkmcnt(6)\n"
  "v_mfma_f32_16x16x32_bf16 v[40:43], v[72:75], v[248:251], v[40:43]\n"
  "v_add_u32 v91, 0x10000, v91\n"
  "ds_read_b64_tr_b16 v[68:69], v91 offset:0\n"
  "ds_read_b64_tr_b16 v[70:71], v91 offset:512\n"
  "v_mul_f32 v107, v29, v29\n"
  "v_mfma_f32_16x16x32_bf16 v[44:47], v[72:75], v[252:255], v[44:47]\n"
  "v_mul_f32 v108, v30, v30\n"
  "v_mul_f32 v109, v31, v31\n"
  "s_waitcnt lgkmcnt(6)\n"
  "v_mfma_f32_16x16x32_bf16 v[0:3], v[76:79], v[112:115], 0\n"
  "ds_read_b64_tr_b16 v[72:73], v88 offset:4096\n"
  "ds_read_b64_tr_b16 v[74:75], v88 offset:4608\n"
  "v_fmaak_f32 v106, v106, v101, 0xc0135761\n"
  "s_setprio 1\n"
  "v_mfma_f32_16x16x32_bf16 v[4:7], v[76:79], v[116:119], 0\n"
  "v_fmaak_f32 v107, v107, v101, 0xc0135761\n"
  "s_waitcnt lgkmcnt(6)\n"
  "v_mfma_f32_16x16x32_bf16 v[8:11], v[80:83], v[112:115], 0\n"
  "ds_read_b64_tr_b16 v[76:77], v89 offset:4096\n"
  "ds_read_b64_tr_b16 v[78:79], v89 offset:4608\n"
  "v_fmaak_f32 v108, v108, v101, 0xc0135761\n"
  "v_mfma_f32_16x16x32_bf16 v[12:15], v[80:83], v[116:119], 0\n"
  "v_fmaak_f32 v109, v109, v101, 0xc0135761\n"
  "s_waitcnt lgkmcnt(6)\n"
  "v_mfma_f32_16x16x32_bf16 v[48:51], v[64:67], v[112:115], 0\n"
  "ds_read_b64_tr_b16 v[80:81], v90 offset:4096\n"
  "ds_read_b64_tr_b16 v[82:83], v90 offset:4608\n"
  "s_add_u32 m0, s28, 16384\n"
  "s_add_u32 s43, s41, s45\n"
  "buffer_load_dwordx4 v95, s[24:27], s43 offen lds\n"
  "v_mul_f32 v106, v106, v28\n"
  "v_mul_f32 v107, v107, v29\n"
  "s_setprio 0\n"
  "v_mfma_f32_16x16x32_bf16 v[52:55], v[64:67], v[116:119], 0\n"
  "v_mul_f32 v108, v108, v30\n"
  "s_waitcnt lgkmcnt(6)\n"
  "v_mfma_f32_16x16x32_bf16 v[56:59], v[68:71], v[112:115], 0\n"
  "ds_read_b64_tr_b16 v[64:65], v91 offset:4096\n"
  "ds_read_b64_tr_b16 v[66:67], v91 offset:4608\n"
  "v_mul_f32 v109, v109, v31\n"
  "v_mfma_f32_16x16x32_bf16 v[60:63], v[68:71], v[116:119], 0\n"
  "v_exp_f32 v106, v106\n"
  "s_waitcnt lgkmcnt(6)\n"
  "v_mfma_f32_16x16x32_bf16 v[0:3], v[72:75], v[120:123], v[0:3]\n"
  "s_waitcnt vmcnt(2)\n"
  "v_cmp_ne_u32 vcc, s47, v97\n"
  "s_cbranch_vccnz LSQ_%=\n"
  "LSQB_%=:\n"
  "s_barrier\n"
  "ds_read_b64_tr_b16 v[68:69], v88 offset:8192\n"
  "ds_read_b64_tr_b16 v[70:71], v88 offset:8704\n"
  "v_exp_f32 v107, v107\n"
  "v_exp_f32 v108, v108\n"
  "s_setprio 3\n"
  "v_mfma_f32_16x16x32_bf16 v[4:7], v[72:75], v[124:127], v[4:7]\n"
  "v_exp_f32 v109, v109\n"
  "s_add_u32 m0, s28, 57344\n"
  "s_mov_b32 s43, 3539072\n"
  "buffer_load_dwordx4 v94, s[16:19], s43 offen lds\n"
  "s_waitcnt lgkmcnt(6)\n"
  "v_mfma_f32_16x16x32_bf16 v[8:11], v[76:79], v[120:123], v[8:11]\n"
  "ds_read_b64_tr_b16 v[72:73], v89 offset:8192\n"
  "ds_read_b64_tr_b16 v[74:75], v89 offset:8704\n"
  "v_add_f32 v106, 1.0, v106\n"
  "v_mfma_f32_16x16x32_bf16 v[12:15], v[76:79], v[124:127], v[12:15]\n"
  "v_add_f32 v107, 1.0, v107\n"
  "s_waitcnt lgkmcnt(6)\n"
  "v_mfma_f32_16x16x32_bf16 v[48:51], v[80:83], v[120:123], v[48:51]\n"
  "ds_read_b64_tr_b16 v[76:77], v90 offset:8192\n"
  "ds_read_b64_tr_b16 v[78:79], v90 offset:8704\n"
  "v_add_f32 v108, 1.0, v108\n"
  "v_mfma_f32_16x16x32_bf16 v[52:55], v[80:83], v[124:127], v[52:55]\n"
  "v_add_f32 v109, 1.0, v109\n"
  "v_rcp_f32 v106, v106\n"
  "s_waitcnt lgkmcnt(6)\n"
  "v_mfma_f32_16x16x32_bf16 v[56:59], v[64:67], v[120:123], v[56:59]\n"
  "ds_read_b64_tr_b16 v[80:81], v91 offset:8192\n"
  "ds_read_b64_tr_b16 v[82:83], v91 offset:8704\n"
  "v_rcp_f32 v107, v107\n"
  "s_add_u32 m0, s28, 65536\n"
  "s_mov_b32 s43, 4128896\n"
  "buffer_load_dwordx4 v94, s[16:19], s43 offen lds\n"
  "v_mfma_f32_16x16x32_bf16 v[60:63], v[64:67], v[124:127], v[60:63]\n"
  "v_rcp_f32 v108, v108\n"
  "s_waitcnt lgkmcnt(6)\n"
  "v_mfma_f32_16x16x32_bf16 v[0:3], v[68:71], v[128:131], v[0:3]\n"
  "ds_read_b64_tr_b16 v[64:65], v88 offset:12288\n"
  "ds_read_b64_tr_b16 v[66:67], v88 offset:12800\n"
  "v_rcp_f32 v109, v109\n"
  "v_mfma_f32_16x16x32_bf16 v[4:7], v[68:71], v[132:135], v[4:7]\n"
  "v_mul_f32 v28, v28, v106\n"
  "v_mul_f32 v29, v29, v107\n"
  "s_waitcnt lgkmcnt(6)\n"
  "v_mfma_f32_16x16x32_bf16 v[8:11], v[72:75], v[128:131], v[8:11]\n"
  "ds_read_b64_tr_b16 v[68:69], v89 offset:12288\n"
  "ds_read_b64_tr_b16 v[70:71], v89 offset:12800\n"
  "v_mul_f32 v30, v30, v108\n"
  "v_mfma_f32_16x16x32_bf16 v[12:15], v[72:75], v[132:135], v[12:15]\n"
  "v_mul_f32 v31, v31, v109\n"
  "s_add_u32 m0, s28, 73728\n"
  "s_mov_b32 s43, 4718720\n"
  "buffer_load_dwordx4 v94, s[16:19], s43 offen lds\n"
  "s_waitcnt lgkmcnt(6)\n"
  "v_mfma_f32_16x16x32_bf16 v[48:51], v[76:79], v[128:131], v[48:51]\n"
  "ds_read_b64_tr_b16 v[72:73], v90 offset:12288\n"
  "ds_read_b64_tr_b16 v[74:75], v90 offset:12800\n"
  "v_mul_f32 v102, v32, v32\n"
  "v_mfma_f32_16x16x32_bf16 v[52:55], v[76:79], v[132:135], v[52:55]\n"
  "v_mul_f32 v103, v33, v33\n"
  "s_waitcnt lgkmcnt(6)\n"
  "v_mfma_f32_16x16x32_bf16 v[56:59], v[80:83], v[128:131], v[56:59]\n"
  "ds_read_b64_tr_b16 v[76:77], v91 offset:12288\n"
  "ds_read_b64_tr_b16 v[78:79], v91 offset:12800\n"
  "v_mul_f32 v104, v34, v34\n"
  "v_mul_f32 v105, v35, v35\n"
  "v_mfma_f32_16x16x32_bf16 v[60:63], v[80:83], v[132:135], v[60:63]\n"
  "v_fmaak_f32 v102, v102, v101, 0xc0135761\n"
  "s_waitcnt lgkmcnt(6)\n"
  "v_mfma_f32_16x16x32_bf16 v[0:3], v[64:67], v[136:139], v[0:3]\n"
  "ds_read_b64_tr_b16 v[80:81], v88 offset:16384\n"
  "ds_read_b64_tr_b16 v[82:83], v88 offset:16896\n"
  "v_fmaak_f32 v103, v103, v101, 0xc0135761\n"
  "v_mov_b32 v87, s47\n"
  "s_mov_b64 exec, 1\n"
  "buffer_store_dword v87, off, s[20:23], s69 offset:256 sc1\n"
  "s_mov_b64 exec, -1\n"
  "s_add_u32 m0, s28, 90048\n"
  "s_add_u32 s43, s72, 0\n"
  "buffer_load_dwordx4 v100, s[20:23], s43 offen sc1 lds\n"
  "v_mfma_f32_16x16x32_bf16 v[4:7], v[64:67], v[140:143], v[4:7]\n"
  "v_fmaak_f32 v104, v104, v101, 0xc0135761\n"
  "s_waitcnt lgkmcnt(6)\n"
  "v_mfma_f32_16x16x32_bf16 v[8:11], v[68:71], v[136:139], v[8:11]\n"
  "ds_read_b64_tr_b16 v[64:65], v89 offset:16384\n"
  "ds_read_b64_tr_b16 v[66:67], v89 offset:16896\n"
  "v_fmaak_f32 v105, v105, v101, 0xc0135761\n"
  "v_mul_f32 v102, v102, v32\n"
  "s_setprio 2\n"
  "v_mfma_f32_16x16x32_bf16 v[12:15], v[68:71], v[140:143], v[12:15]\n"
  "v_mul_f32 v103, v103, v33\n"
  "s_waitcnt lgkmcnt(6)\n"
  "v_mfma_f32_16x16x32_bf16 v[48:51], v[72:75], v[136:139], v[48:51]\n"
  "ds_read_b64_tr_b16 v[68:69], v90 offset:16384\n"
  "ds_read_b64_tr_b16 v[70:71], v90 offset:16896\n"
  "v_mul_f32 v104, v104, v34\n"
  "v_mfma_f32_16x16x32_bf16 v[52:55], v[72:75], v[140:143], v[52:55]\n"
  "v_mul_f32 v105, v105, v35\n"
  "s_lshl_b32 s43, s68, 8\n"
  "s_add_u32 m0, s43, 124864\n"
  "s_add_u32 s43, s73, 0\n"
  "buffer_load_dword v93, s[20:23], s43 offen sc1 lds\n"
  "s_waitcnt lgkmcnt(6)\n"
  "v_mfma_f32_16x16x32_bf16 v[56:59], v[76:79], v[136:139], v[56:59]\n"
  "ds_read_b64_tr_b16 v[72:73], v91 offset:16384\n"
  "ds_read_b64_tr_b16 v[74:75], v91 offset:16896\n"
  "v_exp_f32 v102, v102\n"
  "v_mfma_f32_16x16x32_bf16 v[60:63], v[76:79], v[140:143], v[60:63]\n"
  "v_exp_f32 v103, v103\n"
  "v_exp_f32 v104, v104\n"
  "s_waitcnt lgkmcnt(6)\n"
  "v_mfma_f32_16x16x32_bf16 v[0:3], v[80:83], v[144:147], v[0:3]\n"
  "ds_read_b64_tr_b16 v[76:77], v88 offset:20480\n"
  "ds_read_b64_tr_b16 v[78:79], v88 offset:20992\n"
  "v_exp_f32 v105, v105\n"
  "v_mfma_f32_16x16x32_bf16 v[4:7], v[80:83], v[148:151], v[4:7]\n"
  "v_add_f32 v102, 1.0, v102\n"
  "s_waitcnt lgkmcnt(6)\n"
  "v_mfma_f32_16x16x32_bf16 v[8:11], v[64:67], v[144:147], v[8:11]\n"
  "ds_read_b64_tr_b16 v[80:81], v89 offset:20480\n"
  "ds_read_b64_tr_b16 v[82:83], v89 offset:20992\n"
  "v_add_f32 v103, 1.0, v103\n"
  "v_mfma_f32_16x16x32_bf16 v[12:15], v[64:67], v[148:151], v[12:15]\n"
  "v_add_f32 v104, 1.0, v104\n"
  "v_add_f32 v105, 1.0, v105\n"
  "s_add_u32 m0, s28, 106432\n"
  "s_add_u32 s43, s72, 16384\n"
  "buffer_load_dwordx4 v100, s[20:23], s43 offen sc1 lds\n"
  "s_waitcnt lgkmcnt(6)\n"
  "v_mfma_f32_16x16x32_bf16 v[48:51], v[68:71], v[144:147], v[48:51]\n"
  "ds_read_b64_tr_b16 v[64:65], v90 offset:20480\n"
  "ds_read_b64_tr_b16 v[66:67], v90 offset:20992\n"
  "v_rcp_f32 v102, v102\n"
  "v_mfma_f32_16x16x32_bf16 v[52:55], v[68:71], v[148:151], v[52:55]\n"
  "v_rcp_f32 v103, v103\n"
  "s_waitcnt lgkmcnt(6)\n"
  "v_mfma_f32_16x16x32_bf16 v[56:59], v[72:75], v[144:147], v[56:59]\n"
  "ds_read_b64_tr_b16 v[68:69], v91 offset:20480\n"
  "ds_read_b64_tr_b16 v[70:71], v91 offset:20992\n"
  "v_rcp_f32 v104, v104\n"
  "v_mfma_f32_16x16x32_bf16 v[60:63], v[72:75], v[148:151], v[60:63]\n"
  "v_rcp_f32 v105, v105\n"
  "s_waitcnt lgkmcnt(6)\n"
  "v_mfma_f32_16x16x32_bf16 v[0:3], v[76:79], v[152:155], v[0:3]\n"
  "ds_read_b64_tr_b16 v[72:73], v88 offset:24576\n"
  "ds_read_b64_tr_b16 v[74:75], v88 offset:25088\n"
  "v_mul_f32 v32, v32, v102\n"
  "v_mul_f32 v33, v33, v103\n"
  "s_cmp_ge_u32 s68, 2\n"
  "s_cbranch_scc1 LKX0att_%=\n"
  "s_add_u32 m0, s28, 122816\n"
  "s_add_u32 s43, s72, 32768\n"
  "buffer_load_dwordx4 v100, s[20:23], s43 offen sc1 lds\n"
  "LKX0att_%=:\n"
  "v_mfma_f32_16x16x32_bf16 v[4:7], v[76:79], v[156:159], v[4:7]\n"
  "v_mul_f32 v34, v34, v104\n"
  "s_waitcnt lgkmcnt(6)\n"
  "v_mfma_f32_16x16x32_bf16 v[8:11], v[80:83], v[152:155], v[8:11]\n"
  "ds_read_b64_tr_b16 v[76:77], v89 offset:24576\n"
  "ds_read_b64_tr_b16 v[78:79], v89 offset:25088\n"
  "v_mul_f32 v35, v35, v105\n"
  "v_mfma_f32_16x16x32_bf16 v[12:15], v[80:83], v[156:159], v[12:15]\n"
  "v_mul_f32 v106, v40, v40\n"
  "s_waitcnt lgkmcnt(6)\n"
  "v_mfma_f32_16x16x32_bf16 v[48:51], v[64:67], v[152:155], v[48:51]\n"
  "ds_read_b64_tr_b16 v[80:81], v90 offset:24576\n"
  "ds_read_b64_tr_b16 v[82:83], v90 offset:25088\n"
  "v_mul_f32 v107, v41, v41\n"
  "v_mul_f32 v108, v42, v42\n"
  "s_setprio 1\n"
  "v_mfma_f32_16x16x32_bf16 v[52:55], v[64:67], v[156:159], v[52:55]\n"
  "v_mul_f32 v109, v43, v43\n"
  "s_add_u32 m0, s28, 98240\n"
  "s_add_u32 s43, s72, 8192\n"
  "buffer_load_dwordx4 v100, s[20:23], s43 offen sc1 lds\n"
  "s_waitcnt lgkmcnt(6)\n"
  "v_mfma_f32_16x16x32_bf16 v[56:59], v[68:71], v[152:155], v[56:59]\n"
  "ds_read_b64_tr_b16 v[64:65], v91 offset:24576\n"
  "ds_read_b64_tr_b16 v[66:67], v91 offset:25088\n"
  "v_fmaak_f32 v106, v106, v101, 0xc0135761\n"
  "v_mfma_f32_16x16x32_bf16 v[60:63], v[68:71], v[156:159], v[60:63]\n"
  "v_fmaak_f32 v107, v107, v101, 0xc0135761\n"
  "s_waitcnt lgkmcnt(6)\n"
  "v_mfma_f32_16x16x32_bf16 v[0:3], v[72:75], v[160:163], v[0:3]\n"
  "ds_read_b64_tr_b16 v[68:69], v88 offset:28672\n"
  "ds_read_b64_tr_b16 v[70:71], v88 offset:29184\n"
  "v_fmaak_f32 v108, v108, v101, 0xc0135761\n"
  "v_mfma_f32_16x16x32_bf16 v[4:7], v[72:75], v[164:167], v[4:7]\n"
  "v_fmaak_f32 v109, v109, v101, 0xc0135761\n"
  "v_mul_f32 v106, v106, v40\n"
  "s_waitcnt lgkmcnt(6)\n"
  "v_mfma_f32_16x16x32_bf16 v[8:11], v[76:79], v[160:163], v[8:11]\n"
  "ds_read_b64_tr_b16 v[72:73], v89 offset:28672\n"
  "ds_read_b64_tr_b16 v[74:75], v89 offset:29184\n"
  "v_mul_f32 v107, v107, v41\n"
  "s_add_u32 m0, s28, 114624\n"
  "s_add_u32 s43, s72, 24576\n"
  "buffer_load_dwordx4 v100, s[20:23], s43 offen sc1 lds\n"
  "v_mfma_f32_16x16x32_bf16 v[12:15], v[76:79], v[164:167], v[12:15]\n"
  "v_mul_f32 v108, v108, v42\n"
  "s_waitcnt lgkmcnt(6)\n"
  "v_mfma_f32_16x16x32_bf16 v[48:51], v[80:83], v[160:163], v[48:51]\n"
  "ds_read_b64_tr_b16 v[76:77], v90 offset:28672\n"
  "ds_read_b64_tr_b16 v[78:79], v90 offset:29184\n"
  "v_mul_f32 v109, v109, v43\n"
  "v_mfma_f32_16x16x32_bf16 v[52:55], v[80:83], v[164:167], v[52:55]\n"
  "v_exp_f32 v106, v106\n"
  "v_exp_f32 v107, v107\n"
  "s_waitcnt lgkmcnt(6)\n"
  "v_mfma_f32_16x16x32_bf16 v[56:59], v[64:67], v[160:163], v[56:59]\n"
  "ds_read_b64_tr_b16 v[80:81], v91 offset:28672\n"
  "ds_read_b64_tr_b16 v[82:83], v91 offset:29184\n"
  "v_exp_f32 v108, v108\n"
  "v_mfma_f32_16x16x32_bf16 v[60:63], v[64:67], v[164:167], v[60:63]\n"
  "v_exp_f32 v109, v109\n"
  "s_waitcnt lgkmcnt(6)\n"
  "v_mfma_f32_16x16x32_bf16 v[0:3], v[68:71], v[168:171], v[0:3]\n"
  "v_add_u32 v88, 0xfffea000, v88\n"
  "ds_read_b64_tr_b16 v[64:65], v88 offset:0\n"
  "ds_read_b64_tr_b16 v[66:67], v88 offset:512\n"
  "v_add_f32 v106, 1.0, v106\n"
  "v_mfma_f32_16x16x32_bf16 v[4:7], v[68:71], v[172:175], v[4:7]\n"
  "v_add_f32 v107, 1.0, v107\n"
  "s_waitcnt lgkmcnt(6)\n"
  "v_mfma_f32_16x16x32_bf16 v[8:11], v[72:75], v[168:171], v[8:11]\n"
  "v_add_u32 v89, 0xfffea000, v89\n"
  "ds_read_b64_tr_b16 v[68:69], v89 offset:0\n"
  "ds_read_b64_tr_b16 v[70:71], v89 offset:512\n"
  "v_add_f32 v108, 1.0, v108\n"
  "v_add_f32 v109, 1.0, v109\n"
  "v_mfma_f32_16x16x32_bf16 v[12:15], v[72:75], v[172:175], v[12:15]\n"
  "v_rcp_f32 v106, v106\n"
  "s_waitcnt lgkmcnt(6)\n"
  "v_mfma_f32_16x16x32_bf16 v[48:51], v[76:79], v[168:171], v[48:51]\n"
  "v_add_u32 v90, 0xfffea000, v90\n"
  "ds_read_b64_tr_b16 v[72:73], v90 offset:0\n"
  "ds_read_b64_tr_b16 v[74:75], v90 offset:512\n"
  "s_add_u32 m0, s28, 24576\n"
  "s_add_u32 s43, s41, 1769472\n"
  "buffer_load_dwordx4 v95, s[16:19], s43 offen lds\n"
  "v_rcp_f32 v107, v107\n"
  "v_mfma_f32_16x16x32_bf16 v[52:55], v[76:79], v[172:175], v[52:55]\n"
  "v_rcp_f32 v108, v108\n"
  "s_waitcnt lgkmcnt(6)\n"
  "v_mfma_f32_16x16x32_bf16 v[56:59], v[80:83], v[168:171], v[56:59]\n"
  "v_add_u32 v91, 0xfffea000, v91\n"
  "ds_read_b64_tr_b16 v[76:77], v91 offset:0\n"
  "ds_read_b64_tr_b16 v[78:79], v91 offset:512\n"
  "v_rcp_f32 v109, v109\n"
  "v_mul_f32 v40, v40, v106\n"
  "s_setprio 0\n"
  "v_mfma_f32_16x16x32_bf16 v[60:63], v[80:83], v[172:175], v[60:63]\n"
  "v_mul_f32 v41, v41, v107\n"
  "s_waitcnt lgkmcnt(6)\n"
  "v_mfma_f32_16x16x32_bf16 v[0:3], v[64:67], v[176:179], v[0:3]\n"
  "ds_read_b64_tr_b16 v[80:81], v88 offset:4096\n"
  "ds_read_b64_tr_b16 v[82:83], v88 offset:4608\n"
  "v_mul_f32 v42, v42, v108\n"
  "v_mfma_f32_16x16x32_bf16 v[4:7], v[64:67], v[180:183], v[4:7]\n"
  "v_mul_f32 v43, v43, v109\n"
  "s_waitcnt lgkmcnt(6)\n"
  "v_mfma_f32_16x16x32_bf16 v[8:11], v[68:71], v[176:179], v[8:11]\n"
  "ds_read_b64_tr_b16 v[64:65], v89 offset:4096\n"
  "ds_read_b64_tr_b16 v[66:67], v89 offset:4608\n"
  "v_mul_f32 v102, v36, v36\n"
  "v_mfma_f32_16x16x32_bf16 v[12:15], v[68:71], v[180:183], v[12:15]\n"
  "v_mul_f32 v103, v37, v37\n"
  "v_mul_f32 v104, v38, v38\n"
  "s_waitcnt lgkmcnt(6)\n"
  "v_mfma_f32_16x16x32_bf16 v[48:51], v[72:75], v[176:179], v[48:51]\n"
  "ds_read_b64_tr_b16 v[68:69], v90 offset:4096\n"
  "ds_read_b64_tr_b16 v[70:71], v90 offset:4608\n"
  "v_mul_f32 v105, v39, v39\n"
  "v_mfma_f32_16x16x32_bf16 v[52:55], v[72:75], v[180:183], v[52:55]\n"
  "v_fmaak_f32 v102, v102, v101, 0xc0135761\n"
  "s_waitcnt lgkmcnt(6)\n"
  "v_mfma_f32_16x16x32_bf16 v[56:59], v[76:79], v[176:179], v[56:59]\n"
  "ds_read_b64_tr_b16 v[72:73], v91 offset:4096\n"
  "ds_read_b64_tr_b16 v[74:75], v91 offset:4608\n"
  "s_add_u32 m0, s28, 32768\n"
  "s_add_u32 s43, s41, 4128768\n"
  "s_min_u32 s43, s43, 5013504\n"
  "buffer_load_dwordx4 v95, s[16:19], s43 offen lds\n"
  "v_fmaak_f32 v103, v103, v101, 0xc0135761\n"
  "v_mfma_f32_16x16x32_bf16 v[60:63], v[76:79], v[180:183], v[60:63]\n"
  "v_fmaak_f32 v104, v104, v101, 0xc0135761\n"
  "v_fmaak_f32 v105, v105, v101, 0xc0135761\n"
  "s_waitcnt lgkmcnt(6)\n"
  "v_mfma_f32_16x16x32_bf16 v[0:3], v[80:83], v[184:187], v[0:3]\n"
  "ds_read_b64_tr_b16 v[76:77], v88 offset:8192\n"
  "ds_read_b64_tr_b16 v[78:79], v88 offset:8704\n"
  "v_mul_f32 v102, v102, v36\n"
  "v_mfma_f32_16x16x32_bf16 v[4:7], v[80:83], v[188:191], v[4:7]\n"
  "v_mul_f32 v103, v103, v37\n"
  "s_waitcnt lgkmcnt(6)\n"
  "v_mfma_f32_16x16x32_bf16 v[8:11], v[64:67], v[184:187], v[8:11]\n"
  "ds_read_b64_tr_b16 v[80:81], v89 offset:8192\n"
  "ds_read_b64_tr_b16 v[82:83], v89 offset:8704\n"
  "v_mul_f32 v104, v104, v38\n"
  "v_mfma_f32_16x16x32_bf16 v[12:15], v[64:67], v[188:191], v[12:15]\n"
  "v_mul_f32 v105, v105, v39\n"
  "s_waitcnt lgkmcnt(6)\n"
  "v_mfma_f32_16x16x32_bf16 v[48:51], v[68:71], v[184:187], v[48:51]\n"
  "ds_read_b64_tr_b16 v[64:65], v88 offset:12288\n"
  "ds_read_b64_tr_b16 v[66:67], v88 offset:12800\n"
  "v_exp_f32 v102, v102\n"
  "v_exp_f32 v103, v103\n"
  "v_mfma_f32_16x16x32_bf16 v[52:55], v[68:71], v[188:191], v[52:55]\n"
  "v_exp_f32 v104, v104\n"
  "s_waitcnt lgkmcnt(6)\n"
  "v_mfma_f32_16x16x32_bf16 v[56:59], v[72:75], v[184:187], v[56:59]\n"
  "ds_read_b64_tr_b16 v[68:69], v89 offset:12288\n"
  "ds_read_b64_tr_b16 v[70:71], v89 offset:12800\n"
  "v_exp_f32 v105, v105\n"
  "v_mfma_f32_16x16x32_bf16 v[60:63], v[72:75], v[188:191], v[60:63]\n"
  "v_add_f32 v102, 1.0, v102\n"
  "s_waitcnt lgkmcnt(6)\n"
  "v_mfma_f32_16x16x32_bf16 v[0:3], v[76:79], v[192:195], v[0:3]\n"
  "s_waitcnt vmcnt(0)\n"
  "s_barrier\n"
  "ds_read_b64_tr_b16 v[72:73], v88 offset:16384\n"
  "ds_read_b64_tr_b16 v[74:75], v88 offset:16896\n"
  "v_add_f32 v103, 1.0, v103\n"
  "v_add_f32 v104, 1.0, v104\n"
  "s_setprio 3\n"
  "v_mfma_f32_16x16x32_bf16 v[4:7], v[76:79], v[196:199], v[4:7]\n"
  "v_add_f32 v105, 1.0, v105\n"
  "s_add_u32 m0, s28, 126912\n"
  "s_add_u32 s43, s72, 34816\n"
  "buffer_load_dwordx4 v100, s[20:23], s43 offen sc1 lds\n"
  "s_waitcnt lgkmcnt(6)\n"
  "v_mfma_f32_16x16x32_bf16 v[8:11], v[80:83], v[192:195], v[8:11]\n"
  "ds_read_b64_tr_b16 v[76:77], v89 offset:16384\n"
  "ds_read_b64_tr_b16 v[78:79], v89 offset:16896\n"
  "v_rcp_f32 v102, v102\n"
  "v_mfma_f32_16x16x32_bf16 v[12:15], v[80:83], v[196:199], v[12:15]\n"
  "v_rcp_f32 v103, v103\n"
  "s_waitcnt lgkmcnt(6)\n"
  "v_mfma_f32_16x16x32_bf16 v[0:3], v[64:67], v[200:203], v[0:3]\n"
  "ds_read_b64_tr_b16 v[80:81], v88 offset:20480\n"
  "ds_read_b64_tr_b16 v[82:83], v88 offset:20992\n"
  "v_rcp_f32 v104, v104\n"
  "v_mfma_f32_16x16x32_bf16 v[4:7], v[64:67], v[204:207], v[4:7]\n"
  "v_rcp_f32 v105, v105\n"
  "v_mul_f32 v36, v36, v102\n"
  "s_lshl_b32 s43, s68, 8\n"
  "s_add_u32 m0, s43, 161728\n"
  "s_add_u32 s43, s73, 4096\n"
  "buffer_load_dword v93, s[20:23], s43 offen sc1 lds\n"
  "s_waitcnt lgkmcnt(6)\n"
  "v_mfma_f32_16x16x32_bf16 v[8:11], v[68:71], v[200:203], v[8:11]\n"
  "ds_read_b64_tr_b16 v[64:65], v89 offset:20480\n"
  "ds_read_b64_tr_b16 v[66:67], v89 offset:20992\n"
  "v_mul_f32 v37, v37, v103\n"
  "v_mfma_f32_16x16x32_bf16 v[12:15], v[68:71], v[204:207], v[12:15]\n"
  "v_mul_f32 v38, v38, v104\n"
  "s_waitcnt lgkmcnt(6)\n"
  "v_mfma_f32_16x16x32_bf16 v[0:3], v[72:75], v[208:211], v[0:3]\n"
  "ds_read_b64_tr_b16 v[68:69], v88 offset:24576\n"
  "ds_read_b64_tr_b16 v[70:71], v88 offset:25088\n"
  "v_mul_f32 v39, v39, v105\n"
  "v_mfma_f32_16x16x32_bf16 v[4:7], v[72:75], v[212:215], v[4:7]\n"
  "v_mul_f32 v106, v44, v44\n"
  "v_mul_f32 v107, v45, v45\n"
  "s_add_u32 m0, s28, 143296\n"
  "s_add_u32 s43, s72, 51200\n"
  "buffer_load_dwordx4 v100, s[20:23], s43 offen sc1 lds\n"
  "s_waitcnt lgkmcnt(6)\n"
  "v_mfma_f32_16x16x32_bf16 v[8:11], v[76:79], v[208:211], v[8:11]\n"
  "ds_read_b64_tr_b16 v[72:73], v89 offset:24576\n"
  "ds_read_b64_tr_b16 v[74:75], v89 offset:25088\n"
  "v_mul_f32 v108, v46, v46\n"
  "v_mfma_f32_16x16x32_bf16 v[12:15], v[76:79], v[212:215], v[12:15]\n"
  "v_mul_f32 v109, v47, v47\n"
  "s_waitcnt lgkmcnt(6)\n"
  "v_mfma_f32_16x16x32_bf16 v[0:3], v[80:83], v[216:219], v[0:3]\n"
  "ds_read_b64_tr_b16 v[76:77], v88 offset:28672\n"
  "ds_read_b64_tr_b16 v[78:79], v88 offset:29184\n"
  "v_fmaak_f32 v106, v106, v101, 0xc0135761\n"
  "v_mfma_f32_16x16x32_bf16 v[4:7], v[80:83], v[220:223], v[4:7]\n"
  "v_fmaak_f32 v107, v107, v101, 0xc0135761\n"
  "s_waitcnt lgkmcnt(6)\n"
  "v_mfma_f32_16x16x32_bf16 v[8:11], v[64:67], v[216:219], v[8:11]\n"
  "ds_read_b64_tr_b16 v[80:81], v89 offset:28672\n"
  "ds_read_b64_tr_b16 v[82:83], v89 offset:29184\n"
  "v_fmaak_f32 v108, v108, v101, 0xc0135761\n"
  "v_fmaak_f32 v109, v109, v101, 0xc0135761\n"
  "s_cmp_ge_u32 s68, 2\n"
  "s_cbranch_scc1 LKX1att_%=\n"
  "s_add_u32 m0, s28, 159680\n"
  "s_add_u32 s43, s72, 67584\n"
  "buffer_load_dwordx4 v100, s[20:23], s43 offen sc1 lds\n"
  "LKX1att_%=:\n"
  "v_mfma_f32_16x16x32_bf16 v[12:15], v[64:67], v[220:223], v[12:15]\n"
  "v_mul_f32 v106, v106, v44\n"
  "s_waitcnt lgkmcnt(6)\n"
  "v_mfma_f32_16x16x32_bf16 v[0:3], v[68:71], v[224:227], v[0:3]\n"
  "ds_read_b64_tr_b16 v[64:65], v88 offset:32768\n"
  "ds_read_b64_tr_b16 v[66:67], v88 offset:33280\n"
  "v_mul_f32 v107, v107, v45\n"
  "v_mfma_f32_16x16x32_bf16 v[4:7], v[68:71], v[228:231], v[4:7]\n"
  "v_mul_f32 v108, v108, v46\n"
  "s_setprio 2\n"
  "s_waitcnt lgkmcnt(6)\n"
  "v_mfma_f32_16x16x32_bf16 v[8:11], v[72:75], v[224:227], v[8:11]\n"
  "ds_read_b64_tr_b16 v[68:69], v89 offset:32768\n"
  "ds_read_b64_tr_b16 v[70:71], v89 offset:33280\n"
  "v_mul_f32 v109, v109, v47\n"
  "v_exp_f32 v106, v106\n"
  "s_add_u32 m0, s28, 135104\n"
  "s_add_u32 s43, s72, 43008\n"
  "buffer_load_dwordx4 v100, s[20:23], s43 offen sc1 lds\n"
  "v_mfma_f32_16x16x32_bf16 v[12:15], v[72:75], v[228:231], v[12:15]\n"
  "v_exp_f32 v107, v107\n"
  "s_waitcnt lgkmcnt(6)\n"
  "v_mfma_f32_16x16x32_bf16 v[0:3], v[76:79], v[232:235], v[0:3]\n"
  "ds_read_b64_tr_b16 v[72:73], v88 offset:36864\n"
  "ds_read_b64_tr_b16 v[74:75], v88 offset:37376\n"
  "v_exp_f32 v108, v108\n"
  "v_mfma_f32_16x16x32_bf16 v[4:7], v[76:79], v[236:239], v[4:7]\n"
  "v_exp_f32 v109, v109\n"
  "s_waitcnt lgkmcnt(6)\n"
  "v_mfma_f32_16x16x32_bf16 v[8:11], v[80:83], v[232:235], v[8:11]\n"
  "ds_read_b64_tr_b16 v[76:77], v89 offset:36864\n"
  "ds_read_b64_tr_b16 v[78:79], v89 offset:37376\n"
  "v_add_f32 v106, 1.0, v106\n"
  "v_mfma_f32_16x16x32_bf16 v[12:15], v[80:83], v[236:239], v[12:15]\n"
  "v_add_f32 v107, 1.0, v107\n"
  "v_add_f32 v108, 1.0, v108\n"
  "s_add_u32 m0, s28, 151488\n"
  "s_add_u32 s43, s72, 59392\n"
  "buffer_load_dwordx4 v100, s[20:23], s43 offen sc1 lds\n"
  "s_waitcnt lgkmcnt(6)\n"
  "v_mfma_f32_16x16x32_bf16 v[0:3], v[64:67], v[240:243], v[0:3]\n"
  "ds_read_b64_tr_b16 v[80:81], v90 offset:8192\n"
  "ds_read_b64_tr_b16 v[82:83], v90 offset:8704\n"
  "v_add_f32 v109, 1.0, v109\n"
  "v_mfma_f32_16x16x32_bf16 v[4:7], v[64:67], v[244:247], v[4:7]\n"
  "v_rcp_f32 v106, v106\n"
  "s_waitcnt lgkmcnt(6)\n"
  "v_mfma_f32_16x16x32_bf16 v[8:11], v[68:71], v[240:243], v[8:11]\n"
  "ds_read_b64_tr_b16 v[64:65], v91 offset:8192\n"
  "ds_read_b64_tr_b16 v[66:67], v91 offset:8704\n"
  "v_rcp_f32 v107, v107\n"
  "v_mfma_f32_16x16x32_bf16 v[12:15], v[68:71], v[244:247], v[12:15]\n"
  "v_rcp_f32 v108, v108\n"
  "v_rcp_f32 v109, v109\n"
  "v_bfe_u32 v96, v100, 4, 4\n"
  "v_lshlrev_b32 v96, 2, v96\n"
  "v_add_u32 v96, 163776, v96\n"
  "v_mov_b32 v97, 0\n"
  "ds_write_b32 v96, v97\n"
  "s_waitcnt lgkmcnt(7)\n"
  "v_mfma_f32_16x16x32_bf16 v[0:3], v[72:75], v[248:251], v[0:3]\n"
  "ds_read_b64_tr_b16 v[68:69], v90 offset:12288\n"
  "ds_read_b64_tr_b16 v[70:71], v90 offset:12800\n"
  "v_mul_f32 v44, v44, v106\n"
  "v_mfma_f32_16x16x32_bf16 v[4:7], v[72:75], v[252:255], v[4:7]\n"
  "v_mul_f32 v45, v45, v107\n"
  "s_waitcnt lgkmcnt(7)\n"
  "v_mfma_f32_16x16x32_bf16 v[8:11], v[76:79], v[248:251], v[8:11]\n"
  "ds_read_b64_tr_b16 v[72:73], v91 offset:12288\n"
  "ds_read_b64_tr_b16 v[74:75], v91 offset:12800\n"
  "v_mul_f32 v46, v46, v108\n"
  "v_mfma_f32_16x16x32_bf16 v[12:15], v[76:79], v[252:255], v[12:15]\n"
  "v_mul_f32 v47, v47, v109\n"
  "s_waitcnt lgkmcnt(7)\n"
  "v_mfma_f32_16x16x32_bf16 v[48:51], v[80:83], v[192:195], v[48:51]\n"
  "ds_read_b64_tr_b16 v[76:77], v90 offset:16384\n"
  "ds_read_b64_tr_b16 v[78:79], v90 offset:16896\n"
  "v_mfma_f32_16x16x32_bf16 v[52:55], v[80:83], v[196:199], v[52:55]\n"
  "buffer_load_dword v99, v98, s[20:23], s80 offen sc1\n"
  "s_waitcnt lgkmcnt(7)\n"
  "v_mfma_f32_16x16x32_bf16 v[56:59], v[64:67], v[192:195], v[56:59]\n"
  "ds_read_b64_tr_b16 v[80:81], v91 offset:16384\n"
  "ds_read_b64_tr_b16 v[82:83], v91 offset:16896\n"
  "v_mul_f32 v0, v0, v16\n"
  "s_setprio 1\n"
  "v_mfma_f32_16x16x32_bf16 v[60:63], v[64:67], v[196:199], v[60:63]\n"
  "v_mul_f32 v1, v1, v17\n"
  "s_waitcnt lgkmcnt(6)\n"
  "v_mfma_f32_16x16x32_bf16 v[48:51], v[68:71], v[200:203], v[48:51]\n"
  "ds_read_b64_tr_b16 v[64:65], v90 offset:20480\n"
  "ds_read_b64_tr_b16 v[66:67], v90 offset:20992\n"
  "v_mul_f32 v2, v2, v18\n"
  "v_mfma_f32_16x16x32_bf16 v[52:55], v[68:71], v[204:207], v[52:55]\n"
  "v_mul_f32 v3, v3, v19\n"
  "s_waitcnt lgkmcnt(6)\n"
  "v_mfma_f32_16x16x32_bf16 v[56:59], v[72:75], v[200:203], v[56:59]\n"
  "ds_read_b64_tr_b16 v[68:69], v91 offset:20480\n"
  "ds_read_b64_tr_b16 v[70:71], v91 offset:20992\n"
  "v_mul_f32 v8, v8, v24\n"
  "v_mfma_f32_16x16x32_bf16 v[60:63], v[72:75], v[204:207], v[60:63]\n"
  "v_mul_f32 v9, v9, v25\n"
  "s_waitcnt lgkmcnt(6)\n"
  "v_mfma_f32_16x16x32_bf16 v[48:51], v[76:79], v[208:211], v[48:51]\n"
  "ds_read_b64_tr_b16 v[72:73], v90 offset:24576\n"
  "ds_read_b64_tr_b16 v[74:75], v90 offset:25088\n"
  "v_mul_f32 v10, v10, v26\n"
  "v_mfma_f32_16x16x32_bf16 v[52:55], v[76:79], v[212:215], v[52:55]\n"
  "v_mul_f32 v11, v11, v27\n"
  "s_waitcnt lgkmcnt(6)\n"
  "v_mfma_f32_16x16x32_bf16 v[56:59], v[80:83], v[208:211], v[56:59]\n"
  "ds_read_b64_tr_b16 v[76:77], v91 offset:24576\n"
  "ds_read_b64_tr_b16 v[78:79], v91 offset:25088\n"
  "v_cvt_pk_bf16_f32 v16, v0, v1\n"
  "v_mfma_f32_16x16x32_bf16 v[60:63], v[80:83], v[212:215], v[60:63]\n"
  "v_cvt_pk_bf16_f32 v17, v2, v3\n"
  "s_waitcnt lgkmcnt(6)\n"
  "v_mfma_f32_16x16x32_bf16 v[48:51], v[64:67], v[216:219], v[48:51]\n"
  "ds_read_b64_tr_b16 v[80:81], v90 offset:28672\n"
  "ds_read_b64_tr_b16 v[82:83], v90 offset:29184\n"
  "v_cvt_pk_bf16_f32 v18, v8, v9\n"
  "v_mfma_f32_16x16x32_bf16 v[52:55], v[64:67], v[220:223], v[52:55]\n"
  "v_cvt_pk_bf16_f32 v19, v10, v11\n"
  "s_waitcnt lgkmcnt(6)\n"
  "v_mfma_f32_16x16x32_bf16 v[56:59], v[68:71], v[216:219], v[56:59]\n"
  "ds_read_b64_tr_b16 v[64:65], v91 offset:28672\n"
  "ds_read_b64_tr_b16 v[66:67], v91 offset:29184\n"
  "v_mfma_f32_16x16x32_bf16 v[60:63], v[68:71], v[220:223], v[60:63]\n"
  "s_waitcnt lgkmcnt(6)\n"
  "v_mfma_f32_16x16x32_bf16 v[48:51], v[72:75], v[224:227], v[48:51]\n"
  "ds_read_b64_tr_b16 v[68:69], v90 offset:32768\n"
  "ds_read_b64_tr_b16 v[70:71], v90 offset:33280\n"
  "v_mul_f32 v4, v4, v20\n"
  "s_add_u32 s44, s30, 8192\n"
  "buffer_store_dwordx4 v[16:19], v100, s[20:23], s44 offen offset:0 sc1\n"
  "v_mfma_f32_16x16x32_bf16 v[52:55], v[72:75], v[228:231], v[52:55]\n"
  "v_mul_f32 v5, v5, v21\n"
  "s_waitcnt lgkmcnt(6)\n"
  "v_mfma_f32_16x16x32_bf16 v[56:59], v[76:79], v[224:227], v[56:59]\n"
  "ds_read_b64_tr_b16 v[72:73], v91 offset:32768\n"
  "ds_read_b64_tr_b16 v[74:75], v91 offset:33280\n"
  "v_mul_f32 v6, v6, v22\n"
  "v_mfma_f32_16x16x32_bf16 v[60:63], v[76:79], v[228:231], v[60:63]\n"
  "s_setprio 0\n"
  "s_waitcnt lgkmcnt(6)\n"
  "v_mfma_f32_16x16x32_bf16 v[48:51], v[80:83], v[232:235], v[48:51]\n"
  "ds_read_b64_tr_b16 v[76:77], v90 offset:36864\n"
  "ds_read_b64_tr_b16 v[78:79], v90 offset:37376\n"
  "v_mul_f32 v7, v7, v23\n"
  "v_mfma_f32_16x16x32_bf16 v[52:55], v[80:83], v[236:239], v[52:55]\n"
  "v_mul_f32 v12, v12, v28\n"
  "s_waitcnt lgkmcnt(6)\n"
  "v_mfma_f32_16x16x32_bf16 v[56:59], v[64:67], v[232:235], v[56:59]\n"
  "ds_read_b64_tr_b16 v[80:81], v91 offset:36864\n"
  "ds_read_b64_tr_b16 v[82:83], v91 offset:37376\n"
  "v_mul_f32 v13, v13, v29\n"
  "v_mfma_f32_16x16x32_bf16 v[60:63], v[64:67], v[236:239], v[60:63]\n"
  "s_waitcnt lgkmcnt(6)\n"
  "v_mfma_f32_16x16x32_bf16 v[48:51], v[68:71], v[240:243], v[48:51]\n"
  "ds_read_b64_tr_b16 v[64:65], v92 offset:4096\n"
  "ds_read_b64_tr_b16 v[66:67], v92 offset:4352\n"
  "v_mul_f32 v14, v14, v30\n"
  "v_mfma_f32_16x16x32_bf16 v[52:55], v[68:71], v[244:247], v[52:55]\n"
  "v_mul_f32 v15, v15, v31\n"
  "s_waitcnt lgkmcnt(6)\n"
  "v_mfma_f32_16x16x32_bf16 v[56:59], v[72:75], v[240:243], v[56:59]\n"
  "ds_read_b64_tr_b16 v[68:69], v92 offset:22528\n"
  "ds_read_b64_tr_b16 v[70:71], v92 offset:22784\n"
  "v_cvt_pk_bf16_f32 v20, v4, v5\n"
  "v_mfma_f32_16x16x32_bf16 v[60:63], v[72:75], v[244:247], v[60:63]\n"
  "s_waitcnt lgkmcnt(6)\n"
  "v_mfma_f32_16x16x32_bf16 v[48:51], v[76:79], v[248:251], v[48:51]\n"
  "ds_read_b64_tr_b16 v[72:73], v92 offset:5120\n"
  "ds_read_b64_tr_b16 v[74:75], v92 offset:5376\n"
  "v_cvt_pk_bf16_f32 v21, v6, v7\n"
  "v_mfma_f32_16x16x32_bf16 v[52:55], v[76:79], v[252:255], v[52:55]\n"
  "v_cvt_pk_bf16_f32 v22, v12, v13\n"
  "s_waitcnt lgkmcnt(6)\n"
  "v_mfma_f32_16x16x32_bf16 v[56:59], v[80:83], v[248:251], v[56:59]\n"
  "ds_read_b64_tr_b16 v[76:77], v92 offset:23552\n"
  "ds_read_b64_tr_b16 v[78:79], v92 offset:23808\n"
  "v_cvt_pk_bf16_f32 v23, v14, v15\n"
  "v_mfma_f32_16x16x32_bf16 v[60:63], v[80:83], v[252:255], v[60:63]\n"
  "s_waitcnt lgkmcnt(6)\n"
  "v_mfma_f32_16x16x32_bf16 v[16:19], v[64:67], v[144:147], 0\n"
  "ds_read_b64_tr_b16 v[80:81], v92 offset:8192\n"
  "ds_read_b64_tr_b16 v[82:83], v92 offset:8448\n"
  "s_cmp_ge_u32 s68, 4\n"
  "s_cbranch_scc1 LGEMM_%=\n"
  "v_mfma_f32_16x16x32_bf16 v[24:27], v[64:67], v[148:151], 0\n"
  "s_waitcnt lgkmcnt(6)\n"
  "v_mfma_f32_16x16x32_bf16 v[0:3], v[68:71], v[144:147], 0\n"
  "ds_read_b64_tr_b16 v[64:65], v92 offset:26624\n"
  "ds_read_b64_tr_b16 v[66:67], v92 offset:26880\n"
  "v_mfma_f32_16x16x32_bf16 v[8:11], v[68:71], v[148:151], 0\n"
  "s_waitcnt vmcnt(1)\n"
  "v_cmp_ne_u32 vcc, s47, v99\n"
  "s_cbranch_vccnz LSAatt_%=\n"
  "LSAattB_%=:\n"
  "s_waitcnt lgkmcnt(6)\n"
  "v_mfma_f32_16x16x32_bf16 v[16:19], v[72:75], v[152:155], v[16:19]\n"
  "s_barrier\n"
  "ds_read_b64_tr_b16 v[68:69], v92 offset:9216\n"
  "ds_read_b64_tr_b16 v[70:71], v92 offset:9472\n"
  "v_mul_f32 v48, v48, v32\n"
  "s_setprio 3\n"
  "v_mfma_f32_16x16x32_bf16 v[24:27], v[72:75], v[156:159], v[24:27]\n"
  "v_mul_f32 v49, v49, v33\n"
  "s_waitcnt lgkmcnt(6)\n"
  "v_mfma_f32_16x16x32_bf16 v[0:3], v[76:79], v[152:155], v[0:3]\n"
  "ds_read_b64_tr_b16 v[72:73], v92 offset:27648\n"
  "ds_read_b64_tr_b16 v[74:75], v92 offset:27904\n"
  "v_mul_f32 v50, v50, v34\n"
  "v_mfma_f32_16x16x32_bf16 v[8:11], v[76:79], v[156:159], v[8:11]\n"
  "v_mul_f32 v51, v51, v35\n"
  "s_movk_i32 s43, 0xf0\n"
  "v_lshrrev_b32 v153, 1, v100\n"
  "v_and_b32 v153, 0x100, v153\n"
  "v_and_or_b32 v153, v100, s43, v153\n"
  "v_bfe_u32 v152, v100, 8, 1\n"
  "v_lshl_or_b32 v153, v152, 3, v153\n"
  "s_waitcnt lgkmcnt(6)\n"
  "v_mfma_f32_16x16x32_bf16 v[16:19], v[80:83], v[176:179], v[16:19]\n"
  "ds_read_b64_tr_b16 v[76:77], v92 offset:10240\n"
  "ds_read_b64_tr_b16 v[78:79], v92 offset:10496\n"
  "v_mul_f32 v56, v56, v40\n"
  "v_mfma_f32_16x16x32_bf16 v[24:27], v[80:83], v[180:183], v[24:27]\n"
  "v_mul_f32 v57, v57, v41\n"
  "buffer_load_dwordx2 v[144:145], v153, s[20:23], s74 offen offset:0 sc1\n"
  "s_waitcnt lgkmcnt(6)\n"
  "v_mfma_f32_16x16x32_bf16 v[0:3], v[64:67], v[176:179], v[0:3]\n"
  "ds_read_b64_tr_b16 v[80:81], v92 offset:28672\n"
  "ds_read_b64_tr_b16 v[82:83], v92 offset:28928\n"
  "s_add_u32 s44, s30, 8192\n"
  "buffer_store_dwordx4 v[20:23], v100, s[20:23], s44 offen offset:1024 sc1\n"
  "v_mul_f32 v58, v58, v42\n"
  "v_mfma_f32_16x16x32_bf16 v[8:11], v[64:67], v[180:183], v[8:11]\n"
  "v_mul_f32 v59, v59, v43\n"
  "buffer_load_dwordx2 v[146:147], v153, s[20:23], s74 offen offset:512 sc1\n"
  "s_waitcnt lgkmcnt(6)\n"
  "v_mfma_f32_16x16x32_bf16 v[16:19], v[68:71], v[184:187], v[16:19]\n"
  "ds_read_b64_tr_b16 v[64:65], v92 offset:11264\n"
  "ds_read_b64_tr_b16 v[66:67], v92 offset:11520\n"
  "v_cvt_pk_bf16_f32 v32, v48, v49\n"
  "v_mfma_f32_16x16x32_bf16 v[24:27], v[68:71], v[188:191], v[24:27]\n"
  "v_cvt_pk_bf16_f32 v33, v50, v51\n"
  "buffer_load_dwordx2 v[148:149], v153, s[20:23], s74 offen offset:1024 sc1\n"
  "s_waitcnt lgkmcnt(6)\n"
  "v_mfma_f32_16x16x32_bf16 v[0:3], v[72:75], v[184:187], v[0:3]\n"
  "ds_read_b64_tr_b16 v[68:69], v92 offset:29696\n"
  "ds_read_b64_tr_b16 v[70:71], v92 offset:29952\n"
  "v_cvt_pk_bf16_f32 v34, v56, v57\n"
  "v_mfma_f32_16x16x32_bf16 v[8:11], v[72:75], v[188:191], v[8:11]\n"
  "v_cvt_pk_bf16_f32 v35, v58, v59\n"
  "buffer_load_dwordx2 v[150:151], v153, s[20:23], s74 offen offset:1536 sc1\n"
  "s_waitcnt lgkmcnt(6)\n"
  "v_mfma_f32_16x16x32_bf16 v[16:19], v[76:79], v[192:195], v[16:19]\n"
  "ds_read_b64_tr_b16 v[72:73], v92 offset:12288\n"
  "ds_read_b64_tr_b16 v[74:75], v92 offset:12544\n"
  "v_mfma_f32_16x16x32_bf16 v[24:27], v[76:79], v[196:199], v[24:27]\n"
  "v_mul_f32 v52, v52, v36\n"
  "buffer_load_dword v152, v153, s[20:23], s75 offen sc1\n"
  "s_waitcnt lgkmcnt(6)\n"
  "v_mfma_f32_16x16x32_bf16 v[0:3], v[80:83], v[192:195], v[0:3]\n"
  "ds_read_b64_tr_b16 v[76:77], v92 offset:30720\n"
  "ds_read_b64_tr_b16 v[78:79], v92 offset:30976\n"
  "v_mul_f32 v53, v53, v37\n"
  "v_mfma_f32_16x16x32_bf16 v[8:11], v[80:83], v[196:199], v[8:11]\n"
  "v_mul_f32 v54, v54, v38\n"
  "s_waitcnt lgkmcnt(6)\n"
  "v_mfma_f32_16x16x32_bf16 v[16:19], v[64:67], v[200:203], v[16:19]\n"
  "ds_read_b64_tr_b16 v[80:81], v92 offset:6144\n"
  "ds_read_b64_tr_b16 v[82:83], v92 offset:6400\n"
  "v_mul_f32 v55, v55, v39\n"
  "v_mfma_f32_16x16x32_bf16 v[24:27], v[64:67], v[204:207], v[24:27]\n"
  "v_mul_f32 v60, v60, v44\n"
  "s_setprio 2\n"
  "s_waitcnt lgkmcnt(6)\n"
  "v_mfma_f32_16x16x32_bf16 v[0:3], v[68:71], v[200:203], v[0:3]\n"
  "ds_read_b64_tr_b16 v[64:65], v92 offset:7168\n"
  "ds_read_b64_tr_b16 v[66:67], v92 offset:7424\n"
  "v_mul_f32 v61, v61, v45\n"
  "v_mfma_f32_16x16x32_bf16 v[8:11], v[68:71], v[204:207], v[8:11]\n"
  "v_mul_f32 v62, v62, v46\n"
  "s_waitcnt lgkmcnt(6)\n"
  "v_mfma_f32_16x16x32_bf16 v[16:19], v[72:75], v[208:211], v[16:19]\n"
  "ds_read_b64_tr_b16 v[68:69], v92 offset:24576\n"
  "ds_read_b64_tr_b16 v[70:71], v92 offset:24832\n"
  "v_mul_f32 v63, v63, v47\n"
  "v_mfma_f32_16x16x32_bf16 v[24:27], v[72:75], v[212:215], v[24:27]\n"
  "v_cvt_pk_bf16_f32 v36, v52, v53\n"
  "s_waitcnt lgkmcnt(6)\n"
  "v_mfma_f32_16x16x32_bf16 v[0:3], v[76:79], v[208:211], v[0:3]\n"
  "ds_read_b64_tr_b16 v[72:73], v92 offset:13312\n"
  "ds_read_b64_tr_b16 v[74:75], v92 offset:13568\n"
  "v_cvt_pk_bf16_f32 v37, v54, v55\n"
  "v_mfma_f32_16x16x32_bf16 v[8:11], v[76:79], v[212:215], v[8:11]\n"
  "v_cvt_pk_bf16_f32 v38, v60, v61\n"
  "s_waitcnt lgkmcnt(6)\n"
  "v_mfma_f32_16x16x32_bf16 v[16:19], v[80:83], v[160:163], v[16:19]\n"
  "ds_read_b64_tr_b16 v[76:77], v92 offset:14336\n"
  "ds_read_b64_tr_b16 v[78:79], v92 offset:14592\n"
  "v_cvt_pk_bf16_f32 v39, v62, v63\n"
  "v_mfma_f32_16x16x32_bf16 v[24:27], v[80:83], v[164:167], v[24:27]\n"
  "s_waitcnt lgkmcnt(6)\n"
  "v_mfma_f32_16x16x32_bf16 v[16:19], v[64:67], v[168:171], v[16:19]\n"
  "ds_read_b64_tr_b16 v[80:81], v92 offset:25600\n"
  "ds_read_b64_tr_b16 v[82:83], v92 offset:25856\n"
  "v_mfma_f32_16x16x32_bf16 v[24:27], v[64:67], v[172:175], v[24:27]\n"
  "s_waitcnt lgkmcnt(6)\n"
  "v_mfma_f32_16x16x32_bf16 v[0:3], v[68:71], v[160:163], v[0:3]\n"
  "ds_read_b64_tr_b16 v[64:65], v92 offset:15360\n"
  "ds_read_b64_tr_b16 v[66:67], v92 offset:15616\n"
  "s_add_u32 s44, s30, 12288\n"
  "buffer_store_dwordx4 v[32:35], v100, s[20:23], s44 offen offset:0 sc1\n"
  "v_mfma_f32_16x16x32_bf16 v[8:11], v[68:71], v[164:167], v[8:11]\n"
  "s_waitcnt lgkmcnt(6)\n"
  "v_mfma_f32_16x16x32_bf16 v[16:19], v[72:75], v[216:219], v[16:19]\n"
  "ds_read_b64_tr_b16 v[68:69], v92 offset:16384\n"
  "ds_read_b64_tr_b16 v[70:71], v92 offset:16640\n"
  "v_mfma_f32_16x16x32_bf16 v[24:27], v[72:75], v[220:223], v[24:27]\n"
  "s_waitcnt lgkmcnt(6)\n"
  "v_mfma_f32_16x16x32_bf16 v[16:19], v[76:79], v[224:227], v[16:19]\n"
  "ds_read_b64_tr_b16 v[72:73], v92 offset:31744\n"
  "ds_read_b64_tr_b16 v[74:75], v92 offset:32000\n"
  "v_mfma_f32_16x16x32_bf16 v[24:27], v[76:79], v[228:231], v[24:27]\n"
  "s_waitcnt lgkmcnt(6)\n"
  "v_mfma_f32_16x16x32_bf16 v[0:3], v[80:83], v[168:171], v[0:3]\n"
  "ds_read_b64_tr_b16 v[76:77], v92 offset:17408\n"
  "ds_read_b64_tr_b16 v[78:79], v92 offset:17664\n"
  "s_setprio 1\n"
  "v_mfma_f32_16x16x32_bf16 v[8:11], v[80:83], v[172:175], v[8:11]\n"
  "s_waitcnt lgkmcnt(6)\n"
  "v_mfma_f32_16x16x32_bf16 v[16:19], v[64:67], v[232:235], v[16:19]\n"
  "ds_read_b64_tr_b16 v[80:81], v92 offset:0\n"
  "ds_read_b64_tr_b16 v[82:83], v92 offset:256\n"
  "v_mfma_f32_16x16x32_bf16 v[24:27], v[64:67], v[236:239], v[24:27]\n"
  "s_waitcnt lgkmcnt(6)\n"
  "v_mfma_f32_16x16x32_bf16 v[16:19], v[68:71], v[240:243], v[16:19]\n"
  "ds_read_b64_tr_b16 v[64:65], v92 offset:32768\n"
  "ds_read_b64_tr_b16 v[66:67], v92 offset:33024\n"
  "v_mfma_f32_16x16x32_bf16 v[24:27], v[68:71], v[244:247], v[24:27]\n"
  "s_waitcnt lgkmcnt(6)\n"
  "v_mfma_f32_16x16x32_bf16 v[0:3], v[72:75], v[216:219], v[0:3]\n"
  "ds_read_b64_tr_b16 v[68:69], v92 offset:1024\n"
  "ds_read_b64_tr_b16 v[70:71], v92 offset:1280\n"
  "v_mfma_f32_16x16x32_bf16 v[8:11], v[72:75], v[220:223], v[8:11]\n"
  "s_waitcnt lgkmcnt(6)\n"
  "v_mfma_f32_16x16x32_bf16 v[16:19], v[76:79], v[248:251], v[16:19]\n"
  "ds_read_b64_tr_b16 v[72:73], v92 offset:2048\n"
  "ds_read_b64_tr_b16 v[74:75], v92 offset:2304\n"
  "v_mfma_f32_16x16x32_bf16 v[24:27], v[76:79], v[252:255], v[24:27]\n"
  "s_waitcnt lgkmcnt(6)\n"
  "v_mfma_f32_16x16x32_bf16 v[16:19], v[80:83], v[112:115], v[16:19]\n"
  "ds_read_b64_tr_b16 v[76:77], v92 offset:33792\n"
  "ds_read_b64_tr_b16 v[78:79], v92 offset:34048\n"
  "v_mfma_f32_16x16x32_bf16 v[24:27], v[80:83], v[116:119], v[24:27]\n"
  "s_waitcnt lgkmcnt(6)\n"
  "v_mfma_f32_16x16x32_bf16 v[0:3], v[64:67], v[224:227], v[0:3]\n"
  "ds_read_b64_tr_b16 v[80:81], v92 offset:3072\n"
  "ds_read_b64_tr_b16 v[82:83], v92 offset:3328\n"
  "v_mfma_f32_16x16x32_bf16 v[8:11], v[64:67], v[228:231], v[8:11]\n"
  "s_waitcnt lgkmcnt(6)\n"
  "v_mfma_f32_16x16x32_bf16 v[16:19], v[68:71], v[120:123], v[16:19]\n"
  "ds_read_b64_tr_b16 v[64:65], v92 offset:34816\n"
  "ds_read_b64_tr_b16 v[66:67], v92 offset:35072\n"
  "v_mfma_f32_16x16x32_bf16 v[24:27], v[68:71], v[124:127], v[24:27]\n"
  "s_waitcnt lgkmcnt(6)\n"
  "v_mfma_f32_16x16x32_bf16 v[16:19], v[72:75], v[128:131], v[16:19]\n"
  "ds_read_b64_tr_b16 v[68:69], v92 offset:35840\n"
  "ds_read_b64_tr_b16 v[70:71], v92 offset:36096\n"
  "v_mfma_f32_16x16x32_bf16 v[24:27], v[72:75], v[132:135], v[24:27]\n"
  "s_setprio 0\n"
  "s_waitcnt lgkmcnt(6)\n"
  "v_mfma_f32_16x16x32_bf16 v[0:3], v[76:79], v[232:235], v[0:3]\n"
  "ds_read_b64_tr_b16 v[72:73], v92 offset:18432\n"
  "ds_read_b64_tr_b16 v[74:75], v92 offset:18688\n"
  "s_add_u32 s44, s30, 12288\n"
  "buffer_store_dwordx4 v[36:39], v100, s[20:23], s44 offen offset:1024 sc1\n"
  "v_mfma_f32_16x16x32_bf16 v[8:11], v[76:79], v[236:239], v[8:11]\n"
  "s_waitcnt lgkmcnt(6)\n"
  "v_mfma_f32_16x16x32_bf16 v[16:19], v[80:83], v[136:139], v[16:19]\n"
  "ds_read_b64_tr_b16 v[76:77], v92 offset:19456\n"
  "ds_read_b64_tr_b16 v[78:79], v92 offset:19712\n"
  "v_mfma_f32_16x16x32_bf16 v[24:27], v[80:83], v[140:143], v[24:27]\n"
  "s_waitcnt lgkmcnt(6)\n"
  "v_mfma_f32_16x16x32_bf16 v[0:3], v[64:67], v[240:243], v[0:3]\n"
  "ds_read_b64_tr_b16 v[80:81], v92 offset:20480\n"
  "ds_read_b64_tr_b16 v[82:83], v92 offset:20736\n"
  "v_mfma_f32_16x16x32_bf16 v[8:11], v[64:67], v[244:247], v[8:11]\n"
  "v_mul_f32 v102, v16, v16\n"
  "v_mul_f32 v103, v17, v17\n"
  "v_mul_f32 v104, v18, v18\n"
  "v_mul_f32 v105, v19, v19\n"
  "v_fmaak_f32 v102, v102, v101, 0xc0135761\n"
  "v_fmaak_f32 v103, v103, v101, 0xc0135761\n"
  "s_waitcnt lgkmcnt(6)\n"
  "v_mfma_f32_16x16x32_bf16 v[0:3], v[68:71], v[248:251], v[0:3]\n"
  "ds_read_b64_tr_b16 v[64:65], v92 offset:21504\n"
  "ds_read_b64_tr_b16 v[66:67], v92 offset:21760\n"
  "v_fmaak_f32 v104, v104, v101, 0xc0135761\n"
  "v_fmaak_f32 v105, v105, v101, 0xc0135761\n"
  "v_mul_f32 v102, v102, v16\n"
  "v_mul_f32 v103, v103, v17\n"
  "v_mul_f32 v104, v104, v18\n"
  "v_mfma_f32_16x16x32_bf16 v[8:11], v[68:71], v[252:255], v[8:11]\n"
  "v_mul_f32 v105, v105, v19\n"
  "v_exp_f32 v102, v102\n"
  "v_exp_f32 v103, v103\n"
  "v_exp_f32 v104, v104\n"
  "v_exp_f32 v105, v105\n"
  "s_waitcnt lgkmcnt(6)\n"
  "v_mfma_f32_16x16x32_bf16 v[0:3], v[72:75], v[112:115], v[0:3]\n"
  "v_add_f32 v102, 1.0, v102\n"
  "v_add_f32 v103, 1.0, v103\n"
  "v_add_f32 v104, 1.0, v104\n"
  "v_add_f32 v105, 1.0, v105\n"
  "v_rcp_f32 v102, v102\n"
  "v_mfma_f32_16x16x32_bf16 v[8:11], v[72:75], v[116:119], v[8:11]\n"
  "v_rcp_f32 v103, v103\n"
  "v_rcp_f32 v104, v104\n"
  "v_rcp_f32 v105, v105\n"
  "v_mul_f32 v16, v16, v102\n"
  "v_mul_f32 v17, v17, v103\n"
  "s_waitcnt lgkmcnt(4)\n"
  "v_mfma_f32_16x16x32_bf16 v[0:3], v[76:79], v[120:123], v[0:3]\n"
  "v_mul_f32 v18, v18, v104\n"
  "v_mul_f32 v19, v19, v105\n"
  "v_mul_f32 v106, v24, v24\n"
  "v_mul_f32 v107, v25, v25\n"
  "v_mul_f32 v108, v26, v26\n"
  "v_mfma_f32_16x16x32_bf16 v[8:11], v[76:79], v[124:127], v[8:11]\n"
  "v_mul_f32 v109, v27, v27\n"
  "v_fmaak_f32 v106, v106, v101, 0xc0135761\n"
  "v_fmaak_f32 v107, v107, v101, 0xc0135761\n"
  "v_fmaak_f32 v108, v108, v101, 0xc0135761\n"
  "v_fmaak_f32 v109, v109, v101, 0xc0135761\n"
  "s_waitcnt lgkmcnt(2)\n"
  "v_mfma_f32_16x16x32_bf16 v[0:3], v[80:83], v[128:131], v[0:3]\n"
  "v_mul_f32 v106, v106, v24\n"
  "v_mul_f32 v107, v107, v25\n"
  "v_mul_f32 v108, v108, v26\n"
  "v_mul_f32 v109, v109, v27\n"
  "v_exp_f32 v106, v106\n"
  "v_mfma_f32_16x16x32_bf16 v[8:11], v[80:83], v[132:135], v[8:11]\n"
  "v_exp_f32 v107, v107\n"
  "v_exp_f32 v108, v108\n"
  "v_exp_f32 v109, v109\n"
  "v_add_f32 v106, 1.0, v106\n"
  "v_add_f32 v107, 1.0, v107\n"
  "s_waitcnt lgkmcnt(0)\n"
  "v_mfma_f32_16x16x32_bf16 v[0:3], v[64:67], v[136:139], v[0:3]\n"
  "v_add_f32 v108, 1.0, v108\n"
  "v_add_f32 v109, 1.0, v109\n"
  "v_rcp_f32 v106, v106\n"
  "v_rcp_f32 v107, v107\n"
  "v_rcp_f32 v108, v108\n"
  "v_mfma_f32_16x16x32_bf16 v[8:11], v[64:67], v[140:143], v[8:11]\n"
  "v_rcp_f32 v109, v109\n"
  "v_mul_f32 v24, v24, v106\n"
  "v_mul_f32 v25, v25, v107\n"
  "v_mul_f32 v26, v26, v108\n"
  "v_mul_f32 v27, v27, v109\n"
  "v_mul_f32 v0, v0, v16\n"
  "v_mul_f32 v1, v1, v17\n"
  "v_mul_f32 v2, v2, v18\n"
  "v_mul_f32 v3, v3, v19\n"
  "v_cvt_pk_bf16_f32 v102, v0, v1\n"
  "v_cvt_pk_bf16_f32 v103, v2, v3\n"
  "v_mul_f32 v8, v8, v24\n"
  "v_mul_f32 v9, v9, v25\n"
  "v_mul_f32 v10, v10, v26\n"
  "v_mul_f32 v11, v11, v27\n"
  "v_cvt_pk_bf16_f32 v104, v8, v9\n"
  "v_cvt_pk_bf16_f32 v105, v10, v11\n"
  "buffer_store_dwordx4 v[102:105], v100, s[20:23], s31 offen sc1\n"
  "s_setprio 1\n"
  "v_mov_b32 v248, v100\n"
  "v_lshrrev_b32 v249, 1, v100\n"
  "v_and_b32 v249, 0x1c0, v249\n"
  "v_and_or_b32 v249, v100, 48, v249\n"
  "v_lshrrev_b32 v206, 3, v100\n"
  "v_and_or_b32 v249, v206, 8, v249\n"
  "v_add_u32 v201, 90048, v249\n"
  "v_and_b32 v203, 0x1f0, v100\n"
  "v_add_u32 v203, 90048, v203\n"
  "ds_read2st64_b64 v[168:171], v201 offset0:0 offset1:1\n"
  "ds_read_b128 v[192:195], v203 offset:34816\n"
  "ds_read2st64_b64 v[172:175], v201 offset0:2 offset1:3\n"
  "s_waitcnt vmcnt(3)\n"
  "v_mov_b32 v208, v152\n"
  "v_mov_b32 v209, 0\n"
  "v_mov_b32 v129, 0\n"
  "v_mov_b32 v130, 0\n"
  "v_mov_b32 v131, 0\n"
  "v_mov_b32 v132, 0\n"
  "v_mov_b32 v134, 0\n"
  "v_mov_b32 v135, 0\n"
  "v_mov_b32 v136, 0\n"
  "v_mov_b32 v137, 0\n"
  "v_mov_b32 v139, 0\n"
  "v_mov_b32 v140, 0\n"
  "v_mov_b32 v141, 0\n"
  "v_mov_b32 v142, 0\n"
  "v_mov_b32 v153, 0\n"
  "v_mov_b32 v154, 0\n"
  "v_mov_b32 v155, 0\n"
  "v_mov_b32 v156, 0\n"
  "v_mov_b32 v158, 0\n"
  "v_mov_b32 v159, 0\n"
  "v_mov_b32 v160, 0\n"
  "v_mov_b32 v161, 0\n"
  "v_mov_b32 v163, 0\n"
  "v_mov_b32 v164, 0\n"
  "v_mov_b32 v165, 0\n"
  "v_mov_b32 v166, 0\n"
  "v_permlane32_swap_b32 v208, v209\n"
  "v_mov_b32 v128, v208\n"
  "v_mov_b32 v133, v208\n"
  "v_mov_b32 v138, v208\n"
  "v_mov_b32 v143, v208\n"
  "v_mov_b32 v152, v209\n"
  "v_mov_b32 v157, v209\n"
  "v_mov_b32 v162, v209\n"
  "v_mov_b32 v167, v209\n"
  "s_waitcnt lgkmcnt(2)\n"
  "v_mfma_f32_32x32x16_bf16 v[0:15], v[168:171], v[144:147], 0\n"
  "s_waitcnt lgkmcnt(1)\n"
  "v_mfma_f32_32x32x16_bf16 v[0:15], v[192:195], v[128:131], v[0:15]\n"
  "v_mfma_f32_32x32x16_bf16 v[16:31], v[168:171], v[148:151], 0\n"
  "v_mfma_f32_32x32x16_bf16 v[16:31], v[192:195], v[152:155], v[16:31]\n"
  "s_mov_b32 s26, 0x3ffff\n"
  "s_mov_b32 s27, 0x3ffff\n"
  "s_mov_b32 s24, 0\n"
  "s_mov_b32 s10, 37750719\n"
  "s_mov_b32 s11, 0\n"
  "v_and_b32 v206, 0x1b0, v248\n"
  "v_cmp_eq_u32 vcc, 0x120, v206\n"
  "v_mov_b32 v207, 0x3f803f80\n"
  "v_cndmask_b32 v176, 0, v207, vcc\n"
  "v_mov_b32 v177, v176\n"
  "v_mov_b32 v178, v176\n"
  "v_mov_b32 v179, v176\n"
  "v_mov_b32 v180, v176\n"
  "v_mov_b32 v181, v176\n"
  "v_mov_b32 v182, v176\n"
  "v_mov_b32 v183, v176\n"
  "v_mov_b32 v184, v176\n"
  "v_mov_b32 v185, v176\n"
  "v_mov_b32 v186, v176\n"
  "v_mov_b32 v187, v176\n"
  "v_mov_b32 v188, v176\n"
  "v_mov_b32 v189, v176\n"
  "v_mov_b32 v190, v176\n"
  "v_mov_b32 v191, v176\n"
  "v_and_b32 v253, 0xffff, v111\n"
  "v_add_u32 v202, 90048, v253\n"
  "s_mov_b64 exec, s[26:27]\n"
  "ds_read_b128 v[176:179], v202 offset:0\n"
  "ds_read_b128 v[180:183], v202 offset:64\n"
  "s_mov_b64 exec, -1\n"
  "v_lshrrev_b32 v206, 9, v248\n"
  "v_bfe_u32 v251, v248, 8, 1\n"
  "v_lshlrev_b32 v251, 14, v251\n"
  "v_lshl_add_u32 v251, v206, 8, v251\n"
  "v_and_b32 v207, 0xf0, v248\n"
  "v_add_u32 v251, v251, v207\n"
  "v_bfe_u32 v252, v248, 2, 7\n"
  "v_lshl_add_u32 v252, v206, 30, v252\n"
  "v_mov_b32 v216, -1\n"
  "v_mov_b32 v217, -1\n"
  "v_mov_b32 v218, -1\n"
  "v_mov_b32 v219, -1\n"
  "s_add_u32 s8, s77, 36864\n"
  "s_add_u32 s9, s78, 36864\n"
  "buffer_store_dwordx4 v[216:219], v251, s[20:23], s77 offen sc1\n"
  "v_mov_b32 v200, 0xdddddddd\n"
  "s_lshl_b32 s43, s68, 2\n"
  "s_add_u32 s43, s43, 163776\n"
  "v_mov_b32 v204, s43\n"
  "v_bfe_u32 v205, v248, 2, 5\n"
  "v_add_u32 v205, 163776, v205\n"
  "v_mov_b32 v64, 0\n"
  "v_mov_b32 v65, 0\n"
  "v_mov_b32 v66, 0\n"
  "v_mov_b32 v67, 0\n"
  "v_mov_b32 v68, 0\n"
  "v_mov_b32 v69, 0\n"
  "v_mov_b32 v70, 0\n"
  "v_mov_b32 v71, 0\n"
  "v_mov_b32 v72, 0\n"
  "v_mov_b32 v73, 0\n"
  "v_mov_b32 v74, 0\n"
  "v_mov_b32 v75, 0\n"
  "v_mov_b32 v76, 0\n"
  "v_mov_b32 v77, 0\n"
  "v_mov_b32 v78, 0\n"
  "v_mov_b32 v79, 0\n"
  "v_mov_b32 v80, 0\n"
  "v_mov_b32 v81, 0\n"
  "v_mov_b32 v82, 0\n"
  "v_mov_b32 v83, 0\n"
  "v_mov_b32 v84, 0\n"
  "v_mov_b32 v85, 0\n"
  "v_mov_b32 v86, 0\n"
  "v_mov_b32 v87, 0\n"
  "v_mov_b32 v88, 0\n"
  "v_mov_b32 v89, 0\n"
  "v_mov_b32 v90, 0\n"
  "v_mov_b32 v91, 0\n"
  "v_mov_b32 v92, 0\n"
  "v_mov_b32 v93, 0\n"
  "v_mov_b32 v94, 0\n"
  "v_mov_b32 v95, 0\n"
  "buffer_store_dword v216, v252, s[20:23], s78 offen sc1\n"
  "v_mov_b32 v56, 0\n"
  "v_mov_b32 v57, 0\n"
  "v_mov_b32 v58, 0\n"
  "v_mov_b32 v59, 0\n"
  "v_mov_b32 v60, 0\n"
  "v_mov_b32 v61, 0\n"
  "v_mov_b32 v62, 0\n"
  "v_mov_b32 v63, 0\n"
  "v_max_f32 v206, v0, v1\n"
  "v_max3_f32 v206, v206, v2, v3\n"
  "v_max3_f32 v206, v206, v4, v5\n"
  "v_max3_f32 v206, v206, v6, v7\n"
  "v_max3_f32 v206, v206, v8, v9\n"
  "v_max3_f32 v206, v206, v10, v11\n"
  "v_max3_f32 v206, v206, v12, v13\n"
  "v_max3_f32 v206, v206, v14, v15\n"
  "v_mov_b32 v207, v206\n"
  "s_nop 1\n"
  "v_permlane32_swap_b32 v206, v207\n"
  "v_max_f32 v206, v206, v207\n"
  "v_sub_f32 v96, 0, v206\n"
  "v_sub_f32 v97, 0, v206\n"
  "v_sub_f32 v98, 0, v206\n"
  "v_sub_f32 v99, 0, v206\n"
  "v_sub_f32 v100, 0, v206\n"
  "v_sub_f32 v101, 0, v206\n"
  "v_sub_f32 v102, 0, v206\n"
  "v_sub_f32 v103, 0, v206\n"
  "v_sub_f32 v104, 0, v206\n"
  "v_sub_f32 v105, 0, v206\n"
  "v_sub_f32 v106, 0, v206\n"
  "v_sub_f32 v107, 0, v206\n"
  "v_sub_f32 v108, 0, v206\n"
  "v_sub_f32 v109, 0, v206\n"
  "v_sub_f32 v110, 0, v206\n"
  "v_sub_f32 v111, 0, v206\n"
  "buffer_store_dwordx4 v[216:219], v251, s[20:23], s8 offen sc1\n"
  "v_max_f32 v206, v16, v17\n"
  "v_max3_f32 v206, v206, v18, v19\n"
  "v_max3_f32 v206, v206, v20, v21\n"
  "v_max3_f32 v206, v206, v22, v23\n"
  "v_max3_f32 v206, v206, v24, v25\n"
  "v_max3_f32 v206, v206, v26, v27\n"
  "v_max3_f32 v206, v206, v28, v29\n"
  "v_max3_f32 v206, v206, v30, v31\n"
  "v_mov_b32 v207, v206\n"
  "s_nop 1\n"
  "v_permlane32_swap_b32 v206, v207\n"
  "v_max_f32 v206, v206, v207\n"
  "v_sub_f32 v112, 0, v206\n"
  "v_sub_f32 v113, 0, v206\n"
  "v_sub_f32 v114, 0, v206\n"
  "v_sub_f32 v115, 0, v206\n"
  "v_sub_f32 v116, 0, v206\n"
  "v_sub_f32 v117, 0, v206\n"
  "v_sub_f32 v118, 0, v206\n"
  "v_sub_f32 v119, 0, v206\n"
  "v_sub_f32 v120, 0, v206\n"
  "v_sub_f32 v121, 0, v206\n"
  "v_sub_f32 v122, 0, v206\n"
  "v_sub_f32 v123, 0, v206\n"
  "v_sub_f32 v124, 0, v206\n"
  "v_sub_f32 v125, 0, v206\n"
  "v_sub_f32 v126, 0, v206\n"
  "v_sub_f32 v127, 0, v206\n"
  "buffer_store_dword v216, v252, s[20:23], s9 offen sc1\n"
  "v_mfma_f32_32x32x16_bf16 v[0:15], v[168:171], v[144:147], v[96:111]\n"
  "v_mfma_f32_32x32x16_bf16 v[0:15], v[192:195], v[128:131], v[0:15]\n"
  "s_mov_b32 s25, 4\n"
  "LLOOP_%=:\n"
  "v_mfma_f32_32x32x16_bf16 v[16:31], v[168:171], v[148:151], v[112:127]\n"
  "ds_read2st64_b64 v[168:171], v201 offset0:4 offset1:5\n"
  "s_nop 8\n"
  "v_exp_f32 v0, v0\n"
  "v_exp_f32 v1, v1\n"
  "v_exp_f32 v2, v2\n"
  "v_exp_f32 v3, v3\n"
  "v_mfma_f32_32x32x16_bf16 v[16:31], v[192:195], v[152:155], v[16:31]\n"
  "v_exp_f32 v4, v4\n"
  "v_exp_f32 v5, v5\n"
  "v_exp_f32 v6, v6\n"
  "v_exp_f32 v7, v7\n"
  "v_smfmac_f32_32x32x32_bf16 v[80:95], v[188:191], v[56:63], v200\n"
  "s_mov_b64 exec, s[26:27]\n"
  "ds_read_b128 v[184:187], v202 offset:1152\n"
  "ds_read_b128 v[188:191], v202 offset:1216\n"
  "s_mov_b64 exec, -1\n"
  "v_exp_f32 v8, v8\n"
  "v_exp_f32 v9, v9\n"
  "v_exp_f32 v10, v10\n"
  "v_exp_f32 v11, v11\n"
  "s_waitcnt lgkmcnt(4)\n"
  "v_smfmac_f32_32x32x32_bf16 v[64:79], v[176:179], v[0:7], v200\n"
  "v_exp_f32 v12, v12\n"
  "v_exp_f32 v13, v13\n"
  "v_exp_f32 v14, v14\n"
  "v_exp_f32 v15, v15\n"
  "v_mfma_f32_32x32x16_bf16 v[32:47], v[172:175], v[144:147], v[96:111]\n"
  "v_exp_f32 v16, v16\n"
  "v_exp_f32 v17, v17\n"
  "v_exp_f32 v18, v18\n"
  "v_exp_f32 v19, v19\n"
  "v_mfma_f32_32x32x16_bf16 v[32:47], v[192:195], v[132:135], v[32:47]\n"
  "v_exp_f32 v20, v20\n"
  "v_exp_f32 v21, v21\n"
  "v_exp_f32 v22, v22\n"
  "v_exp_f32 v23, v23\n"
  "s_waitcnt lgkmcnt(3)\n"
  "v_smfmac_f32_32x32x32_bf16 v[64:79], v[180:183], v[8:15], v200\n"
  "v_exp_f32 v24, v24\n"
  "v_exp_f32 v25, v25\n"
  "v_exp_f32 v26, v26\n"
  "v_exp_f32 v27, v27\n"
  "v_smfmac_f32_32x32x32_bf16 v[80:95], v[176:179], v[16:23], v200\n"
  "v_exp_f32 v28, v28\n"
  "v_exp_f32 v29, v29\n"
  "v_exp_f32 v30, v30\n"
  "v_exp_f32 v31, v31\n"
  "v_mfma_f32_32x32x16_bf16 v[48:63], v[172:175], v[148:151], v[112:127]\n"
  "ds_read2st64_b64 v[172:175], v201 offset0:6 offset1:7\n"
  "v_exp_f32 v32, v32\n"
  "v_exp_f32 v33, v33\n"
  "v_exp_f32 v34, v34\n"
  "v_exp_f32 v35, v35\n"
  "v_mfma_f32_32x32x16_bf16 v[48:63], v[192:195], v[156:159], v[48:63]\n"
  "v_exp_f32 v36, v36\n"
  "v_exp_f32 v37, v37\n"
  "v_exp_f32 v38, v38\n"
  "v_exp_f32 v39, v39\n"
  "v_smfmac_f32_32x32x32_bf16 v[80:95], v[180:183], v[24:31], v200\n"
  "s_mov_b64 exec, s[26:27]\n"
  "ds_read_b128 v[176:179], v202 offset:2304\n"
  "ds_read_b128 v[180:183], v202 offset:2368\n"
  "s_mov_b64 exec, -1\n"
  "v_exp_f32 v40, v40\n"
  "v_exp_f32 v41, v41\n"
  "v_exp_f32 v42, v42\n"
  "v_exp_f32 v43, v43\n"
  "s_waitcnt lgkmcnt(4)\n"
  "v_smfmac_f32_32x32x32_bf16 v[64:79], v[184:187], v[32:39], v200\n"
  "v_exp_f32 v44, v44\n"
  "v_exp_f32 v45, v45\n"
  "v_exp_f32 v46, v46\n"
  "v_exp_f32 v47, v47\n"
  "v_mfma_f32_32x32x16_bf16 v[0:15], v[168:171], v[144:147], v[96:111]\n"
  "v_exp_f32 v48, v48\n"
  "v_exp_f32 v49, v49\n"
  "v_exp_f32 v50, v50\n"
  "v_exp_f32 v51, v51\n"
  "v_mfma_f32_32x32x16_bf16 v[0:15], v[192:195], v[136:139], v[0:15]\n"
  "v_exp_f32 v52, v52\n"
  "v_exp_f32 v53, v53\n"
  "v_exp_f32 v54, v54\n"
  "v_exp_f32 v55, v55\n"
  "s_waitcnt lgkmcnt(3)\n"
  "v_smfmac_f32_32x32x32_bf16 v[64:79], v[188:191], v[40:47], v200\n"
  "v_exp_f32 v56, v56\n"
  "v_exp_f32 v57, v57\n"
  "v_exp_f32 v58, v58\n"
  "v_exp_f32 v59, v59\n"
  "v_smfmac_f32_32x32x32_bf16 v[80:95], v[184:187], v[48:55], v200\n"
  "v_exp_f32 v60, v60\n"
  "v_exp_f32 v61, v61\n"
  "v_exp_f32 v62, v62\n"
  "v_exp_f32 v63, v63\n"
  "v_mfma_f32_32x32x16_bf16 v[16:31], v[168:171], v[148:151], v[112:127]\n"
  "ds_read2st64_b64 v[168:171], v201 offset0:8 offset1:9\n"
  "ds_read_b128 v[196:199], v203 offset:35328\n"
  "v_exp_f32 v0, v0\n"
  "v_exp_f32 v1, v1\n"
  "v_exp_f32 v2, v2\n"
  "v_exp_f32 v3, v3\n"
  "v_mfma_f32_32x32x16_bf16 v[16:31], v[192:195], v[160:163], v[16:31]\n"
  "v_exp_f32 v4, v4\n"
  "v_exp_f32 v5, v5\n"
  "v_exp_f32 v6, v6\n"
  "v_exp_f32 v7, v7\n"
  "v_smfmac_f32_32x32x32_bf16 v[80:95], v[188:191], v[56:63], v200\n"
  "s_mov_b64 exec, s[26:27]\n"
  "ds_read_b128 v[184:187], v202 offset:3456\n"
  "ds_read_b128 v[188:191], v202 offset:3520\n"
  "s_mov_b64 exec, -1\n"
  "v_exp_f32 v8, v8\n"
  "v_exp_f32 v9, v9\n"
  "v_exp_f32 v10, v10\n"
  "v_exp_f32 v11, v11\n"
  "s_waitcnt lgkmcnt(5)\n"
  "v_smfmac_f32_32x32x32_bf16 v[64:79], v[176:179], v[0:7], v200\n"
  "v_exp_f32 v12, v12\n"
  "v_exp_f32 v13, v13\n"
  "v_exp_f32 v14, v14\n"
  "v_exp_f32 v15, v15\n"
  "v_mfma_f32_32x32x16_bf16 v[32:47], v[172:175], v[144:147], v[96:111]\n"
  "v_exp_f32 v16, v16\n"
  "v_exp_f32 v17, v17\n"
  "v_exp_f32 v18, v18\n"
  "v_exp_f32 v19, v19\n"
  "v_mfma_f32_32x32x16_bf16 v[32:47], v[192:195], v[140:143], v[32:47]\n"
  "v_exp_f32 v20, v20\n"
  "v_exp_f32 v21, v21\n"
  "v_exp_f32 v22, v22\n"
  "v_exp_f32 v23, v23\n"
  "s_waitcnt lgkmcnt(4)\n"
  "v_smfmac_f32_32x32x32_bf16 v[64:79], v[180:183], v[8:15], v200\n"
  "v_exp_f32 v24, v24\n"
  "v_exp_f32 v25, v25\n"
  "v_exp_f32 v26, v26\n"
  "v_exp_f32 v27, v27\n"
  "v_smfmac_f32_32x32x32_bf16 v[80:95], v[176:179], v[16:23], v200\n"
  "v_exp_f32 v28, v28\n"
  "v_exp_f32 v29, v29\n"
  "v_exp_f32 v30, v30\n"
  "v_exp_f32 v31, v31\n"
  "s_waitcnt vmcnt(0)\n"
  "v_mov_b32 v206, s47\n"
  "s_mov_b64 exec, 1\n"
  "buffer_store_dword v206, off, s[20:23], s69 offset:384 sc1\n"
  "s_mov_b64 exec, -1\n"
  "v_mov_b32 v207, 7\n"
  "ds_write_b32 v204, v207\n"
  "v_mfma_f32_32x32x16_bf16 v[48:63], v[172:175], v[148:151], v[112:127]\n"
  "ds_read2st64_b64 v[172:175], v201 offset0:10 offset1:11\n"
  "v_exp_f32 v32, v32\n"
  "v_exp_f32 v33, v33\n"
  "v_exp_f32 v34, v34\n"
  "v_exp_f32 v35, v35\n"
  "v_mfma_f32_32x32x16_bf16 v[48:63], v[192:195], v[164:167], v[48:63]\n"
  "v_exp_f32 v36, v36\n"
  "v_exp_f32 v37, v37\n"
  "v_exp_f32 v38, v38\n"
  "v_exp_f32 v39, v39\n"
  "v_smfmac_f32_32x32x32_bf16 v[80:95], v[180:183], v[24:31], v200\n"
  "s_mov_b64 exec, s[26:27]\n"
  "ds_read_b128 v[176:179], v202 offset:4608\n"
  "ds_read_b128 v[180:183], v202 offset:4672\n"
  "s_mov_b64 exec, -1\n"
  "v_exp_f32 v40, v40\n"
  "v_exp_f32 v41, v41\n"
  "v_exp_f32 v42, v42\n"
  "v_exp_f32 v43, v43\n"
  "s_waitcnt lgkmcnt(5)\n"
  "v_smfmac_f32_32x32x32_bf16 v[64:79], v[184:187], v[32:39], v200\n"
  "v_exp_f32 v44, v44\n"
  "v_exp_f32 v45, v45\n"
  "v_exp_f32 v46, v46\n"
  "v_exp_f32 v47, v47\n"
  "v_mfma_f32_32x32x16_bf16 v[0:15], v[168:171], v[144:147], v[96:111]\n"
  "v_exp_f32 v48, v48\n"
  "v_exp_f32 v49, v49\n"
  "v_exp_f32 v50, v50\n"
  "v_exp_f32 v51, v51\n"
  "v_mfma_f32_32x32x16_bf16 v[0:15], v[196:199], v[128:131], v[0:15]\n"
  "v_exp_f32 v52, v52\n"
  "v_exp_f32 v53, v53\n"
  "v_exp_f32 v54, v54\n"
  "v_exp_f32 v55, v55\n"
  "s_waitcnt lgkmcnt(4)\n"
  "v_smfmac_f32_32x32x32_bf16 v[64:79], v[188:191], v[40:47], v200\n"
  "v_exp_f32 v56, v56\n"
  "v_exp_f32 v57, v57\n"
  "v_exp_f32 v58, v58\n"
  "v_exp_f32 v59, v59\n"
  "v_smfmac_f32_32x32x32_bf16 v[80:95], v[184:187], v[48:55], v200\n"
  "v_exp_f32 v60, v60\n"
  "v_exp_f32 v61, v61\n"
  "v_exp_f32 v62, v62\n"
  "v_exp_f32 v63, v63\n"
  "v_mfma_f32_32x32x16_bf16 v[16:31], v[168:171], v[148:151], v[112:127]\n"
  "ds_read2st64_b64 v[168:171], v201 offset0:12 offset1:13\n"
  "v_exp_f32 v0, v0\n"
  "v_exp_f32 v1, v1\n"
  "v_exp_f32 v2, v2\n"
  "v_exp_f32 v3, v3\n"
  "v_mfma_f32_32x32x16_bf16 v[16:31], v[196:199], v[152:155], v[16:31]\n"
  "v_exp_f32 v4, v4\n"
  "v_exp_f32 v5, v5\n"
  "v_exp_f32 v6, v6\n"
  "v_exp_f32 v7, v7\n"
  "v_smfmac_f32_32x32x32_bf16 v[80:95], v[188:191], v[56:63], v200\n"
  "s_mov_b64 exec, s[26:27]\n"
  "ds_read_b128 v[184:187], v202 offset:5760\n"
  "ds_read_b128 v[188:191], v202 offset:5824\n"
  "s_mov_b64 exec, -1\n"
  "v_exp_f32 v8, v8\n"
  "v_exp_f32 v9, v9\n"
  "v_exp_f32 v10, v10\n"
  "v_exp_f32 v11, v11\n"
  "s_waitcnt lgkmcnt(4)\n"
  "v_smfmac_f32_32x32x32_bf16 v[64:79], v[176:179], v[0:7], v200\n"
  "v_exp_f32 v12, v12\n"
  "v_exp_f32 v13, v13\n"
  "v_exp_f32 v14, v14\n"
  "v_exp_f32 v15, v15\n"
  "v_mfma_f32_32x32x16_bf16 v[32:47], v[172:175], v[144:147], v[96:111]\n"
  "v_exp_f32 v16, v16\n"
  "v_exp_f32 v17, v17\n"
  "v_exp_f32 v18, v18\n"
  "v_exp_f32 v19, v19\n"
  "v_mfma_f32_32x32x16_bf16 v[32:47], v[196:199], v[132:135], v[32:47]\n"
  "v_exp_f32 v20, v20\n"
  "v_exp_f32 v21, v21\n"
  "v_exp_f32 v22, v22\n"
  "v_exp_f32 v23, v23\n"
  "s_waitcnt lgkmcnt(3)\n"
  "v_smfmac_f32_32x32x32_bf16 v[64:79], v[180:183], v[8:15], v200\n"
  "v_exp_f32 v24, v24\n"
  "v_exp_f32 v25, v25\n"
  "v_exp_f32 v26, v26\n"
  "v_exp_f32 v27, v27\n"
  "v_smfmac_f32_32x32x32_bf16 v[80:95], v[176:179], v[16:23], v200\n"
  "v_exp_f32 v28, v28\n"
  "v_exp_f32 v29, v29\n"
  "v_exp_f32 v30, v30\n"
  "v_exp_f32 v31, v31\n"
  "v_mfma_f32_32x32x16_bf16 v[48:63], v[172:175], v[148:151], v[112:127]\n"
  "ds_read2st64_b64 v[172:175], v201 offset0:14 offset1:15\n"
  "v_exp_f32 v32, v32\n"
  "v_exp_f32 v33, v33\n"
  "v_exp_f32 v34, v34\n"
  "v_exp_f32 v35, v35\n"
  "v_mfma_f32_32x32x16_bf16 v[48:63], v[196:199], v[156:159], v[48:63]\n"
  "v_exp_f32 v36, v36\n"
  "v_exp_f32 v37, v37\n"
  "v_exp_f32 v38, v38\n"
  "v_exp_f32 v39, v39\n"
  "v_smfmac_f32_32x32x32_bf16 v[80:95], v[180:183], v[24:31], v200\n"
  "s_mov_b64 exec, s[26:27]\n"
  "ds_read_b128 v[176:179], v202 offset:6912\n"
  "ds_read_b128 v[180:183], v202 offset:6976\n"
  "s_mov_b64 exec, -1\n"
  "v_exp_f32 v40, v40\n"
  "v_exp_f32 v41, v41\n"
  "v_exp_f32 v42, v42\n"
  "v_exp_f32 v43, v43\n"
  "s_waitcnt lgkmcnt(4)\n"
  "v_smfmac_f32_32x32x32_bf16 v[64:79], v[184:187], v[32:39], v200\n"
  "v_exp_f32 v44, v44\n"
  "v_exp_f32 v45, v45\n"
  "v_exp_f32 v46, v46\n"
  "v_exp_f32 v47, v47\n"
  "v_mfma_f32_32x32x16_bf16 v[0:15], v[168:171], v[144:147], v[96:111]\n"
  "v_exp_f32 v48, v48\n"
  "v_exp_f32 v49, v49\n"
  "v_exp_f32 v50, v50\n"
  "v_exp_f32 v51, v51\n"
  "v_mfma_f32_32x32x16_bf16 v[0:15], v[196:199], v[136:139], v[0:15]\n"
  "v_exp_f32 v52, v52\n"
  "v_exp_f32 v53, v53\n"
  "v_exp_f32 v54, v54\n"
  "v_exp_f32 v55, v55\n"
  "s_waitcnt lgkmcnt(3)\n"
  "v_smfmac_f32_32x32x32_bf16 v[64:79], v[188:191], v[40:47], v200\n"
  "v_exp_f32 v56, v56\n"
  "v_exp_f32 v57, v57\n"
  "v_exp_f32 v58, v58\n"
  "v_exp_f32 v59, v59\n"
  "v_smfmac_f32_32x32x32_bf16 v[80:95], v[184:187], v[48:55], v200\n"
  "v_exp_f32 v60, v60\n"
  "v_exp_f32 v61, v61\n"
  "v_exp_f32 v62, v62\n"
  "v_exp_f32 v63, v63\n"
  "v_mfma_f32_32x32x16_bf16 v[16:31], v[168:171], v[148:151], v[112:127]\n"
  "ds_read2st64_b64 v[168:171], v201 offset0:16 offset1:17\n"
  "ds_read_b128 v[192:195], v203 offset:35840\n"
  "v_exp_f32 v0, v0\n"
  "v_exp_f32 v1, v1\n"
  "v_exp_f32 v2, v2\n"
  "v_exp_f32 v3, v3\n"
  "v_mfma_f32_32x32x16_bf16 v[16:31], v[196:199], v[160:163], v[16:31]\n"
  "v_exp_f32 v4, v4\n"
  "v_exp_f32 v5, v5\n"
  "v_exp_f32 v6, v6\n"
  "v_exp_f32 v7, v7\n"
  "v_smfmac_f32_32x32x32_bf16 v[80:95], v[188:191], v[56:63], v200\n"
  "s_mov_b64 exec, s[26:27]\n"
  "ds_read_b128 v[184:187], v202 offset:8064\n"
  "ds_read_b128 v[188:191], v202 offset:8128\n"
  "s_mov_b64 exec, -1\n"
  "v_exp_f32 v8, v8\n"
  "v_exp_f32 v9, v9\n"
  "v_exp_f32 v10, v10\n"
  "v_exp_f32 v11, v11\n"
  "s_waitcnt lgkmcnt(5)\n"
  "v_smfmac_f32_32x32x32_bf16 v[64:79], v[176:179], v[0:7], v200\n"
  "v_exp_f32 v12, v12\n"
  "v_exp_f32 v13, v13\n"
  "v_exp_f32 v14, v14\n"
  "v_exp_f32 v15, v15\n"
  "v_mfma_f32_32x32x16_bf16 v[32:47], v[172:175], v[144:147], v[96:111]\n"
  "v_exp_f32 v16, v16\n"
  "v_exp_f32 v17, v17\n"
  "v_exp_f32 v18, v18\n"
  "v_exp_f32 v19, v19\n"
  "v_mfma_f32_32x32x16_bf16 v[32:47], v[196:199], v[140:143], v[32:47]\n"
  "v_exp_f32 v20, v20\n"
  "v_exp_f32 v21, v21\n"
  "v_exp_f32 v22, v22\n"
  "v_exp_f32 v23, v23\n"
  "s_waitcnt lgkmcnt(4)\n"
  "v_smfmac_f32_32x32x32_bf16 v[64:79], v[180:183], v[8:15], v200\n"
  "v_exp_f32 v24, v24\n"
  "v_exp_f32 v25, v25\n"
  "v_exp_f32 v26, v26\n"
  "v_exp_f32 v27, v27\n"
  "v_smfmac_f32_32x32x32_bf16 v[80:95], v[176:179], v[16:23], v200\n"
  "v_exp_f32 v28, v28\n"
  "v_exp_f32 v29, v29\n"
  "v_exp_f32 v30, v30\n"
  "v_exp_f32 v31, v31\n"
  "v_mfma_f32_32x32x16_bf16 v[48:63], v[172:175], v[148:151], v[112:127]\n"
  "ds_read2st64_b64 v[172:175], v201 offset0:18 offset1:19\n"
  "v_exp_f32 v32, v32\n"
  "v_exp_f32 v33, v33\n"
  "v_exp_f32 v34, v34\n"
  "v_exp_f32 v35, v35\n"
  "v_mfma_f32_32x32x16_bf16 v[48:63], v[196:199], v[164:167], v[48:63]\n"
  "v_exp_f32 v36, v36\n"
  "v_exp_f32 v37, v37\n"
  "v_exp_f32 v38, v38\n"
  "v_exp_f32 v39, v39\n"
  "v_smfmac_f32_32x32x32_bf16 v[80:95], v[180:183], v[24:31], v200\n"
  "s_mov_b64 exec, s[26:27]\n"
  "ds_read_b128 v[176:179], v202 offset:9216\n"
  "ds_read_b128 v[180:183], v202 offset:9280\n"
  "s_mov_b64 exec, -1\n"
  "v_exp_f32 v40, v40\n"
  "v_exp_f32 v41, v41\n"
  "v_exp_f32 v42, v42\n"
  "v_exp_f32 v43, v43\n"
  "s_waitcnt lgkmcnt(4)\n"
  "v_smfmac_f32_32x32x32_bf16 v[64:79], v[184:187], v[32:39], v200\n"
  "v_exp_f32 v44, v44\n"
  "v_exp_f32 v45, v45\n"
  "v_exp_f32 v46, v46\n"
  "v_exp_f32 v47, v47\n"
  "v_mfma_f32_32x32x16_bf16 v[0:15], v[168:171], v[144:147], v[96:111]\n"
  "v_exp_f32 v48, v48\n"
  "v_exp_f32 v49, v49\n"
  "v_exp_f32 v50, v50\n"
  "v_exp_f32 v51, v51\n"
  "v_mfma_f32_32x32x16_bf16 v[0:15], v[192:195], v[128:131], v[0:15]\n"
  "v_exp_f32 v52, v52\n"
  "v_exp_f32 v53, v53\n"
  "v_exp_f32 v54, v54\n"
  "v_exp_f32 v55, v55\n"
  "s_waitcnt lgkmcnt(3)\n"
  "v_smfmac_f32_32x32x32_bf16 v[64:79], v[188:191], v[40:47], v200\n"
  "v_exp_f32 v56, v56\n"
  "v_exp_f32 v57, v57\n"
  "v_exp_f32 v58, v58\n"
  "v_exp_f32 v59, v59\n"
  "v_smfmac_f32_32x32x32_bf16 v[80:95], v[184:187], v[48:55], v200\n"
  "v_exp_f32 v60, v60\n"
  "v_exp_f32 v61, v61\n"
  "v_exp_f32 v62, v62\n"
  "v_exp_f32 v63, v63\n"
  "v_mfma_f32_32x32x16_bf16 v[16:31], v[168:171], v[148:151], v[112:127]\n"
  "ds_read2st64_b64 v[168:171], v201 offset0:20 offset1:21\n"
  "v_exp_f32 v0, v0\n"
  "v_exp_f32 v1, v1\n"
  "v_exp_f32 v2, v2\n"
  "v_exp_f32 v3, v3\n"
  "v_mfma_f32_32x32x16_bf16 v[16:31], v[192:195], v[152:155], v[16:31]\n"
  "v_exp_f32 v4, v4\n"
  "v_exp_f32 v5, v5\n"
  "v_exp_f32 v6, v6\n"
  "v_exp_f32 v7, v7\n"
  "v_smfmac_f32_32x32x32_bf16 v[80:95], v[188:191], v[56:63], v200\n"
  "s_mov_b64 exec, s[26:27]\n"
  "ds_read_b128 v[184:187], v202 offset:10368\n"
  "ds_read_b128 v[188:191], v202 offset:10432\n"
  "s_mov_b64 exec, -1\n"
  "v_exp_f32 v8, v8\n"
  "v_exp_f32 v9, v9\n"
  "v_exp_f32 v10, v10\n"
  "v_exp_f32 v11, v11\n"
  "s_waitcnt lgkmcnt(4)\n"
  "v_smfmac_f32_32x32x32_bf16 v[64:79], v[176:179], v[0:7], v200\n"
  "v_exp_f32 v12, v12\n"
  "v_exp_f32 v13, v13\n"
  "v_exp_f32 v14, v14\n"
  "v_exp_f32 v15, v15\n"
  "v_mfma_f32_32x32x16_bf16 v[32:47], v[172:175], v[144:147], v[96:111]\n"
  "v_exp_f32 v16, v16\n"
  "v_exp_f32 v17, v17\n"
  "v_exp_f32 v18, v18\n"
  "v_exp_f32 v19, v19\n"
  "v_mfma_f32_32x32x16_bf16 v[32:47], v[192:195], v[132:135], v[32:47]\n"
  "v_exp_f32 v20, v20\n"
  "v_exp_f32 v21, v21\n"
  "v_exp_f32 v22, v22\n"
  "v_exp_f32 v23, v23\n"
  "s_waitcnt lgkmcnt(3)\n"
  "v_smfmac_f32_32x32x32_bf16 v[64:79], v[180:183], v[8:15], v200\n"
  "v_exp_f32 v24, v24\n"
  "v_exp_f32 v25, v25\n"
  "v_exp_f32 v26, v26\n"
  "v_exp_f32 v27, v27\n"
  "v_smfmac_f32_32x32x32_bf16 v[80:95], v[176:179], v[16:23], v200\n"
  "v_exp_f32 v28, v28\n"
  "v_exp_f32 v29, v29\n"
  "v_exp_f32 v30, v30\n"
  "v_exp_f32 v31, v31\n"
  "v_mfma_f32_32x32x16_bf16 v[48:63], v[172:175], v[148:151], v[112:127]\n"
  "ds_read2st64_b64 v[172:175], v201 offset0:22 offset1:23\n"
  "v_exp_f32 v32, v32\n"
  "v_exp_f32 v33, v33\n"
  "v_exp_f32 v34, v34\n"
  "v_exp_f32 v35, v35\n"
  "v_mfma_f32_32x32x16_bf16 v[48:63], v[192:195], v[156:159], v[48:63]\n"
  "v_exp_f32 v36, v36\n"
  "v_exp_f32 v37, v37\n"
  "v_exp_f32 v38, v38\n"
  "v_exp_f32 v39, v39\n"
  "v_smfmac_f32_32x32x32_bf16 v[80:95], v[180:183], v[24:31], v200\n"
  "s_mov_b64 exec, s[26:27]\n"
  "ds_read_b128 v[176:179], v202 offset:11520\n"
  "ds_read_b128 v[180:183], v202 offset:11584\n"
  "s_mov_b64 exec, -1\n"
  "v_exp_f32 v40, v40\n"
  "v_exp_f32 v41, v41\n"
  "v_exp_f32 v42, v42\n"
  "v_exp_f32 v43, v43\n"
  "s_waitcnt lgkmcnt(4)\n"
  "v_smfmac_f32_32x32x32_bf16 v[64:79], v[184:187], v[32:39], v200\n"
  "v_exp_f32 v44, v44\n"
  "v_exp_f32 v45, v45\n"
  "v_exp_f32 v46, v46\n"
  "v_exp_f32 v47, v47\n"
  "v_mfma_f32_32x32x16_bf16 v[0:15], v[168:171], v[144:147], v[96:111]\n"
  "v_exp_f32 v48, v48\n"
  "v_exp_f32 v49, v49\n"
  "v_exp_f32 v50, v50\n"
  "v_exp_f32 v51, v51\n"
  "v_mfma_f32_32x32x16_bf16 v[0:15], v[192:195], v[136:139], v[0:15]\n"
  "v_exp_f32 v52, v52\n"
  "v_exp_f32 v53, v53\n"
  "v_exp_f32 v54, v54\n"
  "v_exp_f32 v55, v55\n"
  "s_waitcnt lgkmcnt(3)\n"
  "v_smfmac_f32_32x32x32_bf16 v[64:79], v[188:191], v[40:47], v200\n"
  "v_exp_f32 v56, v56\n"
  "v_exp_f32 v57, v57\n"
  "v_exp_f32 v58, v58\n"
  "v_exp_f32 v59, v59\n"
  "v_smfmac_f32_32x32x32_bf16 v[80:95], v[184:187], v[48:55], v200\n"
  "v_exp_f32 v60, v60\n"
  "v_exp_f32 v61, v61\n"
  "v_exp_f32 v62, v62\n"
  "v_exp_f32 v63, v63\n"
  "v_mfma_f32_32x32x16_bf16 v[16:31], v[168:171], v[148:151], v[112:127]\n"
  "ds_read2st64_b64 v[168:171], v201 offset0:24 offset1:25\n"
  "ds_read_b128 v[196:199], v203 offset:36352\n"
  "v_exp_f32 v0, v0\n"
  "v_exp_f32 v1, v1\n"
  "v_exp_f32 v2, v2\n"
  "v_exp_f32 v3, v3\n"
  "v_mfma_f32_32x32x16_bf16 v[16:31], v[192:195], v[160:163], v[16:31]\n"
  "v_exp_f32 v4, v4\n"
  "v_exp_f32 v5, v5\n"
  "v_exp_f32 v6, v6\n"
  "v_exp_f32 v7, v7\n"
  "v_smfmac_f32_32x32x32_bf16 v[80:95], v[188:191], v[56:63], v200\n"
  "s_mov_b64 exec, s[26:27]\n"
  "ds_read_b128 v[184:187], v202 offset:12672\n"
  "ds_read_b128 v[188:191], v202 offset:12736\n"
  "s_mov_b64 exec, -1\n"
  "v_exp_f32 v8, v8\n"
  "v_exp_f32 v9, v9\n"
  "v_exp_f32 v10, v10\n"
  "v_exp_f32 v11, v11\n"
  "s_waitcnt lgkmcnt(5)\n"
  "v_smfmac_f32_32x32x32_bf16 v[64:79], v[176:179], v[0:7], v200\n"
  "v_exp_f32 v12, v12\n"
  "v_exp_f32 v13, v13\n"
  "v_exp_f32 v14, v14\n"
  "v_exp_f32 v15, v15\n"
  "v_mfma_f32_32x32x16_bf16 v[32:47], v[172:175], v[144:147], v[96:111]\n"
  "v_exp_f32 v16, v16\n"
  "v_exp_f32 v17, v17\n"
  "v_exp_f32 v18, v18\n"
  "v_exp_f32 v19, v19\n"
  "v_mfma_f32_32x32x16_bf16 v[32:47], v[192:195], v[140:143], v[32:47]\n"
  "v_exp_f32 v20, v20\n"
  "v_exp_f32 v21, v21\n"
  "v_exp_f32 v22, v22\n"
  "v_exp_f32 v23, v23\n"
  "s_waitcnt lgkmcnt(4)\n"
  "v_smfmac_f32_32x32x32_bf16 v[64:79], v[180:183], v[8:15], v200\n"
  "v_exp_f32 v24, v24\n"
  "v_exp_f32 v25, v25\n"
  "v_exp_f32 v26, v26\n"
  "v_exp_f32 v27, v27\n"
  "v_smfmac_f32_32x32x32_bf16 v[80:95], v[176:179], v[16:23], v200\n"
  "v_exp_f32 v28, v28\n"
  "v_exp_f32 v29, v29\n"
  "v_exp_f32 v30, v30\n"
  "v_exp_f32 v31, v31\n"
  "v_mfma_f32_32x32x16_bf16 v[48:63], v[172:175], v[148:151], v[112:127]\n"
  "ds_read2st64_b64 v[172:175], v201 offset0:26 offset1:27\n"
  "v_exp_f32 v32, v32\n"
  "v_exp_f32 v33, v33\n"
  "v_exp_f32 v34, v34\n"
  "v_exp_f32 v35, v35\n"
  "v_mfma_f32_32x32x16_bf16 v[48:63], v[192:195], v[164:167], v[48:63]\n"
  "v_exp_f32 v36, v36\n"
  "v_exp_f32 v37, v37\n"
  "v_exp_f32 v38, v38\n"
  "v_exp_f32 v39, v39\n"
  "v_smfmac_f32_32x32x32_bf16 v[80:95], v[180:183], v[24:31], v200\n"
  "s_mov_b64 exec, s[26:27]\n"
  "ds_read_b128 v[176:179], v202 offset:13824\n"
  "ds_read_b128 v[180:183], v202 offset:13888\n"
  "s_mov_b64 exec, -1\n"
  "v_exp_f32 v40, v40\n"
  "v_exp_f32 v41, v41\n"
  "v_exp_f32 v42, v42\n"
  "v_exp_f32 v43, v43\n"
  "s_waitcnt lgkmcnt(4)\n"
  "v_smfmac_f32_32x32x32_bf16 v[64:79], v[184:187], v[32:39], v200\n"
  "v_exp_f32 v44, v44\n"
  "v_exp_f32 v45, v45\n"
  "v_exp_f32 v46, v46\n"
  "v_exp_f32 v47, v47\n"
  "v_mfma_f32_32x32x16_bf16 v[0:15], v[168:171], v[144:147], v[96:111]\n"
  "v_exp_f32 v48, v48\n"
  "v_exp_f32 v49, v49\n"
  "v_exp_f32 v50, v50\n"
  "v_exp_f32 v51, v51\n"
  "v_mfma_f32_32x32x16_bf16 v[0:15], v[196:199], v[128:131], v[0:15]\n"
  "v_exp_f32 v52, v52\n"
  "v_exp_f32 v53, v53\n"
  "v_exp_f32 v54, v54\n"
  "v_exp_f32 v55, v55\n"
  "s_waitcnt lgkmcnt(3)\n"
  "v_smfmac_f32_32x32x32_bf16 v[64:79], v[188:191], v[40:47], v200\n"
  "v_exp_f32 v56, v56\n"
  "v_exp_f32 v57, v57\n"
  "v_exp_f32 v58, v58\n"
  "v_exp_f32 v59, v59\n"
  "v_smfmac_f32_32x32x32_bf16 v[80:95], v[184:187], v[48:55], v200\n"
  "v_exp_f32 v60, v60\n"
  "v_exp_f32 v61, v61\n"
  "v_exp_f32 v62, v62\n"
  "v_exp_f32 v63, v63\n"
  "v_mfma_f32_32x32x16_bf16 v[16:31], v[168:171], v[148:151], v[112:127]\n"
  "ds_read2st64_b64 v[168:171], v201 offset0:28 offset1:29\n"
  "v_exp_f32 v0, v0\n"
  "v_exp_f32 v1, v1\n"
  "v_exp_f32 v2, v2\n"
  "v_exp_f32 v3, v3\n"
  "v_mfma_f32_32x32x16_bf16 v[16:31], v[196:199], v[152:155], v[16:31]\n"
  "v_exp_f32 v4, v4\n"
  "v_exp_f32 v5, v5\n"
  "v_exp_f32 v6, v6\n"
  "v_exp_f32 v7, v7\n"
  "v_smfmac_f32_32x32x32_bf16 v[80:95], v[188:191], v[56:63], v200\n"
  "s_mov_b64 exec, s[26:27]\n"
  "ds_read_b128 v[184:187], v202 offset:14976\n"
  "ds_read_b128 v[188:191], v202 offset:15040\n"
  "s_mov_b64 exec, -1\n"
  "v_exp_f32 v8, v8\n"
  "v_exp_f32 v9, v9\n"
  "v_exp_f32 v10, v10\n"
  "v_exp_f32 v11, v11\n"
  "s_waitcnt lgkmcnt(4)\n"
  "v_smfmac_f32_32x32x32_bf16 v[64:79], v[176:179], v[0:7], v200\n"
  "v_exp_f32 v12, v12\n"
  "v_exp_f32 v13, v13\n"
  "v_exp_f32 v14, v14\n"
  "v_exp_f32 v15, v15\n"
  "v_mfma_f32_32x32x16_bf16 v[32:47], v[172:175], v[144:147], v[96:111]\n"
  "v_exp_f32 v16, v16\n"
  "v_exp_f32 v17, v17\n"
  "v_exp_f32 v18, v18\n"
  "v_exp_f32 v19, v19\n"
  "v_mfma_f32_32x32x16_bf16 v[32:47], v[196:199], v[132:135], v[32:47]\n"
  "v_exp_f32 v20, v20\n"
  "v_exp_f32 v21, v21\n"
  "v_exp_f32 v22, v22\n"
  "v_exp_f32 v23, v23\n"
  "s_waitcnt lgkmcnt(3)\n"
  "v_smfmac_f32_32x32x32_bf16 v[64:79], v[180:183], v[8:15], v200\n"
  "v_exp_f32 v24, v24\n"
  "v_exp_f32 v25, v25\n"
  "v_exp_f32 v26, v26\n"
  "v_exp_f32 v27, v27\n"
  "v_smfmac_f32_32x32x32_bf16 v[80:95], v[176:179], v[16:23], v200\n"
  "v_exp_f32 v28, v28\n"
  "v_exp_f32 v29, v29\n"
  "v_exp_f32 v30, v30\n"
  "v_exp_f32 v31, v31\n"
  "ds_read_b32 v208, v205\n"
  "v_mfma_f32_32x32x16_bf16 v[48:63], v[172:175], v[148:151], v[112:127]\n"
  "ds_read2st64_b64 v[172:175], v201 offset0:30 offset1:31\n"
  "v_exp_f32 v32, v32\n"
  "v_exp_f32 v33, v33\n"
  "v_exp_f32 v34, v34\n"
  "v_exp_f32 v35, v35\n"
  "v_mfma_f32_32x32x16_bf16 v[48:63], v[196:199], v[156:159], v[48:63]\n"
  "v_exp_f32 v36, v36\n"
  "v_exp_f32 v37, v37\n"
  "v_exp_f32 v38, v38\n"
  "v_exp_f32 v39, v39\n"
  "v_smfmac_f32_32x32x32_bf16 v[80:95], v[180:183], v[24:31], v200\n"
  "s_mov_b64 exec, s[26:27]\n"
  "ds_read_b128 v[176:179], v202 offset:16128\n"
  "ds_read_b128 v[180:183], v202 offset:16192\n"
  "s_mov_b64 exec, -1\n"
  "v_exp_f32 v40, v40\n"
  "v_exp_f32 v41, v41\n"
  "v_exp_f32 v42, v42\n"
  "v_exp_f32 v43, v43\n"
  "s_waitcnt lgkmcnt(5)\n"
  "v_smfmac_f32_32x32x32_bf16 v[64:79], v[184:187], v[32:39], v200\n"
  "v_exp_f32 v44, v44\n"
  "v_exp_f32 v45, v45\n"
  "v_exp_f32 v46, v46\n"
  "v_exp_f32 v47, v47\n"
  "v_mfma_f32_32x32x16_bf16 v[0:15], v[168:171], v[144:147], v[96:111]\n"
  "v_exp_f32 v48, v48\n"
  "v_exp_f32 v49, v49\n"
  "v_exp_f32 v50, v50\n"
  "v_exp_f32 v51, v51\n"
  "v_mfma_f32_32x32x16_bf16 v[0:15], v[196:199], v[136:139], v[0:15]\n"
  "v_exp_f32 v52, v52\n"
  "v_exp_f32 v53, v53\n"
  "v_exp_f32 v54, v54\n"
  "v_exp_f32 v55, v55\n"
  "s_waitcnt lgkmcnt(4)\n"
  "v_smfmac_f32_32x32x32_bf16 v[64:79], v[188:191], v[40:47], v200\n"
  "v_exp_f32 v56, v56\n"
  "v_exp_f32 v57, v57\n"
  "v_exp_f32 v58, v58\n"
  "v_exp_f32 v59, v59\n"
  "v_smfmac_f32_32x32x32_bf16 v[80:95], v[184:187], v[48:55], v200\n"
  "v_exp_f32 v60, v60\n"
  "v_exp_f32 v61, v61\n"
  "v_exp_f32 v62, v62\n"
  "v_exp_f32 v63, v63\n"
  "s_waitcnt lgkmcnt(3)\n"
  "v_cmp_ge_u32 vcc, s24, v208\n"
  "s_cbranch_vccnz LSD_%=\n"
  "LSDB_%=:\n"
  "s_add_u32 s24, s24, 1\n"
  "s_and_b32 s8, s10, 0xffff\n"
  "s_lshl_b32 s8, s8, 6\n"
  "s_lshr_b64 s[10:11], s[10:11], 16\n"
  "v_add_u32 v201, s8, v249\n"
  "v_and_b32 v203, 0x1f0, v248\n"
  "v_add_u32 v203, s8, v203\n"
  "v_mfma_f32_32x32x16_bf16 v[16:31], v[168:171], v[148:151], v[112:127]\n"
  "ds_read2st64_b64 v[168:171], v201 offset0:0 offset1:1\n"
  "ds_read_b128 v[192:195], v203 offset:34816\n"
  "v_exp_f32 v0, v0\n"
  "v_exp_f32 v1, v1\n"
  "v_exp_f32 v2, v2\n"
  "v_exp_f32 v3, v3\n"
  "v_mfma_f32_32x32x16_bf16 v[16:31], v[196:199], v[160:163], v[16:31]\n"
  "v_exp_f32 v4, v4\n"
  "v_exp_f32 v5, v5\n"
  "v_exp_f32 v6, v6\n"
  "v_exp_f32 v7, v7\n"
  "v_smfmac_f32_32x32x32_bf16 v[80:95], v[188:191], v[56:63], v200\n"
  "s_mov_b64 exec, s[26:27]\n"
  "ds_read_b128 v[184:187], v202 offset:17280\n"
  "ds_read_b128 v[188:191], v202 offset:17344\n"
  "s_mov_b64 exec, -1\n"
  "v_exp_f32 v8, v8\n"
  "v_exp_f32 v9, v9\n"
  "v_exp_f32 v10, v10\n"
  "v_exp_f32 v11, v11\n"
  "s_waitcnt lgkmcnt(5)\n"
  "v_smfmac_f32_32x32x32_bf16 v[64:79], v[176:179], v[0:7], v200\n"
  "v_exp_f32 v12, v12\n"
  "v_exp_f32 v13, v13\n"
  "v_exp_f32 v14, v14\n"
  "v_exp_f32 v15, v15\n"
  "v_mfma_f32_32x32x16_bf16 v[32:47], v[172:175], v[144:147], v[96:111]\n"
  "v_exp_f32 v16, v16\n"
  "v_exp_f32 v17, v17\n"
  "v_exp_f32 v18, v18\n"
  "v_exp_f32 v19, v19\n"
  "v_mfma_f32_32x32x16_bf16 v[32:47], v[196:199], v[140:143], v[32:47]\n"
  "v_exp_f32 v20, v20\n"
  "v_exp_f32 v21, v21\n"
  "v_exp_f32 v22, v22\n"
  "v_exp_f32 v23, v23\n"
  "s_waitcnt lgkmcnt(4)\n"
  "v_smfmac_f32_32x32x32_bf16 v[64:79], v[180:183], v[8:15], v200\n"
  "v_exp_f32 v24, v24\n"
  "v_exp_f32 v25, v25\n"
  "v_exp_f32 v26, v26\n"
  "v_exp_f32 v27, v27\n"
  "v_smfmac_f32_32x32x32_bf16 v[80:95], v[176:179], v[16:23], v200\n"
  "v_exp_f32 v28, v28\n"
  "v_exp_f32 v29, v29\n"
  "v_exp_f32 v30, v30\n"
  "v_exp_f32 v31, v31\n"
  "v_add_u32 v202, s8, v253\n"
  "v_mfma_f32_32x32x16_bf16 v[48:63], v[172:175], v[148:151], v[112:127]\n"
  "ds_read2st64_b64 v[172:175], v201 offset0:2 offset1:3\n"
  "v_exp_f32 v32, v32\n"
  "v_exp_f32 v33, v33\n"
  "v_exp_f32 v34, v34\n"
  "v_exp_f32 v35, v35\n"
  "v_mfma_f32_32x32x16_bf16 v[48:63], v[196:199], v[164:167], v[48:63]\n"
  "v_exp_f32 v36, v36\n"
  "v_exp_f32 v37, v37\n"
  "v_exp_f32 v38, v38\n"
  "v_exp_f32 v39, v39\n"
  "v_smfmac_f32_32x32x32_bf16 v[80:95], v[180:183], v[24:31], v200\n"
  "s_mov_b64 exec, s[26:27]\n"
  "ds_read_b128 v[176:179], v202 offset:0\n"
  "ds_read_b128 v[180:183], v202 offset:64\n"
  "s_mov_b64 exec, -1\n"
  "v_exp_f32 v40, v40\n"
  "v_exp_f32 v41, v41\n"
  "v_exp_f32 v42, v42\n"
  "v_exp_f32 v43, v43\n"
  "s_waitcnt lgkmcnt(4)\n"
  "v_smfmac_f32_32x32x32_bf16 v[64:79], v[184:187], v[32:39], v200\n"
  "v_exp_f32 v44, v44\n"
  "v_exp_f32 v45, v45\n"
  "v_exp_f32 v46, v46\n"
  "v_exp_f32 v47, v47\n"
  "v_mfma_f32_32x32x16_bf16 v[0:15], v[168:171], v[144:147], v[96:111]\n"
  "v_exp_f32 v48, v48\n"
  "v_exp_f32 v49, v49\n"
  "v_exp_f32 v50, v50\n"
  "v_exp_f32 v51, v51\n"
  "v_mfma_f32_32x32x16_bf16 v[0:15], v[192:195], v[128:131], v[0:15]\n"
  "v_exp_f32 v52, v52\n"
  "v_exp_f32 v53, v53\n"
  "v_exp_f32 v54, v54\n"
  "v_exp_f32 v55, v55\n"
  "s_waitcnt lgkmcnt(3)\n"
  "v_smfmac_f32_32x32x32_bf16 v[64:79], v[188:191], v[40:47], v200\n"
  "v_mov_b32 v206, s24\n"
  "ds_write_b32 v204, v206 offset:32\n"
  "v_exp_f32 v56, v56\n"
  "v_exp_f32 v57, v57\n"
  "v_exp_f32 v58, v58\n"
  "v_exp_f32 v59, v59\n"
  "v_smfmac_f32_32x32x32_bf16 v[80:95], v[184:187], v[48:55], v200\n"
  "v_exp_f32 v60, v60\n"
  "v_exp_f32 v61, v61\n"
  "v_exp_f32 v62, v62\n"
  "v_exp_f32 v63, v63\n"
  "s_sub_u32 s25, s25, 1\n"
  "s_cmp_lg_u32 s25, 0\n"
  "s_cbranch_scc1 LLOOP_%=\n"
  "v_smfmac_f32_32x32x32_bf16 v[80:95], v[188:191], v[56:63], v200\n"
  "v_add_f32 v206, |v64|, |v65|\n"
  "v_add_f32 v206, v206, |v66|\n"
  "v_add_f32 v206, v206, |v67|\n"
  "v_add_f32 v206, v206, |v68|\n"
  "v_add_f32 v206, v206, |v69|\n"
  "v_add_f32 v206, v206, |v70|\n"
  "v_add_f32 v206, v206, |v71|\n"
  "v_add_f32 v206, v206, |v72|\n"
  "v_add_f32 v206, v206, |v73|\n"
  "v_add_f32 v206, v206, |v74|\n"
  "s_nop 1\n"
  "v_add_f32 v207, |v80|, |v81|\n"
  "v_add_f32 v207, v207, |v82|\n"
  "v_add_f32 v207, v207, |v83|\n"
  "v_add_f32 v207, v207, |v84|\n"
  "v_add_f32 v207, v207, |v85|\n"
  "v_add_f32 v207, v207, |v86|\n"
  "v_add_f32 v207, v207, |v87|\n"
  "v_add_f32 v207, v207, |v88|\n"
  "v_add_f32 v207, v207, |v89|\n"
  "v_add_f32 v207, v207, |v90|\n"
  "v_add_f32 v206, v206, v207\n"
  "v_cmp_ngt_f32 vcc, 0x71800000, v206\n"
  "s_cbranch_vccnz LFB_%=\n"
  "LFIN_%=:\n"
  "v_rcp_f32 v206, v74\n"
  "s_nop 0\n"
  "v_mul_f32 v64, v64, v206\n"
  "v_mul_f32 v65, v65, v206\n"
  "v_mul_f32 v66, v66, v206\n"
  "v_mul_f32 v67, v67, v206\n"
  "v_mul_f32 v68, v68, v206\n"
  "v_mul_f32 v69, v69, v206\n"
  "v_mul_f32 v70, v70, v206\n"
  "v_mul_f32 v71, v71, v206\n"
  "v_mul_f32 v72, v72, v206\n"
  "v_mul_f32 v73, v73, v206\n"
  "v_cvt_pk_bf16_f32 v64, v64, v65\n"
  "v_cvt_pk_bf16_f32 v65, v66, v67\n"
  "v_cvt_pk_bf16_f32 v66, v68, v69\n"
  "v_cvt_pk_bf16_f32 v67, v70, v71\n"
  "v_cvt_pk_bf16_f32 v68, v72, v73\n"
  "v_min_u32 v64, -2, v64\n"
  "v_min_u32 v65, -2, v65\n"
  "v_min_u32 v66, -2, v66\n"
  "v_min_u32 v67, -2, v67\n"
  "v_min_u32 v68, -2, v68\n"
  "buffer_store_dwordx4 v[64:67], v251, s[20:23], s77 offen sc1\n"
  "buffer_store_dword v68, v252, s[20:23], s78 offen sc1\n"
  "v_rcp_f32 v206, v90\n"
  "s_nop 0\n"
  "v_mul_f32 v80, v80, v206\n"
  "v_mul_f32 v81, v81, v206\n"
  "v_mul_f32 v82, v82, v206\n"
  "v_mul_f32 v83, v83, v206\n"
  "v_mul_f32 v84, v84, v206\n"
  "v_mul_f32 v85, v85, v206\n"
  "v_mul_f32 v86, v86, v206\n"
  "v_mul_f32 v87, v87, v206\n"
  "v_mul_f32 v88, v88, v206\n"
  "v_mul_f32 v89, v89, v206\n"
  "v_cvt_pk_bf16_f32 v80, v80, v81\n"
  "v_cvt_pk_bf16_f32 v81, v82, v83\n"
  "v_cvt_pk_bf16_f32 v82, v84, v85\n"
  "v_cvt_pk_bf16_f32 v83, v86, v87\n"
  "v_cvt_pk_bf16_f32 v84, v88, v89\n"
  "v_min_u32 v80, -2, v80\n"
  "v_min_u32 v81, -2, v81\n"
  "v_min_u32 v82, -2, v82\n"
  "v_min_u32 v83, -2, v83\n"
  "v_min_u32 v84, -2, v84\n"
  "s_add_u32 s77, s77, 36864\n"
  "s_add_u32 s78, s78, 36864\n"
  "buffer_store_dwordx4 v[80:83], v251, s[20:23], s77 offen sc1\n"
  "buffer_store_dword v84, v252, s[20:23], s78 offen sc1\n"
  "v_mov_b32 v206, 5\n"
  "ds_write_b32 v204, v206 offset:32\n"
  "s_cmp_lg_u32 s90, 0\n"
  "s_cbranch_scc1 LEP_%=\n"
  "s_cmp_ge_u32 s68, 2\n"
  "s_cbranch_scc1 LEND_%=\n"
  "s_waitcnt lgkmcnt(4)\n"
  "v_bfe_u32 v174, v248, 4, 4\n"
  "v_mul_u32_u24 v174, 1152, v174\n"
  "v_lshrrev_b32 v206, 8, v248\n"
  "v_lshl_add_u32 v174, v206, 4, v174\n"
  "buffer_load_dwordx2 v[168:169], v174, s[92:95], s87 offen\n"
  "s_add_u32 s8, s86, 552960\n"
  "buffer_load_dwordx4 v[156:159], v248, s[20:23], s8 offen sc1\n"
  "s_add_u32 s8, s86, 589824\n"
  "buffer_load_dwordx4 v[160:163], v248, s[20:23], s8 offen sc1\n"
  "s_add_u32 s8, s86, 626688\n"
  "buffer_load_dwordx4 v[164:167], v248, s[20:23], s8 offen sc1\n"
  "s_lshl_b32 s8, s68, 14\n"
  "s_add_u32 s8, s8, s89\n"
  "s_waitcnt lgkmcnt(3)\n"
  "v_add_u32 v176, s8, v248\n"
  "s_lshl_b32 s8, s68, 11\n"
  "s_add_u32 s8, s8, s89\n"
  "v_add_u32 v177, s8, v248\n"
  "v_add_u32 v175, s88, v248\n"
  "s_waitcnt vmcnt(3)\n"
  "v_lshlrev_b32 v170, 16, v168\n"
  "v_and_b32 v171, 0xffff0000, v168\n"
  "v_lshlrev_b32 v172, 16, v169\n"
  "v_and_b32 v173, 0xffff0000, v169\n"
  "s_cmp_eq_u32 s89, 36864\n"
  "s_cselect_b32 s8, 9216, 0\n"
  "s_add_u32 s9, s83, 4096\n"
  "s_add_u32 s9, s9, s8\n"
  "v_lshrrev_b32 v178, 1, v248\n"
  "v_add_u32 v179, 147456, v248\n"
  "s_waitcnt lgkmcnt(0)\n"
  "s_barrier\n"
  "buffer_load_dwordx2 v[168:169], v178, s[20:23], s9 offen sc1\n"
  "ds_read_b128 v[96:99], v179 offset:0\n"
  "ds_read_b128 v[24:27], v176 offset:0\n"
  "ds_read_b128 v[100:103], v179 offset:1024\n"
  "ds_read_b128 v[28:31], v176 offset:1024\n"
  "ds_read_b128 v[104:107], v179 offset:2048\n"
  "ds_read_b128 v[32:35], v176 offset:2048\n"
  "ds_read_b128 v[108:111], v179 offset:3072\n"
  "ds_read_b128 v[36:39], v176 offset:3072\n"
  "ds_read_b128 v[112:115], v179 offset:4096\n"
  "ds_read_b128 v[40:43], v176 offset:4096\n"
  "ds_read_b128 v[56:59], v175\n"
  "s_waitcnt lgkmcnt(9)\n"
  "v_mfma_f32_16x16x32_bf16 v[48:51], v[96:99], v[24:27], 0\n"
  "ds_read_b128 v[116:119], v179 offset:5120\n"
  "ds_read_b128 v[44:47], v176 offset:5120\n"
  "s_waitcnt lgkmcnt(9)\n"
  "v_mfma_f32_16x16x32_bf16 v[52:55], v[100:103], v[28:31], 0\n"
  "ds_read_b128 v[120:123], v179 offset:6144\n"
  "ds_read_b128 v[24:27], v176 offset:6144\n"
  "s_waitcnt lgkmcnt(9)\n"
  "v_mfma_f32_16x16x32_bf16 v[48:51], v[104:107], v[32:35], v[48:51]\n"
  "ds_read_b128 v[124:127], v179 offset:7168\n"
  "ds_read_b128 v[28:31], v176 offset:7168\n"
  "s_waitcnt lgkmcnt(9)\n"
  "v_mfma_f32_16x16x32_bf16 v[52:55], v[108:111], v[36:39], v[52:55]\n"
  "ds_read_b128 v[128:131], v179 offset:8192\n"
  "ds_read_b128 v[32:35], v176 offset:8192\n"
  "s_waitcnt lgkmcnt(9)\n"
  "v_mfma_f32_16x16x32_bf16 v[48:51], v[112:115], v[40:43], v[48:51]\n"
  "ds_read_b128 v[132:135], v179 offset:9216\n"
  "ds_read_b128 v[36:39], v176 offset:9216\n"
  "s_waitcnt lgkmcnt(8)\n"
  "v_mfma_f32_16x16x32_bf16 v[52:55], v[116:119], v[44:47], v[52:55]\n"
  "ds_read_b128 v[136:139], v179 offset:10240\n"
  "ds_read_b128 v[40:43], v176 offset:10240\n"
  "s_waitcnt lgkmcnt(8)\n"
  "v_mfma_f32_16x16x32_bf16 v[48:51], v[120:123], v[24:27], v[48:51]\n"
  "ds_read_b128 v[140:143], v179 offset:11264\n"
  "ds_read_b128 v[44:47], v176 offset:11264\n"
  "s_waitcnt lgkmcnt(8)\n"
  "v_mfma_f32_16x16x32_bf16 v[52:55], v[124:127], v[28:31], v[52:55]\n"
  "ds_read_b128 v[144:147], v179 offset:12288\n"
  "ds_read_b128 v[24:27], v176 offset:12288\n"
  "s_waitcnt lgkmcnt(8)\n"
  "v_mfma_f32_16x16x32_bf16 v[48:51], v[128:131], v[32:35], v[48:51]\n"
  "ds_read_b128 v[148:151], v179 offset:13312\n"
  "ds_read_b128 v[28:31], v176 offset:13312\n"
  "s_waitcnt lgkmcnt(8)\n"
  "v_mfma_f32_16x16x32_bf16 v[52:55], v[132:135], v[36:39], v[52:55]\n"
  "ds_read_b128 v[152:155], v179 offset:14336\n"
  "ds_read_b128 v[32:35], v176 offset:14336\n"
  "s_waitcnt lgkmcnt(8)\n"
  "v_mfma_f32_16x16x32_bf16 v[48:51], v[136:139], v[40:43], v[48:51]\n"
  "ds_read_b128 v[36:39], v176 offset:15360\n"
  "s_waitcnt lgkmcnt(7)\n"
  "v_mfma_f32_16x16x32_bf16 v[52:55], v[140:143], v[44:47], v[52:55]\n"
  "ds_read_b128 v[40:43], v177 offset:32768\n"
  "s_waitcnt lgkmcnt(6)\n"
  "v_mfma_f32_16x16x32_bf16 v[48:51], v[144:147], v[24:27], v[48:51]\n"
  "ds_read_b128 v[44:47], v177 offset:33792\n"
  "s_waitcnt lgkmcnt(5)\n"
  "v_mfma_f32_16x16x32_bf16 v[52:55], v[148:151], v[28:31], v[52:55]\n"
  "s_waitcnt lgkmcnt(3)\n"
  "v_mfma_f32_16x16x32_bf16 v[48:51], v[152:155], v[32:35], v[48:51]\n"
  "s_waitcnt vmcnt(3) lgkmcnt(2)\n"
  "v_mfma_f32_16x16x32_bf16 v[52:55], v[156:159], v[36:39], v[52:55]\n"
  "s_waitcnt vmcnt(2) lgkmcnt(1)\n"
  "v_mfma_f32_16x16x32_bf16 v[48:51], v[160:163], v[40:43], v[48:51]\n"
  "s_waitcnt vmcnt(1) lgkmcnt(0)\n"
  "v_mfma_f32_16x16x32_bf16 v[52:55], v[164:167], v[44:47], v[52:55]\n"
  "s_nop 7\n"
  "v_add_f32 v48, v48, v52\n"
  "v_add_f32 v49, v49, v53\n"
  "v_add_f32 v50, v50, v54\n"
  "v_add_f32 v51, v51, v55\n"
  "s_waitcnt vmcnt(0)\n"
  "v_max_u32 v60, v168, v169\n"
  "v_cmp_eq_u32 vcc, -1, v60\n"
  "s_cbranch_vccnz LSW_%=\n"
  "LSWB_%=:\n"
  "v_lshlrev_b32 v60, 16, v168\n"
  "v_and_b32 v61, 0xffff0000, v168\n"
  "v_lshlrev_b32 v62, 16, v169\n"
  "v_and_b32 v63, 0xffff0000, v169\n"
  "v_add_f32 v56, v56, v60\n"
  "v_add_f32 v57, v57, v61\n"
  "v_add_f32 v58, v58, v62\n"
  "v_add_f32 v59, v59, v63\n"
  "v_mul_f32 v60, 0xbfb8aa3b, v56\n"
  "v_mul_f32 v61, 0xbfb8aa3b, v57\n"
  "v_mul_f32 v62, 0xbfb8aa3b, v58\n"
  "v_mul_f32 v63, 0xbfb8aa3b, v59\n"
  "v_exp_f32 v60, v60\n"
  "v_exp_f32 v61, v61\n"
  "v_exp_f32 v62, v62\n"
  "v_exp_f32 v63, v63\n"
  "v_mul_f32 v48, v48, v56\n"
  "v_mul_f32 v49, v49, v57\n"
  "v_mul_f32 v50, v50, v58\n"
  "v_mul_f32 v51, v51, v59\n"
  "v_add_f32 v60, 1.0, v60\n"
  "v_add_f32 v61, 1.0, v61\n"
  "v_add_f32 v62, 1.0, v62\n"
  "v_add_f32 v63, 1.0, v63\n"
  "v_rcp_f32 v60, v60\n"
  "v_rcp_f32 v61, v61\n"
  "v_rcp_f32 v62, v62\n"
  "v_rcp_f32 v63, v63\n"
  "v_fmac_f32 v170, v48, v60\n"
  "v_fmac_f32 v171, v49, v61\n"
  "v_fmac_f32 v172, v50, v62\n"
  "v_fmac_f32 v173, v51, v63\n"
  "v_cvt_pk_bf16_f32 v168, v170, v171\n"
  "v_cvt_pk_bf16_f32 v169, v172, v173\n"
  "buffer_store_dwordx2 v[168:169], v174, s[96:99], s87 offen\n"
  "s_branch LEND_%=\n"
  "LSD_%=:\n"
  "s_cmp_eq_u32 s46, 0\n"
  "s_cbranch_scc1 LSDB_%=\n"
  "s_sub_u32 s46, s46, 1\n"
  "s_sleep 1\n"
  "ds_read_b32 v208, v205\n"
  "s_waitcnt lgkmcnt(0)\n"
  "v_cmp_ge_u32 vcc, s24, v208\n"
  "s_cbranch_vccnz LSD_%=\n"
  "s_branch LSDB_%=\n"
  "LSW_%=:\n"
  "s_cmp_eq_u32 s46, 0\n"
  "s_cbranch_scc1 LSWB_%=\n"
  "s_sub_u32 s46, s46, 1\n"
  "s_sleep 1\n"
  "buffer_load_dwordx2 v[168:169], v178, s[20:23], s9 offen sc1\n"
  "s_waitcnt vmcnt(0) lgkmcnt(0)\n"
  "v_max_u32 v60, v168, v169\n"
  "v_cmp_eq_u32 vcc, -1, v60\n"
  "s_cbranch_vccnz LSW_%=\n"
  "s_branch LSWB_%=\n"
  "LEP_%=:\n"
  "s_waitcnt vmcnt(0)\n"
  "v_lshlrev_b32 v174, 7, v248\n"
  "LEQ_%=:\n"
  "s_cmp_eq_u32 s46, 0\n"
  "s_cbranch_scc1 LEQB_%=\n"
  "s_sub_u32 s46, s46, 1\n"
  "s_sleep 2\n"
  "buffer_load_dword v48, v174, s[20:23], s80 offen offset:128 sc1\n"
  "buffer_load_dword v49, v174, s[20:23], s80 offen offset:640 sc1\n"
  "buffer_load_dword v50, v174, s[20:23], s80 offen offset:1152 sc1\n"
  "buffer_load_dword v51, v174, s[20:23], s80 offen offset:1664 sc1\n"
  "s_waitcnt vmcnt(0) lgkmcnt(0)\n"
  "v_xor_b32 v48, s47, v48\n"
  "v_xor_b32 v49, s47, v49\n"
  "v_xor_b32 v50, s47, v50\n"
  "v_xor_b32 v51, s47, v51\n"
  "v_or3_b32 v48, v48, v49, v50\n"
  "v_or_b32 v48, v48, v51\n"
  "v_cmp_ne_u32 vcc, 0, v48\n"
  "s_cbranch_vccnz LEQ_%=\n"
  "s_branch LEQB_%=\n"
  "LEQB_%=:\n"
  "v_mov_b32 v48, s47\n"
  "s_mov_b32 s8, 39133184\n"
  "s_mov_b64 exec, 1\n"
  "buffer_store_dword v48, off, s[20:23], s8 sc1\n"
  "s_mov_b64 exec, -1\n"
  "s_branch LEND_%=\n"
  "LFB_%=:\n"
  "s_waitcnt vmcnt(0) lgkmcnt(0)\n"
  "s_nop 15\n"
  "v_mov_b32 v214, 0xff800000\n"
  "v_mov_b32 v215, 0xff800000\n"
  "v_mov_b32 v193, 0\n"
  "v_mov_b32 v194, 0\n"
  "v_mov_b32 v195, 0\n"
  "v_and_b32 v203, 0xff, v249\n"
  "s_lshl_b32 s8, s68, 10\n"
  "s_sub_u32 s12, s72, s8\n"
  "s_mov_b32 s14, s12\n"
  "s_lshr_b32 s8, s68, 1\n"
  "s_lshl_b32 s8, s8, 10\n"
  "s_sub_u32 s13, s73, s8\n"
  "s_and_b32 s8, s68, 1\n"
  "s_lshl_b32 s8, s8, 7\n"
  "s_sub_u32 s13, s13, s8\n"
  "s_mov_b32 s25, 64\n"
  "LFA_%=:\n"
  "buffer_load_dwordx2 v[168:169], v249, s[20:23], s12 offen offset:0 sc1\n"
  "buffer_load_dwordx2 v[170:171], v249, s[20:23], s12 offen offset:512 sc1\n"
  "buffer_load_dword v192, v203, s[20:23], s13 offen sc1\n"
  "s_waitcnt vmcnt(1)\n"
  "v_mfma_f32_32x32x16_bf16 v[0:15], v[168:171], v[144:147], 0\n"
  "s_waitcnt vmcnt(0)\n"
  "v_mfma_f32_32x32x16_bf16 v[0:15], v[192:195], v[128:131], v[0:15]\n"
  "v_mfma_f32_32x32x16_bf16 v[16:31], v[168:171], v[148:151], 0\n"
  "v_mfma_f32_32x32x16_bf16 v[16:31], v[192:195], v[152:155], v[16:31]\n"
  "s_nop 9\n"
  "v_max3_f32 v214, v214, v0, v1\n"
  "v_max3_f32 v214, v214, v2, v3\n"
  "v_max3_f32 v214, v214, v4, v5\n"
  "v_max3_f32 v214, v214, v6, v7\n"
  "v_max3_f32 v214, v214, v8, v9\n"
  "v_max3_f32 v214, v214, v10, v11\n"
  "v_max3_f32 v214, v214, v12, v13\n"
  "v_max3_f32 v214, v214, v14, v15\n"
  "v_max3_f32 v215, v215, v16, v17\n"
  "v_max3_f32 v215, v215, v18, v19\n"
  "v_max3_f32 v215, v215, v20, v21\n"
  "v_max3_f32 v215, v215, v22, v23\n"
  "v_max3_f32 v215, v215, v24, v25\n"
  "v_max3_f32 v215, v215, v26, v27\n"
  "v_max3_f32 v215, v215, v28, v29\n"
  "v_max3_f32 v215, v215, v30, v31\n"
  "s_sub_u32 s25, s25, 1\n"
  "s_and_b32 s8, s25, 15\n"
  "s_cselect_b32 s8, 0, 18432\n"
  "s_cselect_b32 s9, 0, 16384\n"
  "s_add_u32 s12, s12, s8\n"
  "s_add_u32 s14, s14, s9\n"
  "s_add_u32 s12, s12, 1024\n"
  "s_add_u32 s14, s14, 1152\n"
  "s_add_u32 s13, s13, 256\n"
  "s_nop 15\n"
  "s_cmp_lg_u32 s25, 0\n"
  "s_cbranch_scc1 LFA_%=\n"
  "v_mov_b32 v207, v214\n"
  "s_nop 1\n"
  "v_permlane32_swap_b32 v214, v207\n"
  "v_max_f32 v214, v214, v207\n"
  "v_sub_f32 v96, 0, v214\n"
  "v_sub_f32 v97, 0, v214\n"
  "v_sub_f32 v98, 0, v214\n"
  "v_sub_f32 v99, 0, v214\n"
  "v_sub_f32 v100, 0, v214\n"
  "v_sub_f32 v101, 0, v214\n"
  "v_sub_f32 v102, 0, v214\n"
  "v_sub_f32 v103, 0, v214\n"
  "v_sub_f32 v104, 0, v214\n"
  "v_sub_f32 v105, 0, v214\n"
  "v_sub_f32 v106, 0, v214\n"
  "v_sub_f32 v107, 0, v214\n"
  "v_sub_f32 v108, 0, v214\n"
  "v_sub_f32 v109, 0, v214\n"
  "v_sub_f32 v110, 0, v214\n"
  "v_sub_f32 v111, 0, v214\n"
  "v_mov_b32 v207, v215\n"
  "s_nop 1\n"
  "v_permlane32_swap_b32 v215, v207\n"
  "v_max_f32 v215, v215, v207\n"
  "v_sub_f32 v112, 0, v215\n"
  "v_sub_f32 v113, 0, v215\n"
  "v_sub_f32 v114, 0, v215\n"
  "v_sub_f32 v115, 0, v215\n"
  "v_sub_f32 v116, 0, v215\n"
  "v_sub_f32 v117, 0, v215\n"
  "v_sub_f32 v118, 0, v215\n"
  "v_sub_f32 v119, 0, v215\n"
  "v_sub_f32 v120, 0, v215\n"
  "v_sub_f32 v121, 0, v215\n"
  "v_sub_f32 v122, 0, v215\n"
  "v_sub_f32 v123, 0, v215\n"
  "v_sub_f32 v124, 0, v215\n"
  "v_sub_f32 v125, 0, v215\n"
  "v_sub_f32 v126, 0, v215\n"
  "v_sub_f32 v127, 0, v215\n"
  "v_mov_b32 v64, 0\n"
  "v_mov_b32 v65, 0\n"
  "v_mov_b32 v66, 0\n"
  "v_mov_b32 v67, 0\n"
  "v_mov_b32 v68, 0\n"
  "v_mov_b32 v69, 0\n"
  "v_mov_b32 v70, 0\n"
  "v_mov_b32 v71, 0\n"
  "v_mov_b32 v72, 0\n"
  "v_mov_b32 v73, 0\n"
  "v_mov_b32 v74, 0\n"
  "v_mov_b32 v75, 0\n"
  "v_mov_b32 v76, 0\n"
  "v_mov_b32 v77, 0\n"
  "v_mov_b32 v78, 0\n"
  "v_mov_b32 v79, 0\n"
  "v_mov_b32 v80, 0\n"
  "v_mov_b32 v81, 0\n"
  "v_mov_b32 v82, 0\n"
  "v_mov_b32 v83, 0\n"
  "v_mov_b32 v84, 0\n"
  "v_mov_b32 v85, 0\n"
  "v_mov_b32 v86, 0\n"
  "v_mov_b32 v87, 0\n"
  "v_mov_b32 v88, 0\n"
  "v_mov_b32 v89, 0\n"
  "v_mov_b32 v90, 0\n"
  "v_mov_b32 v91, 0\n"
  "v_mov_b32 v92, 0\n"
  "v_mov_b32 v93, 0\n"
  "v_mov_b32 v94, 0\n"
  "v_mov_b32 v95, 0\n"
  "s_lshl_b32 s8, s68, 10\n"
  "s_sub_u32 s12, s72, s8\n"
  "s_mov_b32 s14, s12\n"
  "s_lshr_b32 s8, s68, 1\n"
  "s_lshl_b32 s8, s8, 10\n"
  "s_sub_u32 s13, s73, s8\n"
  "s_and_b32 s8, s68, 1\n"
  "s_lshl_b32 s8, s8, 7\n"
  "s_sub_u32 s13, s13, s8\n"
  "s_mov_b32 s25, 64\n"
  "LFP_%=:\n"
  "buffer_load_dwordx2 v[168:169], v249, s[20:23], s12 offen offset:0 sc1\n"
  "buffer_load_dwordx2 v[170:171], v249, s[20:23], s12 offen offset:512 sc1\n"
  "buffer_load_dword v192, v203, s[20:23], s13 offen sc1\n"
  "s_mov_b64 exec, s[26:27]\n"
  "buffer_load_dwordx4 v[176:179], v253, s[20:23], s14 offen offset:0 sc1\n"
  "buffer_load_dwordx4 v[180:183], v253, s[20:23], s14 offen offset:64 sc1\n"
  "s_mov_b64 exec, -1\n"
  "s_waitcnt vmcnt(3)\n"
  "v_mfma_f32_32x32x16_bf16 v[0:15], v[168:171], v[144:147], v[96:111]\n"
  "s_waitcnt vmcnt(2)\n"
  "v_mfma_f32_32x32x16_bf16 v[0:15], v[192:195], v[128:131], v[0:15]\n"
  "v_mfma_f32_32x32x16_bf16 v[16:31], v[168:171], v[148:151], v[112:127]\n"
  "v_mfma_f32_32x32x16_bf16 v[16:31], v[192:195], v[152:155], v[16:31]\n"
  "s_nop 9\n"
  "v_exp_f32 v0, v0\n"
  "v_exp_f32 v1, v1\n"
  "v_exp_f32 v2, v2\n"
  "v_exp_f32 v3, v3\n"
  "v_exp_f32 v4, v4\n"
  "v_exp_f32 v5, v5\n"
  "v_exp_f32 v6, v6\n"
  "v_exp_f32 v7, v7\n"
  "v_exp_f32 v8, v8\n"
  "v_exp_f32 v9, v9\n"
  "v_exp_f32 v10, v10\n"
  "v_exp_f32 v11, v11\n"
  "v_exp_f32 v12, v12\n"
  "v_exp_f32 v13, v13\n"
  "v_exp_f32 v14, v14\n"
  "v_exp_f32 v15, v15\n"
  "v_exp_f32 v16, v16\n"
  "v_exp_f32 v17, v17\n"
  "v_exp_f32 v18, v18\n"
  "v_exp_f32 v19, v19\n"
  "v_exp_f32 v20, v20\n"
  "v_exp_f32 v21, v21\n"
  "v_exp_f32 v22, v22\n"
  "v_exp_f32 v23, v23\n"
  "v_exp_f32 v24, v24\n"
  "v_exp_f32 v25, v25\n"
  "v_exp_f32 v26, v26\n"
  "v_exp_f32 v27, v27\n"
  "v_exp_f32 v28, v28\n"
  "v_exp_f32 v29, v29\n"
  "v_exp_f32 v30, v30\n"
  "v_exp_f32 v31, v31\n"
  "s_waitcnt vmcnt(1)\n"
  "v_smfmac_f32_32x32x32_bf16 v[64:79], v[176:179], v[0:7], v200\n"
  "s_waitcnt vmcnt(0)\n"
  "v_smfmac_f32_32x32x32_bf16 v[64:79], v[180:183], v[8:15], v200\n"
  "v_smfmac_f32_32x32x32_bf16 v[80:95], v[176:179], v[16:23], v200\n"
  "v_smfmac_f32_32x32x32_bf16 v[80:95], v[180:183], v[24:31], v200\n"
  "s_sub_u32 s25, s25, 1\n"
  "s_and_b32 s8, s25, 15\n"
  "s_cselect_b32 s8, 0, 18432\n"
  "s_cselect_b32 s9, 0, 16384\n"
  "s_add_u32 s12, s12, s8\n"
  "s_add_u32 s14, s14, s9\n"
  "s_add_u32 s12, s12, 1024\n"
  "s_add_u32 s14, s14, 1152\n"
  "s_add_u32 s13, s13, 256\n"
  "s_nop 15\n"
  "s_cmp_lg_u32 s25, 0\n"
  "s_cbranch_scc1 LFP_%=\n"
  "s_nop 15\n"
  "s_branch LFIN_%=\n"
  "LSQ_%=:\n"
  "s_cmp_eq_u32 s46, 0\n"
  "s_cbranch_scc1 LSQB_%=\n"
  "s_sub_u32 s46, s46, 1\n"
  "s_sleep 2\n"
  "buffer_load_dword v97, v96, s[20:23], s80 offen sc1\n"
  "s_waitcnt vmcnt(0) lgkmcnt(0)\n"
  "v_cmp_ne_u32 vcc, s47, v97\n"
  "s_cbranch_vccnz LSQ_%=\n"
  "s_branch LSQB_%=\n"
  "LSAatt_%=:\n"
  "s_cmp_eq_u32 s46, 0\n"
  "s_cbranch_scc1 LSAattB_%=\n"
  "s_sub_u32 s46, s46, 1\n"
  "s_sleep 2\n"
  "buffer_load_dword v99, v98, s[20:23], s80 offen sc1\n"
  "s_waitcnt vmcnt(0) lgkmcnt(0)\n"
  "v_cmp_ne_u32 vcc, s47, v99\n"
  "s_cbranch_vccnz LSAatt_%=\n"
  "s_branch LSAattB_%=\n"
  "LGEMM_%=:\n"
  "v_mfma_f32_16x16x32_bf16 v[24:27], v[64:67], v[148:151], 0\n"
  "s_and_b32 s81, s68, 3\n"
  "s_xor_b32 s8, s81, 0\n"
  "s_lshl_b32 s8, s8, 10\n"
  "s_add_u32 s8, s8, s77\n"
  "s_xor_b32 s9, s81, 1\n"
  "s_lshl_b32 s9, s9, 10\n"
  "s_add_u32 s9, s9, s77\n"
  "s_xor_b32 s10, s81, 2\n"
  "s_lshl_b32 s10, s10, 10\n"
  "s_add_u32 s10, s10, s77\n"
  "s_xor_b32 s11, s81, 3\n"
  "s_lshl_b32 s11, s11, 10\n"
  "s_add_u32 s11, s11, s77\n"
  "s_waitcnt lgkmcnt(6)\n"
  "v_mfma_f32_16x16x32_bf16 v[0:3], v[68:71], v[144:147], 0\n"
  "ds_read_b64_tr_b16 v[64:65], v92 offset:26624\n"
  "ds_read_b64_tr_b16 v[66:67], v92 offset:26880\n"
  "v_mfma_f32_16x16x32_bf16 v[8:11], v[68:71], v[148:151], 0\n"
  "s_waitcnt vmcnt(1)\n"
  "v_cmp_ne_u32 vcc, s47, v99\n"
  "s_cbranch_vccnz LSAgemm_%=\n"
  "LSAgemmB_%=:\n"
  "s_add_u32 s43, s8, 0\n"
  "buffer_load_dwordx4 v[144:147], v100, s[20:23], s43 offen sc1\n"
  "s_waitcnt lgkmcnt(6)\n"
  "v_mfma_f32_16x16x32_bf16 v[16:19], v[72:75], v[152:155], v[16:19]\n"
  "s_barrier\n"
  "ds_read_b64_tr_b16 v[68:69], v92 offset:9216\n"
  "ds_read_b64_tr_b16 v[70:71], v92 offset:9472\n"
  "v_mul_f32 v48, v48, v32\n"
  "s_setprio 3\n"
  "v_mfma_f32_16x16x32_bf16 v[24:27], v[72:75], v[156:159], v[24:27]\n"
  "v_mul_f32 v49, v49, v33\n"
  "s_add_u32 s43, s9, 0\n"
  "buffer_load_dwordx4 v[148:151], v100, s[20:23], s43 offen sc1\n"
  "s_waitcnt lgkmcnt(6)\n"
  "v_mfma_f32_16x16x32_bf16 v[0:3], v[76:79], v[152:155], v[0:3]\n"
  "ds_read_b64_tr_b16 v[72:73], v92 offset:27648\n"
  "ds_read_b64_tr_b16 v[74:75], v92 offset:27904\n"
  "v_mul_f32 v50, v50, v34\n"
  "v_mfma_f32_16x16x32_bf16 v[8:11], v[76:79], v[156:159], v[8:11]\n"
  "v_mul_f32 v51, v51, v35\n"
  "s_add_u32 s43, s10, 0\n"
  "buffer_load_dwordx4 v[152:155], v100, s[20:23], s43 offen sc1\n"
  "s_waitcnt lgkmcnt(6)\n"
  "v_mfma_f32_16x16x32_bf16 v[16:19], v[80:83], v[176:179], v[16:19]\n"
  "ds_read_b64_tr_b16 v[76:77], v92 offset:10240\n"
  "ds_read_b64_tr_b16 v[78:79], v92 offset:10496\n"
  "v_mul_f32 v56, v56, v40\n"
  "v_mfma_f32_16x16x32_bf16 v[24:27], v[80:83], v[180:183], v[24:27]\n"
  "v_mul_f32 v57, v57, v41\n"
  "s_add_u32 s43, s11, 0\n"
  "buffer_load_dwordx4 v[156:159], v100, s[20:23], s43 offen sc1\n"
  "s_waitcnt lgkmcnt(6)\n"
  "v_mfma_f32_16x16x32_bf16 v[0:3], v[64:67], v[176:179], v[0:3]\n"
  "ds_read_b64_tr_b16 v[80:81], v92 offset:28672\n"
  "ds_read_b64_tr_b16 v[82:83], v92 offset:28928\n"
  "s_add_u32 s44, s30, 8192\n"
  "buffer_store_dwordx4 v[20:23], v100, s[20:23], s44 offen offset:1024 sc1\n"
  "v_mul_f32 v58, v58, v42\n"
  "v_mfma_f32_16x16x32_bf16 v[8:11], v[64:67], v[180:183], v[8:11]\n"
  "v_mul_f32 v59, v59, v43\n"
  "s_add_u32 s45, s78, 0\n"
  "buffer_load_dwordx4 v[176:179], v100, s[20:23], s45 offen offset:0 sc1\n"
  "s_waitcnt lgkmcnt(6)\n"
  "v_mfma_f32_16x16x32_bf16 v[16:19], v[68:71], v[184:187], v[16:19]\n"
  "ds_read_b64_tr_b16 v[64:65], v92 offset:11264\n"
  "ds_read_b64_tr_b16 v[66:67], v92 offset:11520\n"
  "v_cvt_pk_bf16_f32 v32, v48, v49\n"
  "v_mfma_f32_16x16x32_bf16 v[24:27], v[68:71], v[188:191], v[24:27]\n"
  "v_cvt_pk_bf16_f32 v33, v50, v51\n"
  "s_add_u32 s45, s78, 0\n"
  "buffer_load_dwordx4 v[180:183], v100, s[20:23], s45 offen offset:1024 sc1\n"
  "s_waitcnt lgkmcnt(6)\n"
  "v_mfma_f32_16x16x32_bf16 v[0:3], v[72:75], v[184:187], v[0:3]\n"
  "ds_read_b64_tr_b16 v[68:69], v92 offset:29696\n"
  "ds_read_b64_tr_b16 v[70:71], v92 offset:29952\n"
  "v_cvt_pk_bf16_f32 v34, v56, v57\n"
  "v_mfma_f32_16x16x32_bf16 v[8:11], v[72:75], v[188:191], v[8:11]\n"
  "v_cvt_pk_bf16_f32 v35, v58, v59\n"
  "s_add_u32 s45, s78, 0\n"
  "buffer_load_dwordx4 v[184:187], v100, s[20:23], s45 offen offset:2048 sc1\n"
  "s_waitcnt lgkmcnt(6)\n"
  "v_mfma_f32_16x16x32_bf16 v[16:19], v[76:79], v[192:195], v[16:19]\n"
  "ds_read_b64_tr_b16 v[72:73], v92 offset:12288\n"
  "ds_read_b64_tr_b16 v[74:75], v92 offset:12544\n"
  "v_mfma_f32_16x16x32_bf16 v[24:27], v[76:79], v[196:199], v[24:27]\n"
  "v_mul_f32 v52, v52, v36\n"
  "s_add_u32 s45, s78, 0\n"
  "buffer_load_dwordx4 v[188:191], v100, s[20:23], s45 offen offset:3072 sc1\n"
  "s_waitcnt lgkmcnt(6)\n"
  "v_mfma_f32_16x16x32_bf16 v[0:3], v[80:83], v[192:195], v[0:3]\n"
  "ds_read_b64_tr_b16 v[76:77], v92 offset:30720\n"
  "ds_read_b64_tr_b16 v[78:79], v92 offset:30976\n"
  "v_mul_f32 v53, v53, v37\n"
  "v_mfma_f32_16x16x32_bf16 v[8:11], v[80:83], v[196:199], v[8:11]\n"
  "v_mul_f32 v54, v54, v38\n"
  "s_add_u32 s45, s78, 4096\n"
  "buffer_load_dwordx4 v[192:195], v100, s[20:23], s45 offen offset:0 sc1\n"
  "s_waitcnt lgkmcnt(6)\n"
  "v_mfma_f32_16x16x32_bf16 v[16:19], v[64:67], v[200:203], v[16:19]\n"
  "ds_read_b64_tr_b16 v[80:81], v92 offset:6144\n"
  "ds_read_b64_tr_b16 v[82:83], v92 offset:6400\n"
  "v_mul_f32 v55, v55, v39\n"
  "v_mfma_f32_16x16x32_bf16 v[24:27], v[64:67], v[204:207], v[24:27]\n"
  "v_mul_f32 v60, v60, v44\n"
  "s_add_u32 s45, s78, 4096\n"
  "buffer_load_dwordx4 v[196:199], v100, s[20:23], s45 offen offset:1024 sc1\n"
  "s_setprio 2\n"
  "s_waitcnt lgkmcnt(6)\n"
  "v_mfma_f32_16x16x32_bf16 v[0:3], v[68:71], v[200:203], v[0:3]\n"
  "ds_read_b64_tr_b16 v[64:65], v92 offset:7168\n"
  "ds_read_b64_tr_b16 v[66:67], v92 offset:7424\n"
  "v_mul_f32 v61, v61, v45\n"
  "v_mfma_f32_16x16x32_bf16 v[8:11], v[68:71], v[204:207], v[8:11]\n"
  "v_mul_f32 v62, v62, v46\n"
  "s_add_u32 s45, s78, 4096\n"
  "buffer_load_dwordx4 v[200:203], v100, s[20:23], s45 offen offset:2048 sc1\n"
  "s_waitcnt lgkmcnt(6)\n"
  "v_mfma_f32_16x16x32_bf16 v[16:19], v[72:75], v[208:211], v[16:19]\n"
  "ds_read_b64_tr_b16 v[68:69], v92 offset:24576\n"
  "ds_read_b64_tr_b16 v[70:71], v92 offset:24832\n"
  "v_mul_f32 v63, v63, v47\n"
  "v_mfma_f32_16x16x32_bf16 v[24:27], v[72:75], v[212:215], v[24:27]\n"
  "v_cvt_pk_bf16_f32 v36, v52, v53\n"
  "s_add_u32 s45, s78, 4096\n"
  "buffer_load_dwordx4 v[204:207], v100, s[20:23], s45 offen offset:3072 sc1\n"
  "s_waitcnt lgkmcnt(6)\n"
  "v_mfma_f32_16x16x32_bf16 v[0:3], v[76:79], v[208:211], v[0:3]\n"
  "ds_read_b64_tr_b16 v[72:73], v92 offset:13312\n"
  "ds_read_b64_tr_b16 v[74:75], v92 offset:13568\n"
  "v_cvt_pk_bf16_f32 v37, v54, v55\n"
  "v_mfma_f32_16x16x32_bf16 v[8:11], v[76:79], v[212:215], v[8:11]\n"
  "v_cvt_pk_bf16_f32 v38, v60, v61\n"
  "s_add_u32 s45, s78, 8192\n"
  "buffer_load_dwordx4 v[208:211], v100, s[20:23], s45 offen offset:0 sc1\n"
  "s_waitcnt lgkmcnt(6)\n"
  "v_mfma_f32_16x16x32_bf16 v[16:19], v[80:83], v[160:163], v[16:19]\n"
  "ds_read_b64_tr_b16 v[76:77], v92 offset:14336\n"
  "ds_read_b64_tr_b16 v[78:79], v92 offset:14592\n"
  "v_cvt_pk_bf16_f32 v39, v62, v63\n"
  "v_mfma_f32_16x16x32_bf16 v[24:27], v[80:83], v[164:167], v[24:27]\n"
  "s_waitcnt lgkmcnt(6)\n"
  "v_mfma_f32_16x16x32_bf16 v[16:19], v[64:67], v[168:171], v[16:19]\n"
  "ds_read_b64_tr_b16 v[80:81], v92 offset:25600\n"
  "ds_read_b64_tr_b16 v[82:83], v92 offset:25856\n"
  "v_mfma_f32_16x16x32_bf16 v[24:27], v[64:67], v[172:175], v[24:27]\n"
  "s_waitcnt lgkmcnt(6)\n"
  "v_mfma_f32_16x16x32_bf16 v[0:3], v[68:71], v[160:163], v[0:3]\n"
  "ds_read_b64_tr_b16 v[64:65], v92 offset:15360\n"
  "ds_read_b64_tr_b16 v[66:67], v92 offset:15616\n"
  "s_add_u32 s44, s30, 12288\n"
  "buffer_store_dwordx4 v[32:35], v100, s[20:23], s44 offen offset:0 sc1\n"
  "v_mfma_f32_16x16x32_bf16 v[8:11], v[68:71], v[164:167], v[8:11]\n"
  "s_waitcnt lgkmcnt(6)\n"
  "v_mfma_f32_16x16x32_bf16 v[16:19], v[72:75], v[216:219], v[16:19]\n"
  "ds_read_b64_tr_b16 v[68:69], v92 offset:16384\n"
  "ds_read_b64_tr_b16 v[70:71], v92 offset:16640\n"
  "v_mfma_f32_16x16x32_bf16 v[24:27], v[72:75], v[220:223], v[24:27]\n"
  "s_waitcnt lgkmcnt(6)\n"
  "v_mfma_f32_16x16x32_bf16 v[16:19], v[76:79], v[224:227], v[16:19]\n"
  "ds_read_b64_tr_b16 v[72:73], v92 offset:31744\n"
  "ds_read_b64_tr_b16 v[74:75], v92 offset:32000\n"
  "v_mfma_f32_16x16x32_bf16 v[24:27], v[76:79], v[228:231], v[24:27]\n"
  "s_waitcnt lgkmcnt(6)\n"
  "v_mfma_f32_16x16x32_bf16 v[0:3], v[80:83], v[168:171], v[0:3]\n"
  "ds_read_b64_tr_b16 v[76:77], v92 offset:17408\n"
  "ds_read_b64_tr_b16 v[78:79], v92 offset:17664\n"
  "s_setprio 1\n"
  "v_mfma_f32_16x16x32_bf16 v[8:11], v[80:83], v[172:175], v[8:11]\n"
  "s_waitcnt lgkmcnt(6)\n"
  "v_mfma_f32_16x16x32_bf16 v[16:19], v[64:67], v[232:235], v[16:19]\n"
  "ds_read_b64_tr_b16 v[80:81], v92 offset:0\n"
  "ds_read_b64_tr_b16 v[82:83], v92 offset:256\n"
  "v_mfma_f32_16x16x32_bf16 v[24:27], v[64:67], v[236:239], v[24:27]\n"
  "s_waitcnt lgkmcnt(6)\n"
  "v_mfma_f32_16x16x32_bf16 v[16:19], v[68:71], v[240:243], v[16:19]\n"
  "ds_read_b64_tr_b16 v[64:65], v92 offset:32768\n"
  "ds_read_b64_tr_b16 v[66:67], v92 offset:33024\n"
  "v_mfma_f32_16x16x32_bf16 v[24:27], v[68:71], v[244:247], v[24:27]\n"
  "s_waitcnt lgkmcnt(6)\n"
  "v_mfma_f32_16x16x32_bf16 v[0:3], v[72:75], v[216:219], v[0:3]\n"
  "ds_read_b64_tr_b16 v[68:69], v92 offset:1024\n"
  "ds_read_b64_tr_b16 v[70:71], v92 offset:1280\n"
  "v_mfma_f32_16x16x32_bf16 v[8:11], v[72:75], v[220:223], v[8:11]\n"
  "s_waitcnt lgkmcnt(6)\n"
  "v_mfma_f32_16x16x32_bf16 v[16:19], v[76:79], v[248:251], v[16:19]\n"
  "ds_read_b64_tr_b16 v[72:73], v92 offset:2048\n"
  "ds_read_b64_tr_b16 v[74:75], v92 offset:2304\n"
  "v_mfma_f32_16x16x32_bf16 v[24:27], v[76:79], v[252:255], v[24:27]\n"
  "s_waitcnt lgkmcnt(6)\n"
  "v_mfma_f32_16x16x32_bf16 v[16:19], v[80:83], v[112:115], v[16:19]\n"
  "ds_read_b64_tr_b16 v[76:77], v92 offset:33792\n"
  "ds_read_b64_tr_b16 v[78:79], v92 offset:34048\n"
  "v_mfma_f32_16x16x32_bf16 v[24:27], v[80:83], v[116:119], v[24:27]\n"
  "s_waitcnt lgkmcnt(6)\n"
  "v_mfma_f32_16x16x32_bf16 v[0:3], v[64:67], v[224:227], v[0:3]\n"
  "ds_read_b64_tr_b16 v[80:81], v92 offset:3072\n"
  "ds_read_b64_tr_b16 v[82:83], v92 offset:3328\n"
  "v_mfma_f32_16x16x32_bf16 v[8:11], v[64:67], v[228:231], v[8:11]\n"
  "s_waitcnt lgkmcnt(6)\n"
  "v_mfma_f32_16x16x32_bf16 v[16:19], v[68:71], v[120:123], v[16:19]\n"
  "ds_read_b64_tr_b16 v[64:65], v92 offset:34816\n"
  "ds_read_b64_tr_b16 v[66:67], v92 offset:35072\n"
  "v_mfma_f32_16x16x32_bf16 v[24:27], v[68:71], v[124:127], v[24:27]\n"
  "s_waitcnt lgkmcnt(6)\n"
  "v_mfma_f32_16x16x32_bf16 v[16:19], v[72:75], v[128:131], v[16:19]\n"
  "ds_read_b64_tr_b16 v[68:69], v92 offset:35840\n"
  "ds_read_b64_tr_b16 v[70:71], v92 offset:36096\n"
  "v_mfma_f32_16x16x32_bf16 v[24:27], v[72:75], v[132:135], v[24:27]\n"
  "s_setprio 0\n"
  "s_waitcnt lgkmcnt(6)\n"
  "v_mfma_f32_16x16x32_bf16 v[0:3], v[76:79], v[232:235], v[0:3]\n"
  "ds_read_b64_tr_b16 v[72:73], v92 offset:18432\n"
  "ds_read_b64_tr_b16 v[74:75], v92 offset:18688\n"
  "s_add_u32 s44, s30, 12288\n"
  "buffer_store_dwordx4 v[36:39], v100, s[20:23], s44 offen offset:1024 sc1\n"
  "v_mfma_f32_16x16x32_bf16 v[8:11], v[76:79], v[236:239], v[8:11]\n"
  "s_waitcnt lgkmcnt(6)\n"
  "v_mfma_f32_16x16x32_bf16 v[16:19], v[80:83], v[136:139], v[16:19]\n"
  "ds_read_b64_tr_b16 v[76:77], v92 offset:19456\n"
  "ds_read_b64_tr_b16 v[78:79], v92 offset:19712\n"
  "v_mfma_f32_16x16x32_bf16 v[24:27], v[80:83], v[140:143], v[24:27]\n"
  "s_waitcnt lgkmcnt(6)\n"
  "v_mfma_f32_16x16x32_bf16 v[0:3], v[64:67], v[240:243], v[0:3]\n"
  "ds_read_b64_tr_b16 v[80:81], v92 offset:20480\n"
  "ds_read_b64_tr_b16 v[82:83], v92 offset:20736\n"
  "v_mfma_f32_16x16x32_bf16 v[8:11], v[64:67], v[244:247], v[8:11]\n"
  "v_mul_f32 v102, v16, v16\n"
  "v_mul_f32 v103, v17, v17\n"
  "v_mul_f32 v104, v18, v18\n"
  "v_mul_f32 v105, v19, v19\n"
  "v_fmaak_f32 v102, v102, v101, 0xc0135761\n"
  "v_fmaak_f32 v103, v103, v101, 0xc0135761\n"
  "s_waitcnt lgkmcnt(6)\n"
  "v_mfma_f32_16x16x32_bf16 v[0:3], v[68:71], v[248:251], v[0:3]\n"
  "ds_read_b64_tr_b16 v[64:65], v92 offset:21504\n"
  "ds_read_b64_tr_b16 v[66:67], v92 offset:21760\n"
  "v_fmaak_f32 v104, v104, v101, 0xc0135761\n"
  "v_fmaak_f32 v105, v105, v101, 0xc0135761\n"
  "v_mul_f32 v102, v102, v16\n"
  "v_mul_f32 v103, v103, v17\n"
  "v_mul_f32 v104, v104, v18\n"
  "v_mfma_f32_16x16x32_bf16 v[8:11], v[68:71], v[252:255], v[8:11]\n"
  "v_mul_f32 v105, v105, v19\n"
  "v_exp_f32 v102, v102\n"
  "v_exp_f32 v103, v103\n"
  "v_exp_f32 v104, v104\n"
  "v_exp_f32 v105, v105\n"
  "s_waitcnt lgkmcnt(6)\n"
  "v_mfma_f32_16x16x32_bf16 v[0:3], v[72:75], v[112:115], v[0:3]\n"
  "v_add_f32 v102, 1.0, v102\n"
  "v_add_f32 v103, 1.0, v103\n"
  "v_add_f32 v104, 1.0, v104\n"
  "v_add_f32 v105, 1.0, v105\n"
  "v_rcp_f32 v102, v102\n"
  "v_mfma_f32_16x16x32_bf16 v[8:11], v[72:75], v[116:119], v[8:11]\n"
  "v_rcp_f32 v103, v103\n"
  "v_rcp_f32 v104, v104\n"
  "v_rcp_f32 v105, v105\n"
  "v_mul_f32 v16, v16, v102\n"
  "v_mul_f32 v17, v17, v103\n"
  "s_waitcnt lgkmcnt(4)\n"
  "v_mfma_f32_16x16x32_bf16 v[0:3], v[76:79], v[120:123], v[0:3]\n"
  "v_mul_f32 v18, v18, v104\n"
  "v_mul_f32 v19, v19, v105\n"
  "v_mul_f32 v106, v24, v24\n"
  "v_mul_f32 v107, v25, v25\n"
  "v_mul_f32 v108, v26, v26\n"
  "v_mfma_f32_16x16x32_bf16 v[8:11], v[76:79], v[124:127], v[8:11]\n"
  "v_mul_f32 v109, v27, v27\n"
  "v_fmaak_f32 v106, v106, v101, 0xc0135761\n"
  "v_fmaak_f32 v107, v107, v101, 0xc0135761\n"
  "v_fmaak_f32 v108, v108, v101, 0xc0135761\n"
  "v_fmaak_f32 v109, v109, v101, 0xc0135761\n"
  "s_waitcnt lgkmcnt(2)\n"
  "v_mfma_f32_16x16x32_bf16 v[0:3], v[80:83], v[128:131], v[0:3]\n"
  "v_mul_f32 v106, v106, v24\n"
  "v_mul_f32 v107, v107, v25\n"
  "v_mul_f32 v108, v108, v26\n"
  "v_mul_f32 v109, v109, v27\n"
  "v_exp_f32 v106, v106\n"
  "v_mfma_f32_16x16x32_bf16 v[8:11], v[80:83], v[132:135], v[8:11]\n"
  "v_exp_f32 v107, v107\n"
  "v_exp_f32 v108, v108\n"
  "v_exp_f32 v109, v109\n"
  "v_add_f32 v106, 1.0, v106\n"
  "v_add_f32 v107, 1.0, v107\n"
  "s_waitcnt lgkmcnt(0)\n"
  "v_mfma_f32_16x16x32_bf16 v[0:3], v[64:67], v[136:139], v[0:3]\n"
  "v_add_f32 v108, 1.0, v108\n"
  "v_add_f32 v109, 1.0, v109\n"
  "v_rcp_f32 v106, v106\n"
  "v_rcp_f32 v107, v107\n"
  "v_rcp_f32 v108, v108\n"
  "v_mfma_f32_16x16x32_bf16 v[8:11], v[64:67], v[140:143], v[8:11]\n"
  "v_rcp_f32 v109, v109\n"
  "v_mul_f32 v24, v24, v106\n"
  "v_mul_f32 v25, v25, v107\n"
  "v_mul_f32 v26, v26, v108\n"
  "v_mul_f32 v27, v27, v109\n"
  "v_mul_f32 v0, v0, v16\n"
  "v_mul_f32 v1, v1, v17\n"
  "v_mul_f32 v2, v2, v18\n"
  "v_mul_f32 v3, v3, v19\n"
  "v_cvt_pk_bf16_f32 v102, v0, v1\n"
  "v_cvt_pk_bf16_f32 v103, v2, v3\n"
  "v_mul_f32 v8, v8, v24\n"
  "v_mul_f32 v9, v9, v25\n"
  "v_mul_f32 v10, v10, v26\n"
  "v_mul_f32 v11, v11, v27\n"
  "v_cvt_pk_bf16_f32 v104, v8, v9\n"
  "v_cvt_pk_bf16_f32 v105, v10, v11\n"
  "buffer_store_dwordx4 v[102:105], v100, s[20:23], s31 offen sc1\n"
  "s_setprio 0\n"
  "v_mov_b32 v248, v100\n"
  "v_mov_b32 v254, v93\n"
  "v_and_b32 v250, 0xffff, v111\n"
  "v_lshlrev_b32 v250, 2, v250\n"
  "v_readlane_b32 s12, v98, 48\n"
  "v_add_u32 v251, 2176, v98\n"
  "v_cmp_gt_u32 vcc, 896, v100\n"
  "v_add_u32 v249, 4608, v98\n"
  "v_cndmask_b32 v251, v251, v249, vcc\n"
  "v_cmp_gt_u32 vcc, 768, v100\n"
  "v_mov_b32 v249, s12\n"
  "v_cndmask_b32 v251, v251, v249, vcc\n"
  "v_mov_b32 v140, -1\n"
  "v_mov_b32 v141, -1\n"
  "v_mov_b32 v142, -1\n"
  "v_mov_b32 v143, -1\n"
  "v_lshrrev_b32 v255, 1, v248\n"
  "s_add_u32 s15, s83, 4096\n"
  "s_cmp_eq_u32 s84, 0\n"
  "s_cselect_b32 s38, 39133312, 0\n"
  "v_bfe_u32 v252, v248, 2, 5\n"
  "v_add_u32 v252, 163776, v252\n"
  "s_lshl_b32 s12, s68, 2\n"
  "s_add_u32 s12, s12, 163776\n"
  "v_mov_b32 v253, s12\n"
  "s_waitcnt vmcnt(11)\n"
  "v_mfma_f32_16x16x32_bf16 v[0:3], v[176:179], v[144:147], 0\n"
  "s_add_u32 s43, s8, 4096\n"
  "buffer_load_dwordx4 v[160:163], v248, s[20:23], s43 offen sc1\n"
  "v_mfma_f32_16x16x32_bf16 v[36:39], v[176:179], v[148:151], 0\n"
  "s_add_u32 s43, s9, 4096\n"
  "buffer_load_dwordx4 v[164:167], v248, s[20:23], s43 offen sc1\n"
  "buffer_store_dwordx4 v[140:143], v248, s[36:39], s83 offen offset:0 sc1\n"
  "v_mfma_f32_16x16x32_bf16 v[72:75], v[176:179], v[152:155], 0\n"
  "s_add_u32 s43, s10, 4096\n"
  "buffer_load_dwordx4 v[168:171], v248, s[20:23], s43 offen sc1\n"
  "v_mfma_f32_16x16x32_bf16 v[108:111], v[176:179], v[156:159], 0\n"
  "s_add_u32 s43, s11, 4096\n"
  "buffer_load_dwordx4 v[172:175], v248, s[20:23], s43 offen sc1\n"
  "s_add_u32 s45, s78, 147456\n"
  "buffer_load_dwordx4 v[176:179], v248, s[20:23], s45 offen offset:0 sc1\n"
  "s_waitcnt vmcnt(16)\n"
  "v_mfma_f32_16x16x32_bf16 v[4:7], v[180:183], v[144:147], 0\n"
  "s_add_u32 s13, s78, 36864\n"
  "buffer_load_dwordx4 v[212:215], v248, s[20:23], s13 offen offset:0 sc1\n"
  "v_mfma_f32_16x16x32_bf16 v[40:43], v[180:183], v[148:151], 0\n"
  "s_add_u32 s13, s78, 36864\n"
  "buffer_load_dwordx4 v[216:219], v248, s[20:23], s13 offen offset:1024 sc1\n"
  "v_mfma_f32_16x16x32_bf16 v[76:79], v[180:183], v[152:155], 0\n"
  "s_add_u32 s13, s78, 36864\n"
  "buffer_load_dwordx4 v[220:223], v248, s[20:23], s13 offen offset:2048 sc1\n"
  "v_mfma_f32_16x16x32_bf16 v[112:115], v[180:183], v[156:159], 0\n"
  "s_add_u32 s13, s78, 36864\n"
  "buffer_load_dwordx4 v[224:227], v248, s[20:23], s13 offen offset:3072 sc1\n"
  "buffer_load_dwordx4 v[180:183], v248, s[20:23], s45 offen offset:1024 sc1\n"
  "s_waitcnt vmcnt(20)\n"
  "v_mfma_f32_16x16x32_bf16 v[8:11], v[184:187], v[144:147], 0\n"
  "s_add_u32 s13, s78, 40960\n"
  "buffer_load_dwordx4 v[228:231], v248, s[20:23], s13 offen offset:0 sc1\n"
  "buffer_store_dwordx4 v[140:143], v248, s[36:39], s83 offen offset:1024 sc1\n"
  "v_mfma_f32_16x16x32_bf16 v[44:47], v[184:187], v[148:151], 0\n"
  "s_add_u32 s13, s78, 40960\n"
  "buffer_load_dwordx4 v[232:235], v248, s[20:23], s13 offen offset:1024 sc1\n"
  "v_mfma_f32_16x16x32_bf16 v[80:83], v[184:187], v[152:155], 0\n"
  "s_add_u32 s13, s78, 40960\n"
  "buffer_load_dwordx4 v[236:239], v248, s[20:23], s13 offen offset:2048 sc1\n"
  "v_mfma_f32_16x16x32_bf16 v[116:119], v[184:187], v[156:159], 0\n"
  "s_add_u32 s13, s78, 40960\n"
  "buffer_load_dwordx4 v[240:243], v248, s[20:23], s13 offen offset:3072 sc1\n"
  "buffer_load_dwordx4 v[184:187], v248, s[20:23], s45 offen offset:2048 sc1\n"
  "s_waitcnt vmcnt(25)\n"
  "v_mfma_f32_16x16x32_bf16 v[12:15], v[188:191], v[144:147], 0\n"
  "s_add_u32 s13, s78, 45056\n"
  "buffer_load_dwordx4 v[244:247], v248, s[20:23], s13 offen offset:0 sc1\n"
  "v_mfma_f32_16x16x32_bf16 v[48:51], v[188:191], v[148:151], 0\n"
  "v_mfma_f32_16x16x32_bf16 v[84:87], v[188:191], v[152:155], 0\n"
  "v_mfma_f32_16x16x32_bf16 v[120:123], v[188:191], v[156:159], 0\n"
  "buffer_store_dwordx4 v[140:143], v248, s[36:39], s83 offen offset:2048 sc1\n"
  "buffer_load_dwordx4 v[188:191], v248, s[20:23], s45 offen offset:3072 sc1\n"
  "s_waitcnt vmcnt(27)\n"
  "v_mfma_f32_16x16x32_bf16 v[16:19], v[192:195], v[144:147], 0\n"
  "v_mfma_f32_16x16x32_bf16 v[52:55], v[192:195], v[148:151], 0\n"
  "v_mfma_f32_16x16x32_bf16 v[88:91], v[192:195], v[152:155], 0\n"
  "v_mfma_f32_16x16x32_bf16 v[124:127], v[192:195], v[156:159], 0\n"
  "s_add_u32 s45, s78, 151552\n"
  "buffer_load_dwordx4 v[192:195], v248, s[20:23], s45 offen offset:0 sc1\n"
  "s_waitcnt vmcnt(27)\n"
  "v_mfma_f32_16x16x32_bf16 v[20:23], v[196:199], v[144:147], 0\n"
  "v_mfma_f32_16x16x32_bf16 v[56:59], v[196:199], v[148:151], 0\n"
  "v_mfma_f32_16x16x32_bf16 v[92:95], v[196:199], v[152:155], 0\n"
  "buffer_store_dwordx4 v[140:143], v248, s[36:39], s83 offen offset:3072 sc1\n"
  "v_mfma_f32_16x16x32_bf16 v[128:131], v[196:199], v[156:159], 0\n"
  "buffer_load_dwordx4 v[196:199], v248, s[20:23], s45 offen offset:1024 sc1\n"
  "s_waitcnt vmcnt(28)\n"
  "v_mfma_f32_16x16x32_bf16 v[24:27], v[200:203], v[144:147], 0\n"
  "v_mfma_f32_16x16x32_bf16 v[60:63], v[200:203], v[148:151], 0\n"
  "v_mfma_f32_16x16x32_bf16 v[96:99], v[200:203], v[152:155], 0\n"
  "v_mfma_f32_16x16x32_bf16 v[132:135], v[200:203], v[156:159], 0\n"
  "buffer_load_dwordx4 v[200:203], v248, s[20:23], s45 offen offset:2048 sc1\n"
  "s_waitcnt vmcnt(28)\n"
  "v_mfma_f32_16x16x32_bf16 v[28:31], v[204:207], v[144:147], 0\n"
  "v_mfma_f32_16x16x32_bf16 v[64:67], v[204:207], v[148:151], 0\n"
  "buffer_store_dwordx2 v[140:141], v255, s[36:39], s15 offen sc1\n"
  "v_mfma_f32_16x16x32_bf16 v[100:103], v[204:207], v[152:155], 0\n"
  "v_mfma_f32_16x16x32_bf16 v[136:139], v[204:207], v[156:159], 0\n"
  "v_mov_b32 v255, v251\n"
  "buffer_load_dwordx4 v[204:207], v248, s[20:23], s45 offen offset:3072 sc1\n"
  "s_waitcnt vmcnt(29)\n"
  "v_mfma_f32_16x16x32_bf16 v[32:35], v[208:211], v[144:147], 0\n"
  "s_add_u32 s43, s8, 16384\n"
  "buffer_load_dwordx4 v[144:147], v248, s[20:23], s43 offen sc1\n"
  "v_mfma_f32_16x16x32_bf16 v[68:71], v[208:211], v[148:151], 0\n"
  "s_add_u32 s43, s9, 16384\n"
  "buffer_load_dwordx4 v[148:151], v248, s[20:23], s43 offen sc1\n"
  "v_mfma_f32_16x16x32_bf16 v[104:107], v[208:211], v[152:155], 0\n"
  "s_add_u32 s43, s10, 16384\n"
  "buffer_load_dwordx4 v[152:155], v248, s[20:23], s43 offen sc1\n"
  "v_mfma_f32_16x16x32_bf16 v[140:143], v[208:211], v[156:159], 0\n"
  "s_add_u32 s43, s11, 16384\n"
  "buffer_load_dwordx4 v[156:159], v248, s[20:23], s43 offen sc1\n"
  "s_add_u32 s45, s78, 155648\n"
  "buffer_load_dwordx4 v[208:211], v248, s[20:23], s45 offen offset:0 sc1\n"
  "s_waitcnt vmcnt(24)\n"
  "v_mfma_f32_16x16x32_bf16 v[0:3], v[212:215], v[160:163], v[0:3]\n"
  "v_mov_b32 v249, 1\n"
  "ds_write_b32 v253, v249\n"
  "v_mfma_f32_16x16x32_bf16 v[36:39], v[212:215], v[164:167], v[36:39]\n"
  "v_mfma_f32_16x16x32_bf16 v[72:75], v[212:215], v[168:171], v[72:75]\n"
  "v_mfma_f32_16x16x32_bf16 v[108:111], v[212:215], v[172:175], v[108:111]\n"
  "s_add_u32 s45, s78, 184320\n"
  "buffer_load_dwordx4 v[212:215], v248, s[20:23], s45 offen offset:0 sc1\n"
  "s_waitcnt vmcnt(24)\n"
  "v_mfma_f32_16x16x32_bf16 v[4:7], v[216:219], v[160:163], v[4:7]\n"
  "s_lshl_b32 s12, s81, 13\n"
  "s_add_u32 m0, s12, 36864\n"
  "s_mul_i32 s14, s81, 7168\n"
  "s_add_u32 s14, s14, s72\n"
  "s_add_u32 s14, s14, 65536\n"
  "buffer_load_dwordx4 v248, s[20:23], s14 offen offset:0 sc1 lds\n"
  "v_mfma_f32_16x16x32_bf16 v[40:43], v[216:219], v[164:167], v[40:43]\n"
  "v_mfma_f32_16x16x32_bf16 v[76:79], v[216:219], v[168:171], v[76:79]\n"
  "v_mfma_f32_16x16x32_bf16 v[112:115], v[216:219], v[172:175], v[112:115]\n"
  "buffer_load_dwordx4 v[216:219], v248, s[20:23], s45 offen offset:1024 sc1\n"
  "s_waitcnt vmcnt(25)\n"
  "v_mfma_f32_16x16x32_bf16 v[8:11], v[220:223], v[160:163], v[8:11]\n"
  "v_mfma_f32_16x16x32_bf16 v[44:47], v[220:223], v[164:167], v[44:47]\n"
  "v_mfma_f32_16x16x32_bf16 v[80:83], v[220:223], v[168:171], v[80:83]\n"
  "buffer_load_dwordx4 v248, s[20:23], s14 offen offset:1024 sc1 lds\n"
  "v_mfma_f32_16x16x32_bf16 v[116:119], v[220:223], v[172:175], v[116:119]\n"
  "buffer_load_dwordx4 v[220:223], v248, s[20:23], s45 offen offset:2048 sc1\n"
  "s_waitcnt vmcnt(26)\n"
  "v_mfma_f32_16x16x32_bf16 v[12:15], v[224:227], v[160:163], v[12:15]\n"
  "v_mfma_f32_16x16x32_bf16 v[48:51], v[224:227], v[164:167], v[48:51]\n"
  "v_mfma_f32_16x16x32_bf16 v[84:87], v[224:227], v[168:171], v[84:87]\n"
  "v_mfma_f32_16x16x32_bf16 v[120:123], v[224:227], v[172:175], v[120:123]\n"
  "buffer_load_dwordx4 v[224:227], v248, s[20:23], s45 offen offset:3072 sc1\n"
  "s_waitcnt vmcnt(25)\n"
  "v_mfma_f32_16x16x32_bf16 v[16:19], v[228:231], v[160:163], v[16:19]\n"
  "buffer_load_dwordx4 v248, s[20:23], s14 offen offset:2048 sc1 lds\n"
  "v_mfma_f32_16x16x32_bf16 v[52:55], v[228:231], v[164:167], v[52:55]\n"
  "v_mfma_f32_16x16x32_bf16 v[88:91], v[228:231], v[168:171], v[88:91]\n"
  "v_mfma_f32_16x16x32_bf16 v[124:127], v[228:231], v[172:175], v[124:127]\n"
  "s_add_u32 s45, s78, 188416\n"
  "buffer_load_dwordx4 v[228:231], v248, s[20:23], s45 offen offset:0 sc1\n"
  "s_waitcnt vmcnt(25)\n"
  "v_mfma_f32_16x16x32_bf16 v[20:23], v[232:235], v[160:163], v[20:23]\n"
  "v_mfma_f32_16x16x32_bf16 v[56:59], v[232:235], v[164:167], v[56:59]\n"
  "v_mfma_f32_16x16x32_bf16 v[92:95], v[232:235], v[168:171], v[92:95]\n"
  "buffer_load_dwordx4 v248, s[20:23], s14 offen offset:3072 sc1 lds\n"
  "v_mfma_f32_16x16x32_bf16 v[128:131], v[232:235], v[172:175], v[128:131]\n"
  "buffer_load_dwordx4 v[232:235], v248, s[20:23], s45 offen offset:1024 sc1\n"
  "s_waitcnt vmcnt(26)\n"
  "v_mfma_f32_16x16x32_bf16 v[24:27], v[236:239], v[160:163], v[24:27]\n"
  "v_mfma_f32_16x16x32_bf16 v[60:63], v[236:239], v[164:167], v[60:63]\n"
  "v_mfma_f32_16x16x32_bf16 v[96:99], v[236:239], v[168:171], v[96:99]\n"
  "v_mfma_f32_16x16x32_bf16 v[132:135], v[236:239], v[172:175], v[132:135]\n"
  "buffer_load_dwordx4 v[236:239], v248, s[20:23], s45 offen offset:2048 sc1\n"
  "s_waitcnt vmcnt(26)\n"
  "v_mfma_f32_16x16x32_bf16 v[28:31], v[240:243], v[160:163], v[28:31]\n"
  "s_lshl_b32 s12, s81, 13\n"
  "s_add_u32 m0, s12, 40960\n"
  "s_mul_i32 s14, s81, 7168\n"
  "s_add_u32 s14, s14, s72\n"
  "s_add_u32 s14, s14, 69632\n"
  "buffer_load_dwordx4 v248, s[20:23], s14 offen offset:0 sc1 lds\n"
  "v_mfma_f32_16x16x32_bf16 v[64:67], v[240:243], v[164:167], v[64:67]\n"
  "v_mfma_f32_16x16x32_bf16 v[100:103], v[240:243], v[168:171], v[100:103]\n"
  "v_mfma_f32_16x16x32_bf16 v[136:139], v[240:243], v[172:175], v[136:139]\n"
  "buffer_load_dwordx4 v[240:243], v248, s[20:23], s45 offen offset:3072 sc1\n"
  "s_waitcnt vmcnt(26)\n"
  "v_mfma_f32_16x16x32_bf16 v[32:35], v[244:247], v[160:163], v[32:35]\n"
  "s_add_u32 s43, s8, 20480\n"
  "buffer_load_dwordx4 v[160:163], v248, s[20:23], s43 offen sc1\n"
  "v_mfma_f32_16x16x32_bf16 v[68:71], v[244:247], v[164:167], v[68:71]\n"
  "s_add_u32 s43, s9, 20480\n"
  "buffer_load_dwordx4 v[164:167], v248, s[20:23], s43 offen sc1\n"
  "v_mfma_f32_16x16x32_bf16 v[104:107], v[244:247], v[168:171], v[104:107]\n"
  "s_add_u32 s43, s10, 20480\n"
  "buffer_load_dwordx4 v[168:171], v248, s[20:23], s43 offen sc1\n"
  "buffer_load_dwordx4 v248, s[20:23], s14 offen offset:1024 sc1 lds\n"
  "v_mfma_f32_16x16x32_bf16 v[140:143], v[244:247], v[172:175], v[140:143]\n"
  "s_add_u32 s43, s11, 20480\n"
  "buffer_load_dwordx4 v[172:175], v248, s[20:23], s43 offen sc1\n"
  "s_add_u32 s45, s78, 192512\n"
  "buffer_load_dwordx4 v[244:247], v248, s[20:23], s45 offen offset:0 sc1\n"
  "s_waitcnt vmcnt(23)\n"
  "v_mfma_f32_16x16x32_bf16 v[0:3], v[176:179], v[144:147], v[0:3]\n"
  "v_mov_b32 v249, s47\n"
  "s_mov_b64 exec, 1\n"
  "buffer_store_dword v249, off, s[20:23], s69 offset:384 sc1\n"
  "s_mov_b64 exec, -1\n"
  "s_waitcnt vmcnt(22)\n"
  "v_mfma_f32_16x16x32_bf16 v[36:39], v[176:179], v[148:151], v[36:39]\n"
  "s_waitcnt vmcnt(21)\n"
  "v_mfma_f32_16x16x32_bf16 v[72:75], v[176:179], v[152:155], v[72:75]\n"
  "s_waitcnt vmcnt(20)\n"
  "v_mfma_f32_16x16x32_bf16 v[108:111], v[176:179], v[156:159], v[108:111]\n"
  "s_add_u32 s45, s78, 294912\n"
  "buffer_load_dwordx4 v[176:179], v248, s[20:23], s45 offen offset:0 sc1\n"
  "v_mfma_f32_16x16x32_bf16 v[4:7], v[180:183], v[144:147], v[4:7]\n"
  "buffer_load_dwordx4 v248, s[20:23], s14 offen offset:2048 sc1 lds\n"
  "v_mfma_f32_16x16x32_bf16 v[40:43], v[180:183], v[148:151], v[40:43]\n"
  "v_mfma_f32_16x16x32_bf16 v[76:79], v[180:183], v[152:155], v[76:79]\n"
  "v_mfma_f32_16x16x32_bf16 v[112:115], v[180:183], v[156:159], v[112:115]\n"
  "buffer_load_dwordx4 v[180:183], v248, s[20:23], s45 offen offset:1024 sc1\n"
  "v_mfma_f32_16x16x32_bf16 v[8:11], v[184:187], v[144:147], v[8:11]\n"
  "v_mfma_f32_16x16x32_bf16 v[44:47], v[184:187], v[148:151], v[44:47]\n"
  "v_mfma_f32_16x16x32_bf16 v[80:83], v[184:187], v[152:155], v[80:83]\n"
  "buffer_load_dwordx4 v248, s[20:23], s14 offen offset:3072 sc1 lds\n"
  "v_mfma_f32_16x16x32_bf16 v[116:119], v[184:187], v[156:159], v[116:119]\n"
  "buffer_load_dwordx4 v[184:187], v248, s[20:23], s45 offen offset:2048 sc1\n"
  "v_mfma_f32_16x16x32_bf16 v[12:15], v[188:191], v[144:147], v[12:15]\n"
  "v_mfma_f32_16x16x32_bf16 v[48:51], v[188:191], v[148:151], v[48:51]\n"
  "v_mfma_f32_16x16x32_bf16 v[84:87], v[188:191], v[152:155], v[84:87]\n"
  "v_mfma_f32_16x16x32_bf16 v[120:123], v[188:191], v[156:159], v[120:123]\n"
  "buffer_load_dwordx4 v[188:191], v248, s[20:23], s45 offen offset:3072 sc1\n"
  "v_mfma_f32_16x16x32_bf16 v[16:19], v[192:195], v[144:147], v[16:19]\n"
  "s_and_b32 s12, s68, 1\n"
  "s_lshl_b32 s12, s12, 10\n"
  "s_add_u32 m0, s12, 69632\n"
  "s_and_b32 s12, s68, 6\n"
  "s_lshl_b32 s12, s12, 10\n"
  "s_sub_u32 s14, s72, s12\n"
  "s_add_u32 s14, s14, 102400\n"
  "buffer_load_dwordx4 v248, s[20:23], s14 offen sc1 lds\n"
  "v_mfma_f32_16x16x32_bf16 v[52:55], v[192:195], v[148:151], v[52:55]\n"
  "v_mfma_f32_16x16x32_bf16 v[88:91], v[192:195], v[152:155], v[88:91]\n"
  "v_mfma_f32_16x16x32_bf16 v[124:127], v[192:195], v[156:159], v[124:127]\n"
  "s_add_u32 s45, s78, 299008\n"
  "buffer_load_dwordx4 v[192:195], v248, s[20:23], s45 offen offset:0 sc1\n"
  "v_mfma_f32_16x16x32_bf16 v[20:23], v[196:199], v[144:147], v[20:23]\n"
  "v_mfma_f32_16x16x32_bf16 v[56:59], v[196:199], v[148:151], v[56:59]\n"
  "v_mfma_f32_16x16x32_bf16 v[92:95], v[196:199], v[152:155], v[92:95]\n"
  "s_lshl_b32 s12, s68, 8\n"
  "s_add_u32 m0, s12, 71680\n"
  "s_add_u32 s14, s73, 8192\n"
  "buffer_load_dword v254, s[20:23], s14 offen sc1 lds\n"
  "v_mfma_f32_16x16x32_bf16 v[128:131], v[196:199], v[156:159], v[128:131]\n"
  "buffer_load_dwordx4 v[196:199], v248, s[20:23], s45 offen offset:1024 sc1\n"
  "v_mfma_f32_16x16x32_bf16 v[24:27], v[200:203], v[144:147], v[24:27]\n"
  "v_mfma_f32_16x16x32_bf16 v[60:63], v[200:203], v[148:151], v[60:63]\n"
  "v_mfma_f32_16x16x32_bf16 v[96:99], v[200:203], v[152:155], v[96:99]\n"
  "v_mfma_f32_16x16x32_bf16 v[132:135], v[200:203], v[156:159], v[132:135]\n"
  "buffer_load_dwordx4 v[200:203], v248, s[20:23], s45 offen offset:2048 sc1\n"
  "v_mfma_f32_16x16x32_bf16 v[28:31], v[204:207], v[144:147], v[28:31]\n"
  "s_lshl_b32 s12, s68, 8\n"
  "s_add_u32 m0, s12, 70656\n"
  "s_add_u32 s14, s73, 6144\n"
  "buffer_load_dword v254, s[20:23], s14 offen sc1 lds\n"
  "v_mfma_f32_16x16x32_bf16 v[64:67], v[204:207], v[148:151], v[64:67]\n"
  "v_mfma_f32_16x16x32_bf16 v[100:103], v[204:207], v[152:155], v[100:103]\n"
  "v_mfma_f32_16x16x32_bf16 v[136:139], v[204:207], v[156:159], v[136:139]\n"
  "buffer_load_dwordx4 v[204:207], v248, s[20:23], s45 offen offset:3072 sc1\n"
  "s_waitcnt vmcnt(32)\n"
  "v_mfma_f32_16x16x32_bf16 v[32:35], v[208:211], v[144:147], v[32:35]\n"
  "s_add_u32 s43, s8, 32768\n"
  "buffer_load_dwordx4 v[144:147], v248, s[20:23], s43 offen sc1\n"
  "v_mfma_f32_16x16x32_bf16 v[68:71], v[208:211], v[148:151], v[68:71]\n"
  "s_add_u32 s43, s9, 32768\n"
  "buffer_load_dwordx4 v[148:151], v248, s[20:23], s43 offen sc1\n"
  "v_mfma_f32_16x16x32_bf16 v[104:107], v[208:211], v[152:155], v[104:107]\n"
  "s_add_u32 s43, s10, 32768\n"
  "buffer_load_dwordx4 v[152:155], v248, s[20:23], s43 offen sc1\n"
  "v_mfma_f32_16x16x32_bf16 v[140:143], v[208:211], v[156:159], v[140:143]\n"
  "s_add_u32 s43, s11, 32768\n"
  "buffer_load_dwordx4 v[156:159], v248, s[20:23], s43 offen sc1\n"
  "s_add_u32 s45, s78, 303104\n"
  "buffer_load_dwordx4 v[208:211], v248, s[20:23], s45 offen offset:0 sc1\n"
  "s_waitcnt vmcnt(23)\n"
  "v_mfma_f32_16x16x32_bf16 v[0:3], v[212:215], v[160:163], v[0:3]\n"
  "s_waitcnt vmcnt(22)\n"
  "v_mfma_f32_16x16x32_bf16 v[36:39], v[212:215], v[164:167], v[36:39]\n"
  "s_waitcnt vmcnt(21)\n"
  "v_mfma_f32_16x16x32_bf16 v[72:75], v[212:215], v[168:171], v[72:75]\n"
  "s_waitcnt vmcnt(19)\n"
  "v_mfma_f32_16x16x32_bf16 v[108:111], v[212:215], v[172:175], v[108:111]\n"
  "s_add_u32 s45, s78, 331776\n"
  "buffer_load_dwordx4 v[212:215], v248, s[20:23], s45 offen offset:0 sc1\n"
  "v_mfma_f32_16x16x32_bf16 v[4:7], v[216:219], v[160:163], v[4:7]\n"
  "v_mfma_f32_16x16x32_bf16 v[40:43], v[216:219], v[164:167], v[40:43]\n"
  "v_mfma_f32_16x16x32_bf16 v[76:79], v[216:219], v[168:171], v[76:79]\n"
  "v_mfma_f32_16x16x32_bf16 v[112:115], v[216:219], v[172:175], v[112:115]\n"
  "buffer_load_dwordx4 v[216:219], v248, s[20:23], s45 offen offset:1024 sc1\n"
  "v_mfma_f32_16x16x32_bf16 v[8:11], v[220:223], v[160:163], v[8:11]\n"
  "v_mfma_f32_16x16x32_bf16 v[44:47], v[220:223], v[164:167], v[44:47]\n"
  "v_mfma_f32_16x16x32_bf16 v[80:83], v[220:223], v[168:171], v[80:83]\n"
  "v_mfma_f32_16x16x32_bf16 v[116:119], v[220:223], v[172:175], v[116:119]\n"
  "buffer_load_dwordx4 v[220:223], v248, s[20:23], s45 offen offset:2048 sc1\n"
  "v_mfma_f32_16x16x32_bf16 v[12:15], v[224:227], v[160:163], v[12:15]\n"
  "v_mfma_f32_16x16x32_bf16 v[48:51], v[224:227], v[164:167], v[48:51]\n"
  "v_mfma_f32_16x16x32_bf16 v[84:87], v[224:227], v[168:171], v[84:87]\n"
  "v_mfma_f32_16x16x32_bf16 v[120:123], v[224:227], v[172:175], v[120:123]\n"
  "buffer_load_dwordx4 v[224:227], v248, s[20:23], s45 offen offset:3072 sc1\n"
  "v_mfma_f32_16x16x32_bf16 v[16:19], v[228:231], v[160:163], v[16:19]\n"
  "v_mfma_f32_16x16x32_bf16 v[52:55], v[228:231], v[164:167], v[52:55]\n"
  "v_mfma_f32_16x16x32_bf16 v[88:91], v[228:231], v[168:171], v[88:91]\n"
  "v_mfma_f32_16x16x32_bf16 v[124:127], v[228:231], v[172:175], v[124:127]\n"
  "s_add_u32 s45, s78, 335872\n"
  "buffer_load_dwordx4 v[228:231], v248, s[20:23], s45 offen offset:0 sc1\n"
  "v_mfma_f32_16x16x32_bf16 v[20:23], v[232:235], v[160:163], v[20:23]\n"
  "v_mfma_f32_16x16x32_bf16 v[56:59], v[232:235], v[164:167], v[56:59]\n"
  "v_mfma_f32_16x16x32_bf16 v[92:95], v[232:235], v[168:171], v[92:95]\n"
  "v_mfma_f32_16x16x32_bf16 v[128:131], v[232:235], v[172:175], v[128:131]\n"
  "buffer_load_dwordx4 v[232:235], v248, s[20:23], s45 offen offset:1024 sc1\n"
  "v_mfma_f32_16x16x32_bf16 v[24:27], v[236:239], v[160:163], v[24:27]\n"
  "v_mfma_f32_16x16x32_bf16 v[60:63], v[236:239], v[164:167], v[60:63]\n"
  "v_mfma_f32_16x16x32_bf16 v[96:99], v[236:239], v[168:171], v[96:99]\n"
  "v_mfma_f32_16x16x32_bf16 v[132:135], v[236:239], v[172:175], v[132:135]\n"
  "buffer_load_dwordx4 v[236:239], v248, s[20:23], s45 offen offset:2048 sc1\n"
  "v_mfma_f32_16x16x32_bf16 v[28:31], v[240:243], v[160:163], v[28:31]\n"
  "v_mfma_f32_16x16x32_bf16 v[64:67], v[240:243], v[164:167], v[64:67]\n"
  "v_mfma_f32_16x16x32_bf16 v[100:103], v[240:243], v[168:171], v[100:103]\n"
  "v_mfma_f32_16x16x32_bf16 v[136:139], v[240:243], v[172:175], v[136:139]\n"
  "buffer_load_dwordx4 v[240:243], v248, s[20:23], s45 offen offset:3072 sc1\n"
  "s_waitcnt vmcnt(26)\n"
  "v_mfma_f32_16x16x32_bf16 v[32:35], v[244:247], v[160:163], v[32:35]\n"
  "s_add_u32 s43, s8, 36864\n"
  "buffer_load_dwordx4 v[160:163], v248, s[20:23], s43 offen sc1\n"
  "v_mfma_f32_16x16x32_bf16 v[68:71], v[244:247], v[164:167], v[68:71]\n"
  "s_add_u32 s43, s9, 36864\n"
  "buffer_load_dwordx4 v[164:167], v248, s[20:23], s43 offen sc1\n"
  "v_mfma_f32_16x16x32_bf16 v[104:107], v[244:247], v[168:171], v[104:107]\n"
  "s_add_u32 s43, s10, 36864\n"
  "buffer_load_dwordx4 v[168:171], v248, s[20:23], s43 offen sc1\n"
  "v_mfma_f32_16x16x32_bf16 v[140:143], v[244:247], v[172:175], v[140:143]\n"
  "s_add_u32 s43, s11, 36864\n"
  "buffer_load_dwordx4 v[172:175], v248, s[20:23], s43 offen sc1\n"
  "s_add_u32 s45, s78, 339968\n"
  "buffer_load_dwordx4 v[244:247], v248, s[20:23], s45 offen offset:0 sc1\n"
  "s_waitcnt vmcnt(17)\n"
  "v_mfma_f32_16x16x32_bf16 v[0:3], v[176:179], v[144:147], v[0:3]\n"
  "s_waitcnt vmcnt(16)\n"
  "v_mfma_f32_16x16x32_bf16 v[36:39], v[176:179], v[148:151], v[36:39]\n"
  "s_waitcnt vmcnt(15)\n"
  "v_mfma_f32_16x16x32_bf16 v[72:75], v[176:179], v[152:155], v[72:75]\n"
  "ds_read_b32 v251, v252\n"
  "s_waitcnt vmcnt(14)\n"
  "v_mfma_f32_16x16x32_bf16 v[108:111], v[176:179], v[156:159], v[108:111]\n"
  "s_add_u32 s45, s78, 442368\n"
  "buffer_load_dwordx4 v[176:179], v248, s[20:23], s45 offen offset:0 sc1\n"
  "v_mfma_f32_16x16x32_bf16 v[4:7], v[180:183], v[144:147], v[4:7]\n"
  "v_mfma_f32_16x16x32_bf16 v[40:43], v[180:183], v[148:151], v[40:43]\n"
  "v_mfma_f32_16x16x32_bf16 v[76:79], v[180:183], v[152:155], v[76:79]\n"
  "v_mov_b32 v249, 2\n"
  "ds_write_b32 v253, v249\n"
  "s_waitcnt lgkmcnt(1)\n"
  "v_cmp_gt_u32 vcc, 1, v251\n"
  "s_cbranch_vccnz LSE_%=\n"
  "LSEB_%=:\n"
  "v_mfma_f32_16x16x32_bf16 v[112:115], v[180:183], v[156:159], v[112:115]\n"
  "s_lshl_b32 s12, s81, 13\n"
  "s_add_u32 m0, s12, 0\n"
  "s_mul_i32 s14, s81, 7168\n"
  "s_add_u32 s14, s14, s72\n"
  "s_add_u32 s14, s14, 100352\n"
  "buffer_load_dwordx4 v248, s[20:23], s14 offen offset:0 sc1 lds\n"
  "buffer_load_dwordx4 v[180:183], v248, s[20:23], s45 offen offset:1024 sc1\n"
  "v_mfma_f32_16x16x32_bf16 v[8:11], v[184:187], v[144:147], v[8:11]\n"
  "v_mfma_f32_16x16x32_bf16 v[44:47], v[184:187], v[148:151], v[44:47]\n"
  "v_mfma_f32_16x16x32_bf16 v[80:83], v[184:187], v[152:155], v[80:83]\n"
  "v_mfma_f32_16x16x32_bf16 v[116:119], v[184:187], v[156:159], v[116:119]\n"
  "buffer_load_dwordx4 v[184:187], v248, s[20:23], s45 offen offset:2048 sc1\n"
  "v_mfma_f32_16x16x32_bf16 v[12:15], v[188:191], v[144:147], v[12:15]\n"
  "v_mfma_f32_16x16x32_bf16 v[48:51], v[188:191], v[148:151], v[48:51]\n"
  "buffer_load_dwordx4 v248, s[20:23], s14 offen offset:1024 sc1 lds\n"
  "v_mfma_f32_16x16x32_bf16 v[84:87], v[188:191], v[152:155], v[84:87]\n"
  "v_mfma_f32_16x16x32_bf16 v[120:123], v[188:191], v[156:159], v[120:123]\n"
  "buffer_load_dwordx4 v[188:191], v248, s[20:23], s45 offen offset:3072 sc1\n"
  "v_mfma_f32_16x16x32_bf16 v[16:19], v[192:195], v[144:147], v[16:19]\n"
  "v_mfma_f32_16x16x32_bf16 v[52:55], v[192:195], v[148:151], v[52:55]\n"
  "v_mfma_f32_16x16x32_bf16 v[88:91], v[192:195], v[152:155], v[88:91]\n"
  "v_mfma_f32_16x16x32_bf16 v[124:127], v[192:195], v[156:159], v[124:127]\n"
  "buffer_load_dwordx4 v248, s[20:23], s14 offen offset:2048 sc1 lds\n"
  "s_add_u32 s45, s78, 446464\n"
  "buffer_load_dwordx4 v[192:195], v248, s[20:23], s45 offen offset:0 sc1\n"
  "v_mfma_f32_16x16x32_bf16 v[20:23], v[196:199], v[144:147], v[20:23]\n"
  "v_mfma_f32_16x16x32_bf16 v[56:59], v[196:199], v[148:151], v[56:59]\n"
  "v_mfma_f32_16x16x32_bf16 v[92:95], v[196:199], v[152:155], v[92:95]\n"
  "v_mfma_f32_16x16x32_bf16 v[128:131], v[196:199], v[156:159], v[128:131]\n"
  "buffer_load_dwordx4 v[196:199], v248, s[20:23], s45 offen offset:1024 sc1\n"
  "v_mfma_f32_16x16x32_bf16 v[24:27], v[200:203], v[144:147], v[24:27]\n"
  "v_mfma_f32_16x16x32_bf16 v[60:63], v[200:203], v[148:151], v[60:63]\n"
  "buffer_load_dwordx4 v248, s[20:23], s14 offen offset:3072 sc1 lds\n"
  "v_mfma_f32_16x16x32_bf16 v[96:99], v[200:203], v[152:155], v[96:99]\n"
  "v_mfma_f32_16x16x32_bf16 v[132:135], v[200:203], v[156:159], v[132:135]\n"
  "buffer_load_dwordx4 v[200:203], v248, s[20:23], s45 offen offset:2048 sc1\n"
  "v_mfma_f32_16x16x32_bf16 v[28:31], v[204:207], v[144:147], v[28:31]\n"
  "v_mfma_f32_16x16x32_bf16 v[64:67], v[204:207], v[148:151], v[64:67]\n"
  "v_mfma_f32_16x16x32_bf16 v[100:103], v[204:207], v[152:155], v[100:103]\n"
  "v_mfma_f32_16x16x32_bf16 v[136:139], v[204:207], v[156:159], v[136:139]\n"
  "s_lshl_b32 s12, s81, 13\n"
  "s_add_u32 m0, s12, 4096\n"
  "s_mul_i32 s14, s81, 7168\n"
  "s_add_u32 s14, s14, s72\n"
  "s_add_u32 s14, s14, 104448\n"
  "buffer_load_dwordx4 v248, s[20:23], s14 offen offset:0 sc1 lds\n"
  "buffer_load_dwordx4 v[204:207], v248, s[20:23], s45 offen offset:3072 sc1\n"
  "s_waitcnt vmcnt(26)\n"
  "v_mfma_f32_16x16x32_bf16 v[32:35], v[208:211], v[144:147], v[32:35]\n"
  "s_add_u32 s43, s8, 49152\n"
  "buffer_load_dwordx4 v[144:147], v248, s[20:23], s43 offen sc1\n"
  "v_mfma_f32_16x16x32_bf16 v[68:71], v[208:211], v[148:151], v[68:71]\n"
  "s_add_u32 s43, s9, 49152\n"
  "buffer_load_dwordx4 v[148:151], v248, s[20:23], s43 offen sc1\n"
  "v_mfma_f32_16x16x32_bf16 v[104:107], v[208:211], v[152:155], v[104:107]\n"
  "s_add_u32 s43, s10, 49152\n"
  "buffer_load_dwordx4 v[152:155], v248, s[20:23], s43 offen sc1\n"
  "v_mfma_f32_16x16x32_bf16 v[140:143], v[208:211], v[156:159], v[140:143]\n"
  "s_add_u32 s43, s11, 49152\n"
  "buffer_load_dwordx4 v[156:159], v248, s[20:23], s43 offen sc1\n"
  "s_add_u32 s45, s78, 450560\n"
  "buffer_load_dwordx4 v[208:211], v248, s[20:23], s45 offen offset:0 sc1\n"
  "s_waitcnt vmcnt(22)\n"
  "v_mfma_f32_16x16x32_bf16 v[0:3], v[212:215], v[160:163], v[0:3]\n"
  "buffer_load_dword v251, v250, s[20:23], s80 offen sc1\n"
  "s_waitcnt vmcnt(22)\n"
  "v_mfma_f32_16x16x32_bf16 v[36:39], v[212:215], v[164:167], v[36:39]\n"
  "buffer_load_dwordx4 v248, s[20:23], s14 offen offset:1024 sc1 lds\n"
  "s_waitcnt vmcnt(22)\n"
  "v_mfma_f32_16x16x32_bf16 v[72:75], v[212:215], v[168:171], v[72:75]\n"
  "s_waitcnt vmcnt(21)\n"
  "v_mfma_f32_16x16x32_bf16 v[108:111], v[212:215], v[172:175], v[108:111]\n"
  "s_add_u32 s45, s78, 479232\n"
  "buffer_load_dwordx4 v[212:215], v248, s[20:23], s45 offen offset:0 sc1\n"
  "v_mfma_f32_16x16x32_bf16 v[4:7], v[216:219], v[160:163], v[4:7]\n"
  "v_mfma_f32_16x16x32_bf16 v[40:43], v[216:219], v[164:167], v[40:43]\n"
  "v_mfma_f32_16x16x32_bf16 v[76:79], v[216:219], v[168:171], v[76:79]\n"
  "v_mfma_f32_16x16x32_bf16 v[112:115], v[216:219], v[172:175], v[112:115]\n"
  "buffer_load_dwordx4 v248, s[20:23], s14 offen offset:2048 sc1 lds\n"
  "buffer_load_dwordx4 v[216:219], v248, s[20:23], s45 offen offset:1024 sc1\n"
  "v_mfma_f32_16x16x32_bf16 v[8:11], v[220:223], v[160:163], v[8:11]\n"
  "v_mfma_f32_16x16x32_bf16 v[44:47], v[220:223], v[164:167], v[44:47]\n"
  "v_mfma_f32_16x16x32_bf16 v[80:83], v[220:223], v[168:171], v[80:83]\n"
  "v_mfma_f32_16x16x32_bf16 v[116:119], v[220:223], v[172:175], v[116:119]\n"
  "buffer_load_dwordx4 v[220:223], v248, s[20:23], s45 offen offset:2048 sc1\n"
  "v_mfma_f32_16x16x32_bf16 v[12:15], v[224:227], v[160:163], v[12:15]\n"
  "v_mfma_f32_16x16x32_bf16 v[48:51], v[224:227], v[164:167], v[48:51]\n"
  "buffer_load_dwordx4 v248, s[20:23], s14 offen offset:3072 sc1 lds\n"
  "v_mfma_f32_16x16x32_bf16 v[84:87], v[224:227], v[168:171], v[84:87]\n"
  "v_mfma_f32_16x16x32_bf16 v[120:123], v[224:227], v[172:175], v[120:123]\n"
  "buffer_load_dwordx4 v[224:227], v248, s[20:23], s45 offen offset:3072 sc1\n"
  "v_mfma_f32_16x16x32_bf16 v[16:19], v[228:231], v[160:163], v[16:19]\n"
  "v_mfma_f32_16x16x32_bf16 v[52:55], v[228:231], v[164:167], v[52:55]\n"
  "v_mfma_f32_16x16x32_bf16 v[88:91], v[228:231], v[168:171], v[88:91]\n"
  "v_mfma_f32_16x16x32_bf16 v[124:127], v[228:231], v[172:175], v[124:127]\n"
  "s_and_b32 s12, s68, 1\n"
  "s_lshl_b32 s12, s12, 10\n"
  "s_add_u32 m0, s12, 32768\n"
  "s_and_b32 s12, s68, 6\n"
  "s_lshl_b32 s12, s12, 10\n"
  "s_sub_u32 s14, s72, s12\n"
  "s_add_u32 s14, s14, 137216\n"
  "buffer_load_dwordx4 v248, s[20:23], s14 offen sc1 lds\n"
  "s_add_u32 s45, s78, 483328\n"
  "buffer_load_dwordx4 v[228:231], v248, s[20:23], s45 offen offset:0 sc1\n"
  "v_mfma_f32_16x16x32_bf16 v[20:23], v[232:235], v[160:163], v[20:23]\n"
  "v_mfma_f32_16x16x32_bf16 v[56:59], v[232:235], v[164:167], v[56:59]\n"
  "v_mfma_f32_16x16x32_bf16 v[92:95], v[232:235], v[168:171], v[92:95]\n"
  "v_mfma_f32_16x16x32_bf16 v[128:131], v[232:235], v[172:175], v[128:131]\n"
  "buffer_load_dwordx4 v[232:235], v248, s[20:23], s45 offen offset:1024 sc1\n"
  "v_mfma_f32_16x16x32_bf16 v[24:27], v[236:239], v[160:163], v[24:27]\n"
  "v_mfma_f32_16x16x32_bf16 v[60:63], v[236:239], v[164:167], v[60:63]\n"
  "s_lshl_b32 s12, s68, 8\n"
  "s_add_u32 m0, s12, 34816\n"
  "s_add_u32 s14, s73, 12288\n"
  "buffer_load_dword v254, s[20:23], s14 offen sc1 lds\n"
  "v_mfma_f32_16x16x32_bf16 v[96:99], v[236:239], v[168:171], v[96:99]\n"
  "v_mfma_f32_16x16x32_bf16 v[132:135], v[236:239], v[172:175], v[132:135]\n"
  "buffer_load_dwordx4 v[236:239], v248, s[20:23], s45 offen offset:2048 sc1\n"
  "v_mfma_f32_16x16x32_bf16 v[28:31], v[240:243], v[160:163], v[28:31]\n"
  "v_mfma_f32_16x16x32_bf16 v[64:67], v[240:243], v[164:167], v[64:67]\n"
  "v_mfma_f32_16x16x32_bf16 v[100:103], v[240:243], v[168:171], v[100:103]\n"
  "v_mfma_f32_16x16x32_bf16 v[136:139], v[240:243], v[172:175], v[136:139]\n"
  "s_lshl_b32 s12, s68, 8\n"
  "s_add_u32 m0, s12, 33792\n"
  "s_add_u32 s14, s73, 10240\n"
  "buffer_load_dword v254, s[20:23], s14 offen sc1 lds\n"
  "buffer_load_dwordx4 v[240:243], v248, s[20:23], s45 offen offset:3072 sc1\n"
  "s_waitcnt vmcnt(33)\n"
  "v_mfma_f32_16x16x32_bf16 v[32:35], v[244:247], v[160:163], v[32:35]\n"
  "s_add_u32 s43, s8, 53248\n"
  "buffer_load_dwordx4 v[160:163], v248, s[20:23], s43 offen sc1\n"
  "v_mfma_f32_16x16x32_bf16 v[68:71], v[244:247], v[164:167], v[68:71]\n"
  "s_add_u32 s43, s9, 53248\n"
  "buffer_load_dwordx4 v[164:167], v248, s[20:23], s43 offen sc1\n"
  "v_mfma_f32_16x16x32_bf16 v[104:107], v[244:247], v[168:171], v[104:107]\n"
  "s_add_u32 s43, s10, 53248\n"
  "buffer_load_dwordx4 v[168:171], v248, s[20:23], s43 offen sc1\n"
  "v_mfma_f32_16x16x32_bf16 v[140:143], v[244:247], v[172:175], v[140:143]\n"
  "s_add_u32 s43, s11, 53248\n"
  "buffer_load_dwordx4 v[172:175], v248, s[20:23], s43 offen sc1\n"
  "s_add_u32 s45, s78, 487424\n"
  "buffer_load_dwordx4 v[244:247], v248, s[20:23], s45 offen offset:0 sc1\n"
  "s_waitcnt vmcnt(24)\n"
  "v_mfma_f32_16x16x32_bf16 v[0:3], v[176:179], v[144:147], v[0:3]\n"
  "s_waitcnt vmcnt(23)\n"
  "v_mfma_f32_16x16x32_bf16 v[36:39], v[176:179], v[148:151], v[36:39]\n"
  "s_waitcnt vmcnt(22)\n"
  "v_mfma_f32_16x16x32_bf16 v[72:75], v[176:179], v[152:155], v[72:75]\n"
  "s_waitcnt vmcnt(21)\n"
  "v_mfma_f32_16x16x32_bf16 v[108:111], v[176:179], v[156:159], v[108:111]\n"
  "s_add_u32 s45, s78, 73728\n"
  "buffer_load_dwordx4 v[176:179], v248, s[20:23], s45 offen offset:0 sc1\n"
  "v_mfma_f32_16x16x32_bf16 v[4:7], v[180:183], v[144:147], v[4:7]\n"
  "v_mfma_f32_16x16x32_bf16 v[40:43], v[180:183], v[148:151], v[40:43]\n"
  "v_mfma_f32_16x16x32_bf16 v[76:79], v[180:183], v[152:155], v[76:79]\n"
  "v_mfma_f32_16x16x32_bf16 v[112:115], v[180:183], v[156:159], v[112:115]\n"
  "buffer_load_dwordx4 v[180:183], v248, s[20:23], s45 offen offset:1024 sc1\n"
  "v_mfma_f32_16x16x32_bf16 v[8:11], v[184:187], v[144:147], v[8:11]\n"
  "v_mfma_f32_16x16x32_bf16 v[44:47], v[184:187], v[148:151], v[44:47]\n"
  "v_mfma_f32_16x16x32_bf16 v[80:83], v[184:187], v[152:155], v[80:83]\n"
  "v_mfma_f32_16x16x32_bf16 v[116:119], v[184:187], v[156:159], v[116:119]\n"
  "buffer_load_dwordx4 v[184:187], v248, s[20:23], s45 offen offset:2048 sc1\n"
  "v_mfma_f32_16x16x32_bf16 v[12:15], v[188:191], v[144:147], v[12:15]\n"
  "v_mfma_f32_16x16x32_bf16 v[48:51], v[188:191], v[148:151], v[48:51]\n"
  "v_mfma_f32_16x16x32_bf16 v[84:87], v[188:191], v[152:155], v[84:87]\n"
  "v_mfma_f32_16x16x32_bf16 v[120:123], v[188:191], v[156:159], v[120:123]\n"
  "buffer_load_dwordx4 v[188:191], v248, s[20:23], s45 offen offset:3072 sc1\n"
  "v_mfma_f32_16x16x32_bf16 v[16:19], v[192:195], v[144:147], v[16:19]\n"
  "v_mfma_f32_16x16x32_bf16 v[52:55], v[192:195], v[148:151], v[52:55]\n"
  "v_mfma_f32_16x16x32_bf16 v[88:91], v[192:195], v[152:155], v[88:91]\n"
  "v_mfma_f32_16x16x32_bf16 v[124:127], v[192:195], v[156:159], v[124:127]\n"
  "s_add_u32 s45, s78, 77824\n"
  "buffer_load_dwordx4 v[192:195], v248, s[20:23], s45 offen offset:0 sc1\n"
  "v_mfma_f32_16x16x32_bf16 v[20:23], v[196:199], v[144:147], v[20:23]\n"
  "v_mfma_f32_16x16x32_bf16 v[56:59], v[196:199], v[148:151], v[56:59]\n"
  "v_mfma_f32_16x16x32_bf16 v[92:95], v[196:199], v[152:155], v[92:95]\n"
  "v_mfma_f32_16x16x32_bf16 v[128:131], v[196:199], v[156:159], v[128:131]\n"
  "buffer_load_dwordx4 v[196:199], v248, s[20:23], s45 offen offset:1024 sc1\n"
  "v_mfma_f32_16x16x32_bf16 v[24:27], v[200:203], v[144:147], v[24:27]\n"
  "v_mfma_f32_16x16x32_bf16 v[60:63], v[200:203], v[148:151], v[60:63]\n"
  "v_mfma_f32_16x16x32_bf16 v[96:99], v[200:203], v[152:155], v[96:99]\n"
  "v_mfma_f32_16x16x32_bf16 v[132:135], v[200:203], v[156:159], v[132:135]\n"
  "buffer_load_dwordx4 v[200:203], v248, s[20:23], s45 offen offset:2048 sc1\n"
  "v_mfma_f32_16x16x32_bf16 v[28:31], v[204:207], v[144:147], v[28:31]\n"
  "v_mfma_f32_16x16x32_bf16 v[64:67], v[204:207], v[148:151], v[64:67]\n"
  "v_mfma_f32_16x16x32_bf16 v[100:103], v[204:207], v[152:155], v[100:103]\n"
  "v_mfma_f32_16x16x32_bf16 v[136:139], v[204:207], v[156:159], v[136:139]\n"
  "s_waitcnt vmcnt(26)\n"
  "v_cmp_ne_u32 vcc, s47, v251\n"
  "s_cbranch_vccnz LSB_%=\n"
  "LSBB_%=:\n"
  "buffer_load_dwordx4 v[204:207], v248, s[20:23], s45 offen offset:3072 sc1\n"
  "v_mfma_f32_16x16x32_bf16 v[32:35], v[208:211], v[144:147], v[32:35]\n"
  "s_add_u32 s43, s8, 8192\n"
  "buffer_load_dwordx4 v[144:147], v248, s[20:23], s43 offen sc1\n"
  "v_mfma_f32_16x16x32_bf16 v[68:71], v[208:211], v[148:151], v[68:71]\n"
  "s_add_u32 s43, s9, 8192\n"
  "buffer_load_dwordx4 v[148:151], v248, s[20:23], s43 offen sc1\n"
  "v_mfma_f32_16x16x32_bf16 v[104:107], v[208:211], v[152:155], v[104:107]\n"
  "s_add_u32 s43, s10, 8192\n"
  "buffer_load_dwordx4 v[152:155], v248, s[20:23], s43 offen sc1\n"
  "v_mfma_f32_16x16x32_bf16 v[140:143], v[208:211], v[156:159], v[140:143]\n"
  "s_add_u32 s43, s11, 8192\n"
  "buffer_load_dwordx4 v[156:159], v248, s[20:23], s43 offen sc1\n"
  "s_add_u32 s45, s78, 81920\n"
  "buffer_load_dwordx4 v[208:211], v248, s[20:23], s45 offen offset:0 sc1\n"
  "s_waitcnt vmcnt(17)\n"
  "v_mfma_f32_16x16x32_bf16 v[0:3], v[212:215], v[160:163], v[0:3]\n"
  "s_waitcnt vmcnt(16)\n"
  "v_mfma_f32_16x16x32_bf16 v[36:39], v[212:215], v[164:167], v[36:39]\n"
  "s_waitcnt vmcnt(15)\n"
  "v_mfma_f32_16x16x32_bf16 v[72:75], v[212:215], v[168:171], v[72:75]\n"
  "s_waitcnt vmcnt(14)\n"
  "v_mfma_f32_16x16x32_bf16 v[108:111], v[212:215], v[172:175], v[108:111]\n"
  "s_add_u32 s45, s78, 110592\n"
  "buffer_load_dwordx4 v[212:215], v248, s[20:23], s45 offen offset:0 sc1\n"
  "v_mfma_f32_16x16x32_bf16 v[4:7], v[216:219], v[160:163], v[4:7]\n"
  "v_mfma_f32_16x16x32_bf16 v[40:43], v[216:219], v[164:167], v[40:43]\n"
  "v_mfma_f32_16x16x32_bf16 v[76:79], v[216:219], v[168:171], v[76:79]\n"
  "v_mfma_f32_16x16x32_bf16 v[112:115], v[216:219], v[172:175], v[112:115]\n"
  "buffer_load_dwordx4 v[216:219], v248, s[20:23], s45 offen offset:1024 sc1\n"
  "v_mfma_f32_16x16x32_bf16 v[8:11], v[220:223], v[160:163], v[8:11]\n"
  "v_mfma_f32_16x16x32_bf16 v[44:47], v[220:223], v[164:167], v[44:47]\n"
  "v_mfma_f32_16x16x32_bf16 v[80:83], v[220:223], v[168:171], v[80:83]\n"
  "v_mfma_f32_16x16x32_bf16 v[116:119], v[220:223], v[172:175], v[116:119]\n"
  "buffer_load_dwordx4 v[220:223], v248, s[20:23], s45 offen offset:2048 sc1\n"
  "v_mfma_f32_16x16x32_bf16 v[12:15], v[224:227], v[160:163], v[12:15]\n"
  "v_mfma_f32_16x16x32_bf16 v[48:51], v[224:227], v[164:167], v[48:51]\n"
  "v_mfma_f32_16x16x32_bf16 v[84:87], v[224:227], v[168:171], v[84:87]\n"
  "v_mfma_f32_16x16x32_bf16 v[120:123], v[224:227], v[172:175], v[120:123]\n"
  "buffer_load_dwordx4 v[224:227], v248, s[20:23], s45 offen offset:3072 sc1\n"
  "v_mfma_f32_16x16x32_bf16 v[16:19], v[228:231], v[160:163], v[16:19]\n"
  "v_mfma_f32_16x16x32_bf16 v[52:55], v[228:231], v[164:167], v[52:55]\n"
  "v_mfma_f32_16x16x32_bf16 v[88:91], v[228:231], v[168:171], v[88:91]\n"
  "v_mfma_f32_16x16x32_bf16 v[124:127], v[228:231], v[172:175], v[124:127]\n"
  "s_add_u32 s45, s78, 114688\n"
  "buffer_load_dwordx4 v[228:231], v248, s[20:23], s45 offen offset:0 sc1\n"
  "v_mfma_f32_16x16x32_bf16 v[20:23], v[232:235], v[160:163], v[20:23]\n"
  "v_mfma_f32_16x16x32_bf16 v[56:59], v[232:235], v[164:167], v[56:59]\n"
  "v_mfma_f32_16x16x32_bf16 v[92:95], v[232:235], v[168:171], v[92:95]\n"
  "v_mfma_f32_16x16x32_bf16 v[128:131], v[232:235], v[172:175], v[128:131]\n"
  "buffer_load_dwordx4 v[232:235], v248, s[20:23], s45 offen offset:1024 sc1\n"
  "v_mfma_f32_16x16x32_bf16 v[24:27], v[236:239], v[160:163], v[24:27]\n"
  "v_mfma_f32_16x16x32_bf16 v[60:63], v[236:239], v[164:167], v[60:63]\n"
  "v_mfma_f32_16x16x32_bf16 v[96:99], v[236:239], v[168:171], v[96:99]\n"
  "v_mfma_f32_16x16x32_bf16 v[132:135], v[236:239], v[172:175], v[132:135]\n"
  "buffer_load_dwordx4 v[236:239], v248, s[20:23], s45 offen offset:2048 sc1\n"
  "v_mfma_f32_16x16x32_bf16 v[28:31], v[240:243], v[160:163], v[28:31]\n"
  "v_mov_b32 v249, 7\n"
  "ds_write_b32 v253, v249\n"
  "v_mfma_f32_16x16x32_bf16 v[64:67], v[240:243], v[164:167], v[64:67]\n"
  "v_mfma_f32_16x16x32_bf16 v[100:103], v[240:243], v[168:171], v[100:103]\n"
  "v_mfma_f32_16x16x32_bf16 v[136:139], v[240:243], v[172:175], v[136:139]\n"
  "buffer_load_dwordx4 v[240:243], v248, s[20:23], s45 offen offset:3072 sc1\n"
  "s_waitcnt vmcnt(21)\n"
  "v_mfma_f32_16x16x32_bf16 v[32:35], v[244:247], v[160:163], v[32:35]\n"
  "s_add_u32 s43, s8, 12288\n"
  "buffer_load_dwordx4 v[160:163], v248, s[20:23], s43 offen sc1\n"
  "v_mfma_f32_16x16x32_bf16 v[68:71], v[244:247], v[164:167], v[68:71]\n"
  "s_add_u32 s43, s9, 12288\n"
  "buffer_load_dwordx4 v[164:167], v248, s[20:23], s43 offen sc1\n"
  "v_mfma_f32_16x16x32_bf16 v[104:107], v[244:247], v[168:171], v[104:107]\n"
  "s_add_u32 s43, s10, 12288\n"
  "buffer_load_dwordx4 v[168:171], v248, s[20:23], s43 offen sc1\n"
  "v_mfma_f32_16x16x32_bf16 v[140:143], v[244:247], v[172:175], v[140:143]\n"
  "s_add_u32 s43, s11, 12288\n"
  "buffer_load_dwordx4 v[172:175], v248, s[20:23], s43 offen sc1\n"
  "s_add_u32 s45, s78, 118784\n"
  "buffer_load_dwordx4 v[244:247], v248, s[20:23], s45 offen offset:0 sc1\n"
  "s_waitcnt vmcnt(17)\n"
  "v_mfma_f32_16x16x32_bf16 v[0:3], v[176:179], v[144:147], v[0:3]\n"
  "s_waitcnt vmcnt(16)\n"
  "v_mfma_f32_16x16x32_bf16 v[36:39], v[176:179], v[148:151], v[36:39]\n"
  "s_waitcnt vmcnt(15)\n"
  "v_mfma_f32_16x16x32_bf16 v[72:75], v[176:179], v[152:155], v[72:75]\n"
  "s_waitcnt vmcnt(14)\n"
  "v_mfma_f32_16x16x32_bf16 v[108:111], v[176:179], v[156:159], v[108:111]\n"
  "s_add_u32 s45, s78, 221184\n"
  "buffer_load_dwordx4 v[176:179], v248, s[20:23], s45 offen offset:0 sc1\n"
  "v_mfma_f32_16x16x32_bf16 v[4:7], v[180:183], v[144:147], v[4:7]\n"
  "v_mfma_f32_16x16x32_bf16 v[40:43], v[180:183], v[148:151], v[40:43]\n"
  "v_mfma_f32_16x16x32_bf16 v[76:79], v[180:183], v[152:155], v[76:79]\n"
  "v_mfma_f32_16x16x32_bf16 v[112:115], v[180:183], v[156:159], v[112:115]\n"
  "buffer_load_dwordx4 v[180:183], v248, s[20:23], s45 offen offset:1024 sc1\n"
  "v_mfma_f32_16x16x32_bf16 v[8:11], v[184:187], v[144:147], v[8:11]\n"
  "v_mfma_f32_16x16x32_bf16 v[44:47], v[184:187], v[148:151], v[44:47]\n"
  "v_mfma_f32_16x16x32_bf16 v[80:83], v[184:187], v[152:155], v[80:83]\n"
  "v_mfma_f32_16x16x32_bf16 v[116:119], v[184:187], v[156:159], v[116:119]\n"
  "buffer_load_dwordx4 v[184:187], v248, s[20:23], s45 offen offset:2048 sc1\n"
  "v_mfma_f32_16x16x32_bf16 v[12:15], v[188:191], v[144:147], v[12:15]\n"
  "s_cmp_lt_u32 s79, 80\n"
  "s_cbranch_scc1 LGP300_%=\n"
  "s_setprio 2\n"
  "LGP300_%=:\n"
  "v_mfma_f32_16x16x32_bf16 v[48:51], v[188:191], v[148:151], v[48:51]\n"
  "v_mfma_f32_16x16x32_bf16 v[84:87], v[188:191], v[152:155], v[84:87]\n"
  "v_mfma_f32_16x16x32_bf16 v[120:123], v[188:191], v[156:159], v[120:123]\n"
  "buffer_load_dwordx4 v[188:191], v248, s[20:23], s45 offen offset:3072 sc1\n"
  "v_mfma_f32_16x16x32_bf16 v[16:19], v[192:195], v[144:147], v[16:19]\n"
  "v_mfma_f32_16x16x32_bf16 v[52:55], v[192:195], v[148:151], v[52:55]\n"
  "v_mfma_f32_16x16x32_bf16 v[88:91], v[192:195], v[152:155], v[88:91]\n"
  "v_mfma_f32_16x16x32_bf16 v[124:127], v[192:195], v[156:159], v[124:127]\n"
  "s_add_u32 s45, s78, 225280\n"
  "buffer_load_dwordx4 v[192:195], v248, s[20:23], s45 offen offset:0 sc1\n"
  "v_mfma_f32_16x16x32_bf16 v[20:23], v[196:199], v[144:147], v[20:23]\n"
  "v_mfma_f32_16x16x32_bf16 v[56:59], v[196:199], v[148:151], v[56:59]\n"
  "v_mfma_f32_16x16x32_bf16 v[92:95], v[196:199], v[152:155], v[92:95]\n"
  "v_mfma_f32_16x16x32_bf16 v[128:131], v[196:199], v[156:159], v[128:131]\n"
  "buffer_load_dwordx4 v[196:199], v248, s[20:23], s45 offen offset:1024 sc1\n"
  "v_mfma_f32_16x16x32_bf16 v[24:27], v[200:203], v[144:147], v[24:27]\n"
  "v_mfma_f32_16x16x32_bf16 v[60:63], v[200:203], v[148:151], v[60:63]\n"
  "v_mfma_f32_16x16x32_bf16 v[96:99], v[200:203], v[152:155], v[96:99]\n"
  "v_mfma_f32_16x16x32_bf16 v[132:135], v[200:203], v[156:159], v[132:135]\n"
  "buffer_load_dwordx4 v[200:203], v248, s[20:23], s45 offen offset:2048 sc1\n"
  "v_mfma_f32_16x16x32_bf16 v[28:31], v[204:207], v[144:147], v[28:31]\n"
  "v_mfma_f32_16x16x32_bf16 v[64:67], v[204:207], v[148:151], v[64:67]\n"
  "v_mfma_f32_16x16x32_bf16 v[100:103], v[204:207], v[152:155], v[100:103]\n"
  "v_mfma_f32_16x16x32_bf16 v[136:139], v[204:207], v[156:159], v[136:139]\n"
  "buffer_load_dwordx4 v[204:207], v248, s[20:23], s45 offen offset:3072 sc1\n"
  "s_waitcnt vmcnt(21)\n"
  "v_mfma_f32_16x16x32_bf16 v[32:35], v[208:211], v[144:147], v[32:35]\n"
  "s_add_u32 s43, s8, 24576\n"
  "buffer_load_dwordx4 v[144:147], v248, s[20:23], s43 offen sc1\n"
  "v_mfma_f32_16x16x32_bf16 v[68:71], v[208:211], v[148:151], v[68:71]\n"
  "s_add_u32 s43, s9, 24576\n"
  "buffer_load_dwordx4 v[148:151], v248, s[20:23], s43 offen sc1\n"
  "v_mfma_f32_16x16x32_bf16 v[104:107], v[208:211], v[152:155], v[104:107]\n"
  "s_add_u32 s43, s10, 24576\n"
  "buffer_load_dwordx4 v[152:155], v248, s[20:23], s43 offen sc1\n"
  "v_mfma_f32_16x16x32_bf16 v[140:143], v[208:211], v[156:159], v[140:143]\n"
  "s_add_u32 s43, s11, 24576\n"
  "buffer_load_dwordx4 v[156:159], v248, s[20:23], s43 offen sc1\n"
  "s_add_u32 s45, s78, 229376\n"
  "buffer_load_dwordx4 v[208:211], v248, s[20:23], s45 offen offset:0 sc1\n"
  "s_waitcnt vmcnt(17)\n"
  "v_mfma_f32_16x16x32_bf16 v[0:3], v[212:215], v[160:163], v[0:3]\n"
  "s_waitcnt vmcnt(16)\n"
  "v_mfma_f32_16x16x32_bf16 v[36:39], v[212:215], v[164:167], v[36:39]\n"
  "s_waitcnt vmcnt(15)\n"
  "v_mfma_f32_16x16x32_bf16 v[72:75], v[212:215], v[168:171], v[72:75]\n"
  "s_waitcnt vmcnt(14)\n"
  "v_mfma_f32_16x16x32_bf16 v[108:111], v[212:215], v[172:175], v[108:111]\n"
  "s_add_u32 s45, s78, 258048\n"
  "buffer_load_dwordx4 v[212:215], v248, s[20:23], s45 offen offset:0 sc1\n"
  "v_mfma_f32_16x16x32_bf16 v[4:7], v[216:219], v[160:163], v[4:7]\n"
  "v_mfma_f32_16x16x32_bf16 v[40:43], v[216:219], v[164:167], v[40:43]\n"
  "v_mfma_f32_16x16x32_bf16 v[76:79], v[216:219], v[168:171], v[76:79]\n"
  "v_mfma_f32_16x16x32_bf16 v[112:115], v[216:219], v[172:175], v[112:115]\n"
  "buffer_load_dwordx4 v[216:219], v248, s[20:23], s45 offen offset:1024 sc1\n"
  "v_mfma_f32_16x16x32_bf16 v[8:11], v[220:223], v[160:163], v[8:11]\n"
  "v_mfma_f32_16x16x32_bf16 v[44:47], v[220:223], v[164:167], v[44:47]\n"
  "v_mfma_f32_16x16x32_bf16 v[80:83], v[220:223], v[168:171], v[80:83]\n"
  "v_mfma_f32_16x16x32_bf16 v[116:119], v[220:223], v[172:175], v[116:119]\n"
  "buffer_load_dwordx4 v[220:223], v248, s[20:23], s45 offen offset:2048 sc1\n"
  "v_mfma_f32_16x16x32_bf16 v[12:15], v[224:227], v[160:163], v[12:15]\n"
  "v_mfma_f32_16x16x32_bf16 v[48:51], v[224:227], v[164:167], v[48:51]\n"
  "v_mfma_f32_16x16x32_bf16 v[84:87], v[224:227], v[168:171], v[84:87]\n"
  "v_mfma_f32_16x16x32_bf16 v[120:123], v[224:227], v[172:175], v[120:123]\n"
  "buffer_load_dwordx4 v[224:227], v248, s[20:23], s45 offen offset:3072 sc1\n"
  "v_mfma_f32_16x16x32_bf16 v[16:19], v[228:231], v[160:163], v[16:19]\n"
  "v_mfma_f32_16x16x32_bf16 v[52:55], v[228:231], v[164:167], v[52:55]\n"
  "v_mfma_f32_16x16x32_bf16 v[88:91], v[228:231], v[168:171], v[88:91]\n"
  "v_mfma_f32_16x16x32_bf16 v[124:127], v[228:231], v[172:175], v[124:127]\n"
  "s_add_u32 s45, s78, 262144\n"
  "buffer_load_dwordx4 v[228:231], v248, s[20:23], s45 offen offset:0 sc1\n"
  "v_mfma_f32_16x16x32_bf16 v[20:23], v[232:235], v[160:163], v[20:23]\n"
  "v_mfma_f32_16x16x32_bf16 v[56:59], v[232:235], v[164:167], v[56:59]\n"
  "v_mfma_f32_16x16x32_bf16 v[92:95], v[232:235], v[168:171], v[92:95]\n"
  "v_mfma_f32_16x16x32_bf16 v[128:131], v[232:235], v[172:175], v[128:131]\n"
  "buffer_load_dwordx4 v[232:235], v248, s[20:23], s45 offen offset:1024 sc1\n"
  "v_mfma_f32_16x16x32_bf16 v[24:27], v[236:239], v[160:163], v[24:27]\n"
  "v_mfma_f32_16x16x32_bf16 v[60:63], v[236:239], v[164:167], v[60:63]\n"
  "v_mfma_f32_16x16x32_bf16 v[96:99], v[236:239], v[168:171], v[96:99]\n"
  "v_mfma_f32_16x16x32_bf16 v[132:135], v[236:239], v[172:175], v[132:135]\n"
  "buffer_load_dwordx4 v[236:239], v248, s[20:23], s45 offen offset:2048 sc1\n"
  "v_mfma_f32_16x16x32_bf16 v[28:31], v[240:243], v[160:163], v[28:31]\n"
  "v_mfma_f32_16x16x32_bf16 v[64:67], v[240:243], v[164:167], v[64:67]\n"
  "v_mfma_f32_16x16x32_bf16 v[100:103], v[240:243], v[168:171], v[100:103]\n"
  "v_mfma_f32_16x16x32_bf16 v[136:139], v[240:243], v[172:175], v[136:139]\n"
  "buffer_load_dwordx4 v[240:243], v248, s[20:23], s45 offen offset:3072 sc1\n"
  "s_waitcnt vmcnt(21)\n"
  "v_mfma_f32_16x16x32_bf16 v[32:35], v[244:247], v[160:163], v[32:35]\n"
  "s_add_u32 s43, s8, 28672\n"
  "buffer_load_dwordx4 v[160:163], v248, s[20:23], s43 offen sc1\n"
  "v_mfma_f32_16x16x32_bf16 v[68:71], v[244:247], v[164:167], v[68:71]\n"
  "s_add_u32 s43, s9, 28672\n"
  "buffer_load_dwordx4 v[164:167], v248, s[20:23], s43 offen sc1\n"
  "v_mfma_f32_16x16x32_bf16 v[104:107], v[244:247], v[168:171], v[104:107]\n"
  "s_add_u32 s43, s10, 28672\n"
  "buffer_load_dwordx4 v[168:171], v248, s[20:23], s43 offen sc1\n"
  "v_mfma_f32_16x16x32_bf16 v[140:143], v[244:247], v[172:175], v[140:143]\n"
  "s_add_u32 s43, s11, 28672\n"
  "buffer_load_dwordx4 v[172:175], v248, s[20:23], s43 offen sc1\n"
  "s_add_u32 s45, s78, 266240\n"
  "buffer_load_dwordx4 v[244:247], v248, s[20:23], s45 offen offset:0 sc1\n"
  "s_waitcnt vmcnt(17)\n"
  "v_mfma_f32_16x16x32_bf16 v[0:3], v[176:179], v[144:147], v[0:3]\n"
  "s_waitcnt vmcnt(16)\n"
  "v_mfma_f32_16x16x32_bf16 v[36:39], v[176:179], v[148:151], v[36:39]\n"
  "s_waitcnt vmcnt(15)\n"
  "v_mfma_f32_16x16x32_bf16 v[72:75], v[176:179], v[152:155], v[72:75]\n"
  "s_waitcnt vmcnt(14)\n"
  "v_mfma_f32_16x16x32_bf16 v[108:111], v[176:179], v[156:159], v[108:111]\n"
  "s_add_u32 s45, s78, 368640\n"
  "buffer_load_dwordx4 v[176:179], v248, s[20:23], s45 offen offset:0 sc1\n"
  "v_mfma_f32_16x16x32_bf16 v[4:7], v[180:183], v[144:147], v[4:7]\n"
  "v_mfma_f32_16x16x32_bf16 v[40:43], v[180:183], v[148:151], v[40:43]\n"
  "v_mfma_f32_16x16x32_bf16 v[76:79], v[180:183], v[152:155], v[76:79]\n"
  "v_mfma_f32_16x16x32_bf16 v[112:115], v[180:183], v[156:159], v[112:115]\n"
  "buffer_load_dwordx4 v[180:183], v248, s[20:23], s45 offen offset:1024 sc1\n"
  "v_mfma_f32_16x16x32_bf16 v[8:11], v[184:187], v[144:147], v[8:11]\n"
  "v_mfma_f32_16x16x32_bf16 v[44:47], v[184:187], v[148:151], v[44:47]\n"
  "v_mfma_f32_16x16x32_bf16 v[80:83], v[184:187], v[152:155], v[80:83]\n"
  "v_mfma_f32_16x16x32_bf16 v[116:119], v[184:187], v[156:159], v[116:119]\n"
  "buffer_load_dwordx4 v[184:187], v248, s[20:23], s45 offen offset:2048 sc1\n"
  "v_mfma_f32_16x16x32_bf16 v[12:15], v[188:191], v[144:147], v[12:15]\n"
  "v_mfma_f32_16x16x32_bf16 v[48:51], v[188:191], v[148:151], v[48:51]\n"
  "v_mfma_f32_16x16x32_bf16 v[84:87], v[188:191], v[152:155], v[84:87]\n"
  "v_mfma_f32_16x16x32_bf16 v[120:123], v[188:191], v[156:159], v[120:123]\n"
  "buffer_load_dwordx4 v[188:191], v248, s[20:23], s45 offen offset:3072 sc1\n"
  "v_mfma_f32_16x16x32_bf16 v[16:19], v[192:195], v[144:147], v[16:19]\n"
  "v_mfma_f32_16x16x32_bf16 v[52:55], v[192:195], v[148:151], v[52:55]\n"
  "v_mfma_f32_16x16x32_bf16 v[88:91], v[192:195], v[152:155], v[88:91]\n"
  "v_mfma_f32_16x16x32_bf16 v[124:127], v[192:195], v[156:159], v[124:127]\n"
  "s_add_u32 s45, s78, 372736\n"
  "buffer_load_dwordx4 v[192:195], v248, s[20:23], s45 offen offset:0 sc1\n"
  "v_mfma_f32_16x16x32_bf16 v[20:23], v[196:199], v[144:147], v[20:23]\n"
  "v_mfma_f32_16x16x32_bf16 v[56:59], v[196:199], v[148:151], v[56:59]\n"
  "v_mfma_f32_16x16x32_bf16 v[92:95], v[196:199], v[152:155], v[92:95]\n"
  "v_mfma_f32_16x16x32_bf16 v[128:131], v[196:199], v[156:159], v[128:131]\n"
  "buffer_load_dwordx4 v[196:199], v248, s[20:23], s45 offen offset:1024 sc1\n"
  "v_mfma_f32_16x16x32_bf16 v[24:27], v[200:203], v[144:147], v[24:27]\n"
  "v_mfma_f32_16x16x32_bf16 v[60:63], v[200:203], v[148:151], v[60:63]\n"
  "v_mfma_f32_16x16x32_bf16 v[96:99], v[200:203], v[152:155], v[96:99]\n"
  "v_mfma_f32_16x16x32_bf16 v[132:135], v[200:203], v[156:159], v[132:135]\n"
  "buffer_load_dwordx4 v[200:203], v248, s[20:23], s45 offen offset:2048 sc1\n"
  "v_mfma_f32_16x16x32_bf16 v[28:31], v[204:207], v[144:147], v[28:31]\n"
  "v_mfma_f32_16x16x32_bf16 v[64:67], v[204:207], v[148:151], v[64:67]\n"
  "v_mfma_f32_16x16x32_bf16 v[100:103], v[204:207], v[152:155], v[100:103]\n"
  "v_mfma_f32_16x16x32_bf16 v[136:139], v[204:207], v[156:159], v[136:139]\n"
  "buffer_load_dwordx4 v[204:207], v248, s[20:23], s45 offen offset:3072 sc1\n"
  "s_waitcnt vmcnt(21)\n"
  "v_mfma_f32_16x16x32_bf16 v[32:35], v[208:211], v[144:147], v[32:35]\n"
  "s_add_u32 s43, s8, 40960\n"
  "buffer_load_dwordx4 v[144:147], v248, s[20:23], s43 offen sc1\n"
  "v_mfma_f32_16x16x32_bf16 v[68:71], v[208:211], v[148:151], v[68:71]\n"
  "s_add_u32 s43, s9, 40960\n"
  "buffer_load_dwordx4 v[148:151], v248, s[20:23], s43 offen sc1\n"
  "v_mfma_f32_16x16x32_bf16 v[104:107], v[208:211], v[152:155], v[104:107]\n"
  "s_add_u32 s43, s10, 40960\n"
  "buffer_load_dwordx4 v[152:155], v248, s[20:23], s43 offen sc1\n"
  "ds_read_b32 v249, v252 offset:32\n"
  "v_mfma_f32_16x16x32_bf16 v[140:143], v[208:211], v[156:159], v[140:143]\n"
  "s_add_u32 s43, s11, 40960\n"
  "buffer_load_dwordx4 v[156:159], v248, s[20:23], s43 offen sc1\n"
  "s_add_u32 s45, s78, 376832\n"
  "buffer_load_dwordx4 v[208:211], v248, s[20:23], s45 offen offset:0 sc1\n"
  "s_waitcnt vmcnt(17)\n"
  "v_mfma_f32_16x16x32_bf16 v[0:3], v[212:215], v[160:163], v[0:3]\n"
  "s_waitcnt vmcnt(16)\n"
  "v_mfma_f32_16x16x32_bf16 v[36:39], v[212:215], v[164:167], v[36:39]\n"
  "s_waitcnt vmcnt(15)\n"
  "v_mfma_f32_16x16x32_bf16 v[72:75], v[212:215], v[168:171], v[72:75]\n"
  "s_waitcnt vmcnt(14)\n"
  "v_mfma_f32_16x16x32_bf16 v[108:111], v[212:215], v[172:175], v[108:111]\n"
  "s_add_u32 s45, s78, 405504\n"
  "buffer_load_dwordx4 v[212:215], v248, s[20:23], s45 offen offset:0 sc1\n"
  "v_mfma_f32_16x16x32_bf16 v[4:7], v[216:219], v[160:163], v[4:7]\n"
  "s_waitcnt lgkmcnt(0)\n"
  "v_cmp_gt_u32 vcc, 2, v249\n"
  "s_and_b64 vcc, vcc, 15\n"
  "s_cbranch_vccnz LS9_%=\n"
  "LS9B_%=:\n"
  "v_mfma_f32_16x16x32_bf16 v[40:43], v[216:219], v[164:167], v[40:43]\n"
  "s_bitcmp1_b32 s88, 10\n"
  "s_cselect_b32 s12, 0, 9216\n"
  "s_add_u32 s12, s12, s86\n"
  "s_lshl2_add_u32 s14, s81, 0\n"
  "s_min_u32 s14, s14, 14\n"
  "s_lshl_b32 m0, s14, 10\n"
  "s_add_u32 m0, m0, 147456\n"
  "s_mul_i32 s14, s14, 36864\n"
  "s_add_u32 s12, s12, s14\n"
  "s_lshl_b32 s14, s81, 11\n"
  "s_sub_u32 s12, s12, s14\n"
  "s_sub_u32 s12, s12, 1024\n"
  "buffer_load_dwordx4 v248, s[20:23], s12 offen sc1 lds\n"
  "v_mfma_f32_16x16x32_bf16 v[76:79], v[216:219], v[168:171], v[76:79]\n"
  "v_mfma_f32_16x16x32_bf16 v[112:115], v[216:219], v[172:175], v[112:115]\n"
  "buffer_load_dwordx4 v[216:219], v248, s[20:23], s45 offen offset:1024 sc1\n"
  "v_mfma_f32_16x16x32_bf16 v[8:11], v[220:223], v[160:163], v[8:11]\n"
  "v_mfma_f32_16x16x32_bf16 v[44:47], v[220:223], v[164:167], v[44:47]\n"
  "v_mfma_f32_16x16x32_bf16 v[80:83], v[220:223], v[168:171], v[80:83]\n"
  "v_mfma_f32_16x16x32_bf16 v[116:119], v[220:223], v[172:175], v[116:119]\n"
  "s_bitcmp1_b32 s88, 10\n"
  "s_cselect_b32 s12, 0, 9216\n"
  "s_add_u32 s12, s12, s86\n"
  "s_lshl2_add_u32 s14, s81, 1\n"
  "s_min_u32 s14, s14, 14\n"
  "s_lshl_b32 m0, s14, 10\n"
  "s_add_u32 m0, m0, 147456\n"
  "s_mul_i32 s14, s14, 36864\n"
  "s_add_u32 s12, s12, s14\n"
  "s_lshl_b32 s14, s81, 11\n"
  "s_sub_u32 s12, s12, s14\n"
  "s_sub_u32 s12, s12, 1024\n"
  "buffer_load_dwordx4 v248, s[20:23], s12 offen sc1 lds\n"
  "buffer_load_dwordx4 v[220:223], v248, s[20:23], s45 offen offset:2048 sc1\n"
  "v_mfma_f32_16x16x32_bf16 v[12:15], v[224:227], v[160:163], v[12:15]\n"
  "v_mfma_f32_16x16x32_bf16 v[48:51], v[224:227], v[164:167], v[48:51]\n"
  "v_mfma_f32_16x16x32_bf16 v[84:87], v[224:227], v[168:171], v[84:87]\n"
  "v_mfma_f32_16x16x32_bf16 v[120:123], v[224:227], v[172:175], v[120:123]\n"
  "buffer_load_dwordx4 v[224:227], v248, s[20:23], s45 offen offset:3072 sc1\n"
  "v_mfma_f32_16x16x32_bf16 v[16:19], v[228:231], v[160:163], v[16:19]\n"
  "v_mfma_f32_16x16x32_bf16 v[52:55], v[228:231], v[164:167], v[52:55]\n"
  "s_bitcmp1_b32 s88, 10\n"
  "s_cselect_b32 s12, 0, 9216\n"
  "s_add_u32 s12, s12, s86\n"
  "s_lshl2_add_u32 s14, s81, 2\n"
  "s_min_u32 s14, s14, 14\n"
  "s_lshl_b32 m0, s14, 10\n"
  "s_add_u32 m0, m0, 147456\n"
  "s_mul_i32 s14, s14, 36864\n"
  "s_add_u32 s12, s12, s14\n"
  "s_lshl_b32 s14, s81, 11\n"
  "s_sub_u32 s12, s12, s14\n"
  "s_sub_u32 s12, s12, 1024\n"
  "buffer_load_dwordx4 v248, s[20:23], s12 offen sc1 lds\n"
  "v_mfma_f32_16x16x32_bf16 v[88:91], v[228:231], v[168:171], v[88:91]\n"
  "v_mfma_f32_16x16x32_bf16 v[124:127], v[228:231], v[172:175], v[124:127]\n"
  "s_add_u32 s45, s78, 409600\n"
  "buffer_load_dwordx4 v[228:231], v248, s[20:23], s45 offen offset:0 sc1\n"
  "v_mfma_f32_16x16x32_bf16 v[20:23], v[232:235], v[160:163], v[20:23]\n"
  "v_mfma_f32_16x16x32_bf16 v[56:59], v[232:235], v[164:167], v[56:59]\n"
  "v_mfma_f32_16x16x32_bf16 v[92:95], v[232:235], v[168:171], v[92:95]\n"
  "v_mfma_f32_16x16x32_bf16 v[128:131], v[232:235], v[172:175], v[128:131]\n"
  "s_bitcmp1_b32 s88, 10\n"
  "s_cselect_b32 s12, 0, 9216\n"
  "s_add_u32 s12, s12, s86\n"
  "s_lshl2_add_u32 s14, s81, 3\n"
  "s_min_u32 s14, s14, 14\n"
  "s_lshl_b32 m0, s14, 10\n"
  "s_add_u32 m0, m0, 147456\n"
  "s_mul_i32 s14, s14, 36864\n"
  "s_add_u32 s12, s12, s14\n"
  "s_lshl_b32 s14, s81, 11\n"
  "s_sub_u32 s12, s12, s14\n"
  "s_sub_u32 s12, s12, 1024\n"
  "buffer_load_dwordx4 v248, s[20:23], s12 offen sc1 lds\n"
  "buffer_load_dwordx4 v[232:235], v248, s[20:23], s45 offen offset:1024 sc1\n"
  "v_mfma_f32_16x16x32_bf16 v[24:27], v[236:239], v[160:163], v[24:27]\n"
  "v_mfma_f32_16x16x32_bf16 v[60:63], v[236:239], v[164:167], v[60:63]\n"
  "v_mfma_f32_16x16x32_bf16 v[96:99], v[236:239], v[168:171], v[96:99]\n"
  "v_mfma_f32_16x16x32_bf16 v[132:135], v[236:239], v[172:175], v[132:135]\n"
  "buffer_load_dwordx4 v[236:239], v248, s[20:23], s45 offen offset:2048 sc1\n"
  "v_mfma_f32_16x16x32_bf16 v[28:31], v[240:243], v[160:163], v[28:31]\n"
  "v_mfma_f32_16x16x32_bf16 v[64:67], v[240:243], v[164:167], v[64:67]\n"
  "v_mfma_f32_16x16x32_bf16 v[100:103], v[240:243], v[168:171], v[100:103]\n"
  "v_mfma_f32_16x16x32_bf16 v[136:139], v[240:243], v[172:175], v[136:139]\n"
  "buffer_load_dwordx4 v[240:243], v248, s[20:23], s45 offen offset:3072 sc1\n"
  "s_waitcnt vmcnt(25)\n"
  "v_mfma_f32_16x16x32_bf16 v[32:35], v[244:247], v[160:163], v[32:35]\n"
  "s_add_u32 s43, s8, 45056\n"
  "buffer_load_dwordx4 v[160:163], v248, s[20:23], s43 offen sc1\n"
  "v_mfma_f32_16x16x32_bf16 v[68:71], v[244:247], v[164:167], v[68:71]\n"
  "s_add_u32 s43, s9, 45056\n"
  "buffer_load_dwordx4 v[164:167], v248, s[20:23], s43 offen sc1\n"
  "v_mfma_f32_16x16x32_bf16 v[104:107], v[244:247], v[168:171], v[104:107]\n"
  "s_add_u32 s43, s10, 45056\n"
  "buffer_load_dwordx4 v[168:171], v248, s[20:23], s43 offen sc1\n"
  "v_mfma_f32_16x16x32_bf16 v[140:143], v[244:247], v[172:175], v[140:143]\n"
  "s_add_u32 s43, s11, 45056\n"
  "buffer_load_dwordx4 v[172:175], v248, s[20:23], s43 offen sc1\n"
  "s_add_u32 s45, s78, 413696\n"
  "buffer_load_dwordx4 v[244:247], v248, s[20:23], s45 offen offset:0 sc1\n"
  "s_waitcnt vmcnt(21)\n"
  "v_mfma_f32_16x16x32_bf16 v[0:3], v[176:179], v[144:147], v[0:3]\n"
  "s_waitcnt vmcnt(20)\n"
  "v_mfma_f32_16x16x32_bf16 v[36:39], v[176:179], v[148:151], v[36:39]\n"
  "s_waitcnt vmcnt(19)\n"
  "v_mfma_f32_16x16x32_bf16 v[72:75], v[176:179], v[152:155], v[72:75]\n"
  "s_waitcnt vmcnt(18)\n"
  "v_mfma_f32_16x16x32_bf16 v[108:111], v[176:179], v[156:159], v[108:111]\n"
  "s_add_u32 s45, s78, 516096\n"
  "buffer_load_dwordx4 v[176:179], v248, s[20:23], s45 offen offset:0 sc1\n"
  "v_mfma_f32_16x16x32_bf16 v[4:7], v[180:183], v[144:147], v[4:7]\n"
  "v_mfma_f32_16x16x32_bf16 v[40:43], v[180:183], v[148:151], v[40:43]\n"
  "v_mfma_f32_16x16x32_bf16 v[76:79], v[180:183], v[152:155], v[76:79]\n"
  "v_mfma_f32_16x16x32_bf16 v[112:115], v[180:183], v[156:159], v[112:115]\n"
  "buffer_load_dwordx4 v[180:183], v248, s[20:23], s45 offen offset:1024 sc1\n"
  "v_mfma_f32_16x16x32_bf16 v[8:11], v[184:187], v[144:147], v[8:11]\n"
  "v_mfma_f32_16x16x32_bf16 v[44:47], v[184:187], v[148:151], v[44:47]\n"
  "v_mfma_f32_16x16x32_bf16 v[80:83], v[184:187], v[152:155], v[80:83]\n"
  "v_mfma_f32_16x16x32_bf16 v[116:119], v[184:187], v[156:159], v[116:119]\n"
  "buffer_load_dwordx4 v[184:187], v248, s[20:23], s45 offen offset:2048 sc1\n"
  "v_mfma_f32_16x16x32_bf16 v[12:15], v[188:191], v[144:147], v[12:15]\n"
  "v_mfma_f32_16x16x32_bf16 v[48:51], v[188:191], v[148:151], v[48:51]\n"
  "v_mfma_f32_16x16x32_bf16 v[84:87], v[188:191], v[152:155], v[84:87]\n"
  "v_mfma_f32_16x16x32_bf16 v[120:123], v[188:191], v[156:159], v[120:123]\n"
  "buffer_load_dwordx4 v[188:191], v248, s[20:23], s45 offen offset:3072 sc1\n"
  "v_mfma_f32_16x16x32_bf16 v[16:19], v[192:195], v[144:147], v[16:19]\n"
  "v_mfma_f32_16x16x32_bf16 v[52:55], v[192:195], v[148:151], v[52:55]\n"
  "v_mfma_f32_16x16x32_bf16 v[88:91], v[192:195], v[152:155], v[88:91]\n"
  "v_mfma_f32_16x16x32_bf16 v[124:127], v[192:195], v[156:159], v[124:127]\n"
  "s_add_u32 s45, s78, 520192\n"
  "buffer_load_dwordx4 v[192:195], v248, s[20:23], s45 offen offset:0 sc1\n"
  "v_mfma_f32_16x16x32_bf16 v[20:23], v[196:199], v[144:147], v[20:23]\n"
  "v_mfma_f32_16x16x32_bf16 v[56:59], v[196:199], v[148:151], v[56:59]\n"
  "v_mfma_f32_16x16x32_bf16 v[92:95], v[196:199], v[152:155], v[92:95]\n"
  "v_mfma_f32_16x16x32_bf16 v[128:131], v[196:199], v[156:159], v[128:131]\n"
  "buffer_load_dwordx4 v[196:199], v248, s[20:23], s45 offen offset:1024 sc1\n"
  "v_mfma_f32_16x16x32_bf16 v[24:27], v[200:203], v[144:147], v[24:27]\n"
  "v_mfma_f32_16x16x32_bf16 v[60:63], v[200:203], v[148:151], v[60:63]\n"
  "v_mfma_f32_16x16x32_bf16 v[96:99], v[200:203], v[152:155], v[96:99]\n"
  "v_mfma_f32_16x16x32_bf16 v[132:135], v[200:203], v[156:159], v[132:135]\n"
  "buffer_load_dwordx4 v[200:203], v248, s[20:23], s45 offen offset:2048 sc1\n"
  "v_mfma_f32_16x16x32_bf16 v[28:31], v[204:207], v[144:147], v[28:31]\n"
  "v_mfma_f32_16x16x32_bf16 v[64:67], v[204:207], v[148:151], v[64:67]\n"
  "v_mfma_f32_16x16x32_bf16 v[100:103], v[204:207], v[152:155], v[100:103]\n"
  "v_mfma_f32_16x16x32_bf16 v[136:139], v[204:207], v[156:159], v[136:139]\n"
  "buffer_load_dwordx4 v[204:207], v248, s[20:23], s45 offen offset:3072 sc1\n"
  "s_waitcnt vmcnt(25)\n"
  "v_mfma_f32_16x16x32_bf16 v[32:35], v[208:211], v[144:147], v[32:35]\n"
  "s_add_u32 s43, s8, 57344\n"
  "buffer_load_dwordx4 v[144:147], v248, s[20:23], s43 offen sc1\n"
  "v_mfma_f32_16x16x32_bf16 v[68:71], v[208:211], v[148:151], v[68:71]\n"
  "s_add_u32 s43, s9, 57344\n"
  "buffer_load_dwordx4 v[148:151], v248, s[20:23], s43 offen sc1\n"
  "v_mfma_f32_16x16x32_bf16 v[104:107], v[208:211], v[152:155], v[104:107]\n"
  "s_add_u32 s43, s10, 57344\n"
  "buffer_load_dwordx4 v[152:155], v248, s[20:23], s43 offen sc1\n"
  "v_mfma_f32_16x16x32_bf16 v[140:143], v[208:211], v[156:159], v[140:143]\n"
  "s_add_u32 s43, s11, 57344\n"
  "buffer_load_dwordx4 v[156:159], v248, s[20:23], s43 offen sc1\n"
  "s_add_u32 s45, s78, 524288\n"
  "buffer_load_dwordx4 v[208:211], v248, s[20:23], s45 offen offset:0 sc1\n"
  "s_waitcnt vmcnt(17)\n"
  "v_mfma_f32_16x16x32_bf16 v[0:3], v[212:215], v[160:163], v[0:3]\n"
  "s_waitcnt vmcnt(16)\n"
  "v_mfma_f32_16x16x32_bf16 v[36:39], v[212:215], v[164:167], v[36:39]\n"
  "s_waitcnt vmcnt(15)\n"
  "v_mfma_f32_16x16x32_bf16 v[72:75], v[212:215], v[168:171], v[72:75]\n"
  "s_waitcnt vmcnt(14)\n"
  "v_mfma_f32_16x16x32_bf16 v[108:111], v[212:215], v[172:175], v[108:111]\n"
  "s_add_u32 s45, s78, 552960\n"
  "buffer_load_dwordx4 v[212:215], v248, s[20:23], s45 offen offset:0 sc1\n"
  "v_mfma_f32_16x16x32_bf16 v[4:7], v[216:219], v[160:163], v[4:7]\n"
  "v_mfma_f32_16x16x32_bf16 v[40:43], v[216:219], v[164:167], v[40:43]\n"
  "v_mfma_f32_16x16x32_bf16 v[76:79], v[216:219], v[168:171], v[76:79]\n"
  "v_mfma_f32_16x16x32_bf16 v[112:115], v[216:219], v[172:175], v[112:115]\n"
  "buffer_load_dwordx4 v[216:219], v248, s[20:23], s45 offen offset:1024 sc1\n"
  "v_mfma_f32_16x16x32_bf16 v[8:11], v[220:223], v[160:163], v[8:11]\n"
  "v_mfma_f32_16x16x32_bf16 v[44:47], v[220:223], v[164:167], v[44:47]\n"
  "v_mfma_f32_16x16x32_bf16 v[80:83], v[220:223], v[168:171], v[80:83]\n"
  "v_mfma_f32_16x16x32_bf16 v[116:119], v[220:223], v[172:175], v[116:119]\n"
  "buffer_load_dwordx4 v[220:223], v248, s[20:23], s45 offen offset:2048 sc1\n"
  "v_mfma_f32_16x16x32_bf16 v[12:15], v[224:227], v[160:163], v[12:15]\n"
  "v_mfma_f32_16x16x32_bf16 v[48:51], v[224:227], v[164:167], v[48:51]\n"
  "v_mfma_f32_16x16x32_bf16 v[84:87], v[224:227], v[168:171], v[84:87]\n"
  "v_mfma_f32_16x16x32_bf16 v[120:123], v[224:227], v[172:175], v[120:123]\n"
  "buffer_load_dwordx4 v[224:227], v248, s[20:23], s45 offen offset:3072 sc1\n"
  "v_mfma_f32_16x16x32_bf16 v[16:19], v[228:231], v[160:163], v[16:19]\n"
  "v_mfma_f32_16x16x32_bf16 v[52:55], v[228:231], v[164:167], v[52:55]\n"
  "v_mfma_f32_16x16x32_bf16 v[88:91], v[228:231], v[168:171], v[88:91]\n"
  "v_mfma_f32_16x16x32_bf16 v[124:127], v[228:231], v[172:175], v[124:127]\n"
  "s_add_u32 s45, s78, 557056\n"
  "buffer_load_dwordx4 v[228:231], v248, s[20:23], s45 offen offset:0 sc1\n"
  "v_mfma_f32_16x16x32_bf16 v[20:23], v[232:235], v[160:163], v[20:23]\n"
  "v_mfma_f32_16x16x32_bf16 v[56:59], v[232:235], v[164:167], v[56:59]\n"
  "v_mfma_f32_16x16x32_bf16 v[92:95], v[232:235], v[168:171], v[92:95]\n"
  "v_mfma_f32_16x16x32_bf16 v[128:131], v[232:235], v[172:175], v[128:131]\n"
  "buffer_load_dwordx4 v[232:235], v248, s[20:23], s45 offen offset:1024 sc1\n"
  "v_mfma_f32_16x16x32_bf16 v[24:27], v[236:239], v[160:163], v[24:27]\n"
  "v_mfma_f32_16x16x32_bf16 v[60:63], v[236:239], v[164:167], v[60:63]\n"
  "v_mfma_f32_16x16x32_bf16 v[96:99], v[236:239], v[168:171], v[96:99]\n"
  "v_mfma_f32_16x16x32_bf16 v[132:135], v[236:239], v[172:175], v[132:135]\n"
  "buffer_load_dwordx4 v[236:239], v248, s[20:23], s45 offen offset:2048 sc1\n"
  "v_mfma_f32_16x16x32_bf16 v[28:31], v[240:243], v[160:163], v[28:31]\n"
  "v_mfma_f32_16x16x32_bf16 v[64:67], v[240:243], v[164:167], v[64:67]\n"
  "v_mfma_f32_16x16x32_bf16 v[100:103], v[240:243], v[168:171], v[100:103]\n"
  "v_mfma_f32_16x16x32_bf16 v[136:139], v[240:243], v[172:175], v[136:139]\n"
  "buffer_load_dwordx4 v[240:243], v248, s[20:23], s45 offen offset:3072 sc1\n"
  "s_waitcnt vmcnt(21)\n"
  "v_mfma_f32_16x16x32_bf16 v[32:35], v[244:247], v[160:163], v[32:35]\n"
  "s_add_u32 s43, s8, 61440\n"
  "buffer_load_dwordx4 v[160:163], v248, s[20:23], s43 offen sc1\n"
  "v_mfma_f32_16x16x32_bf16 v[68:71], v[244:247], v[164:167], v[68:71]\n"
  "s_add_u32 s43, s9, 61440\n"
  "buffer_load_dwordx4 v[164:167], v248, s[20:23], s43 offen sc1\n"
  "v_mfma_f32_16x16x32_bf16 v[104:107], v[244:247], v[168:171], v[104:107]\n"
  "s_add_u32 s43, s10, 61440\n"
  "buffer_load_dwordx4 v[168:171], v248, s[20:23], s43 offen sc1\n"
  "v_mfma_f32_16x16x32_bf16 v[140:143], v[244:247], v[172:175], v[140:143]\n"
  "s_add_u32 s43, s11, 61440\n"
  "buffer_load_dwordx4 v[172:175], v248, s[20:23], s43 offen sc1\n"
  "s_add_u32 s45, s78, 561152\n"
  "buffer_load_dwordx4 v[244:247], v248, s[20:23], s45 offen offset:0 sc1\n"
  "s_cmp_lt_u32 s79, 80\n"
  "s_cbranch_scc1 LODD_%=\n"
  "s_waitcnt vmcnt(17)\n"
  "v_mfma_f32_16x16x32_bf16 v[0:3], v[176:179], v[144:147], v[0:3]\n"
  "s_mul_i32 s12, s79, 36864\n"
  "s_add_u32 s78, s78, s12\n"
  "s_lshl_b32 s12, s79, 12\n"
  "s_add_u32 s12, s12, s77\n"
  "s_xor_b32 s13, s81, 0\n"
  "s_and_b32 s14, s13, 1\n"
  "s_lshl3_add_u32 s8, s14, s12\n"
  "s_lshr_b32 s13, s13, 1\n"
  "s_lshl_b32 s13, s13, 10\n"
  "s_add_u32 s8, s8, s13\n"
  "s_xor_b32 s13, s81, 1\n"
  "s_and_b32 s14, s13, 1\n"
  "s_lshl3_add_u32 s9, s14, s12\n"
  "s_lshr_b32 s13, s13, 1\n"
  "s_lshl_b32 s13, s13, 10\n"
  "s_add_u32 s9, s9, s13\n"
  "s_xor_b32 s13, s81, 2\n"
  "s_and_b32 s14, s13, 1\n"
  "s_lshl3_add_u32 s10, s14, s12\n"
  "s_lshr_b32 s13, s13, 1\n"
  "s_lshl_b32 s13, s13, 10\n"
  "s_add_u32 s10, s10, s13\n"
  "s_xor_b32 s13, s81, 3\n"
  "s_and_b32 s14, s13, 1\n"
  "s_lshl3_add_u32 s11, s14, s12\n"
  "s_lshr_b32 s13, s13, 1\n"
  "s_lshl_b32 s13, s13, 10\n"
  "s_add_u32 s11, s11, s13\n"
  "s_waitcnt vmcnt(16)\n"
  "v_mfma_f32_16x16x32_bf16 v[36:39], v[176:179], v[148:151], v[36:39]\n"
  "s_waitcnt vmcnt(15)\n"
  "v_mfma_f32_16x16x32_bf16 v[72:75], v[176:179], v[152:155], v[72:75]\n"
  "s_waitcnt vmcnt(14)\n"
  "v_mfma_f32_16x16x32_bf16 v[108:111], v[176:179], v[156:159], v[108:111]\n"
  "s_add_u32 s45, s78, 0\n"
  "buffer_load_dwordx4 v[176:179], v248, s[20:23], s45 offen offset:0 sc1\n"
  "v_mfma_f32_16x16x32_bf16 v[4:7], v[180:183], v[144:147], v[4:7]\n"
  "v_mfma_f32_16x16x32_bf16 v[40:43], v[180:183], v[148:151], v[40:43]\n"
  "v_mfma_f32_16x16x32_bf16 v[76:79], v[180:183], v[152:155], v[76:79]\n"
  "v_mfma_f32_16x16x32_bf16 v[112:115], v[180:183], v[156:159], v[112:115]\n"
  "buffer_load_dwordx4 v[180:183], v248, s[20:23], s45 offen offset:1024 sc1\n"
  "v_mfma_f32_16x16x32_bf16 v[8:11], v[184:187], v[144:147], v[8:11]\n"
  "v_mfma_f32_16x16x32_bf16 v[44:47], v[184:187], v[148:151], v[44:47]\n"
  "v_mfma_f32_16x16x32_bf16 v[80:83], v[184:187], v[152:155], v[80:83]\n"
  "v_mfma_f32_16x16x32_bf16 v[116:119], v[184:187], v[156:159], v[116:119]\n"
  "buffer_load_dwordx4 v[184:187], v248, s[20:23], s45 offen offset:2048 sc1\n"
  "v_mfma_f32_16x16x32_bf16 v[12:15], v[188:191], v[144:147], v[12:15]\n"
  "v_mfma_f32_16x16x32_bf16 v[48:51], v[188:191], v[148:151], v[48:51]\n"
  "v_mfma_f32_16x16x32_bf16 v[84:87], v[188:191], v[152:155], v[84:87]\n"
  "v_mfma_f32_16x16x32_bf16 v[120:123], v[188:191], v[156:159], v[120:123]\n"
  "buffer_load_dwordx4 v[188:191], v248, s[20:23], s45 offen offset:3072 sc1\n"
  "v_mfma_f32_16x16x32_bf16 v[16:19], v[192:195], v[144:147], v[16:19]\n"
  "v_mfma_f32_16x16x32_bf16 v[52:55], v[192:195], v[148:151], v[52:55]\n"
  "v_mfma_f32_16x16x32_bf16 v[88:91], v[192:195], v[152:155], v[88:91]\n"
  "v_mfma_f32_16x16x32_bf16 v[124:127], v[192:195], v[156:159], v[124:127]\n"
  "s_add_u32 s45, s78, 4096\n"
  "buffer_load_dwordx4 v[192:195], v248, s[20:23], s45 offen offset:0 sc1\n"
  "v_mfma_f32_16x16x32_bf16 v[20:23], v[196:199], v[144:147], v[20:23]\n"
  "v_mfma_f32_16x16x32_bf16 v[56:59], v[196:199], v[148:151], v[56:59]\n"
  "v_mfma_f32_16x16x32_bf16 v[92:95], v[196:199], v[152:155], v[92:95]\n"
  "v_mfma_f32_16x16x32_bf16 v[128:131], v[196:199], v[156:159], v[128:131]\n"
  "buffer_load_dwordx4 v[196:199], v248, s[20:23], s45 offen offset:1024 sc1\n"
  "v_mfma_f32_16x16x32_bf16 v[24:27], v[200:203], v[144:147], v[24:27]\n"
  "v_mfma_f32_16x16x32_bf16 v[60:63], v[200:203], v[148:151], v[60:63]\n"
  "v_mfma_f32_16x16x32_bf16 v[96:99], v[200:203], v[152:155], v[96:99]\n"
  "v_mfma_f32_16x16x32_bf16 v[132:135], v[200:203], v[156:159], v[132:135]\n"
  "buffer_load_dwordx4 v[200:203], v248, s[20:23], s45 offen offset:2048 sc1\n"
  "v_mfma_f32_16x16x32_bf16 v[28:31], v[204:207], v[144:147], v[28:31]\n"
  "v_mfma_f32_16x16x32_bf16 v[64:67], v[204:207], v[148:151], v[64:67]\n"
  "v_mfma_f32_16x16x32_bf16 v[100:103], v[204:207], v[152:155], v[100:103]\n"
  "v_mfma_f32_16x16x32_bf16 v[136:139], v[204:207], v[156:159], v[136:139]\n"
  "buffer_load_dwordx4 v[204:207], v248, s[20:23], s45 offen offset:3072 sc1\n"
  "s_waitcnt vmcnt(21)\n"
  "v_mfma_f32_16x16x32_bf16 v[32:35], v[208:211], v[144:147], v[32:35]\n"
  "s_add_u32 s43, s8, 0\n"
  "buffer_load_dwordx2 v[144:145], v248, s[20:23], s43 offen offset:0 sc1\n"
  "buffer_load_dwordx2 v[146:147], v248, s[20:23], s43 offen offset:2048 sc1\n"
  "v_mfma_f32_16x16x32_bf16 v[68:71], v[208:211], v[148:151], v[68:71]\n"
  "s_add_u32 s43, s9, 0\n"
  "buffer_load_dwordx2 v[148:149], v248, s[20:23], s43 offen offset:0 sc1\n"
  "buffer_load_dwordx2 v[150:151], v248, s[20:23], s43 offen offset:2048 sc1\n"
  "v_mfma_f32_16x16x32_bf16 v[104:107], v[208:211], v[152:155], v[104:107]\n"
  "s_add_u32 s43, s10, 0\n"
  "buffer_load_dwordx2 v[152:153], v248, s[20:23], s43 offen offset:0 sc1\n"
  "buffer_load_dwordx2 v[154:155], v248, s[20:23], s43 offen offset:2048 sc1\n"
  "v_mfma_f32_16x16x32_bf16 v[140:143], v[208:211], v[156:159], v[140:143]\n"
  "s_add_u32 s43, s11, 0\n"
  "buffer_load_dwordx2 v[156:157], v248, s[20:23], s43 offen offset:0 sc1\n"
  "buffer_load_dwordx2 v[158:159], v248, s[20:23], s43 offen offset:2048 sc1\n"
  "s_add_u32 s45, s78, 8192\n"
  "buffer_load_dwordx4 v[208:211], v248, s[20:23], s45 offen offset:0 sc1\n"
  "s_waitcnt vmcnt(21)\n"
  "v_mfma_f32_16x16x32_bf16 v[0:3], v[212:215], v[160:163], v[0:3]\n"
  "buffer_load_dword v251, v255, s[20:23], s80 offen sc1\n"
  "s_waitcnt vmcnt(21)\n"
  "v_mfma_f32_16x16x32_bf16 v[36:39], v[212:215], v[164:167], v[36:39]\n"
  "s_waitcnt vmcnt(20)\n"
  "v_mfma_f32_16x16x32_bf16 v[72:75], v[212:215], v[168:171], v[72:75]\n"
  "s_waitcnt vmcnt(19)\n"
  "v_mfma_f32_16x16x32_bf16 v[108:111], v[212:215], v[172:175], v[108:111]\n"
  "s_add_u32 s45, s78, 36864\n"
  "buffer_load_dwordx4 v[212:215], v248, s[20:23], s45 offen offset:0 sc1\n"
  "v_mfma_f32_16x16x32_bf16 v[4:7], v[216:219], v[160:163], v[4:7]\n"
  "v_mfma_f32_16x16x32_bf16 v[40:43], v[216:219], v[164:167], v[40:43]\n"
  "v_mfma_f32_16x16x32_bf16 v[76:79], v[216:219], v[168:171], v[76:79]\n"
  "v_mfma_f32_16x16x32_bf16 v[112:115], v[216:219], v[172:175], v[112:115]\n"
  "buffer_load_dwordx4 v[216:219], v248, s[20:23], s45 offen offset:1024 sc1\n"
  "v_mfma_f32_16x16x32_bf16 v[8:11], v[220:223], v[160:163], v[8:11]\n"
  "v_mfma_f32_16x16x32_bf16 v[44:47], v[220:223], v[164:167], v[44:47]\n"
  "v_mfma_f32_16x16x32_bf16 v[80:83], v[220:223], v[168:171], v[80:83]\n"
  "v_mfma_f32_16x16x32_bf16 v[116:119], v[220:223], v[172:175], v[116:119]\n"
  "buffer_load_dwordx4 v[220:223], v248, s[20:23], s45 offen offset:2048 sc1\n"
  "v_mfma_f32_16x16x32_bf16 v[12:15], v[224:227], v[160:163], v[12:15]\n"
  "v_mfma_f32_16x16x32_bf16 v[48:51], v[224:227], v[164:167], v[48:51]\n"
  "v_mfma_f32_16x16x32_bf16 v[84:87], v[224:227], v[168:171], v[84:87]\n"
  "v_mfma_f32_16x16x32_bf16 v[120:123], v[224:227], v[172:175], v[120:123]\n"
  "buffer_load_dwordx4 v[224:227], v248, s[20:23], s45 offen offset:3072 sc1\n"
  "v_mfma_f32_16x16x32_bf16 v[16:19], v[228:231], v[160:163], v[16:19]\n"
  "v_mfma_f32_16x16x32_bf16 v[52:55], v[228:231], v[164:167], v[52:55]\n"
  "v_mfma_f32_16x16x32_bf16 v[88:91], v[228:231], v[168:171], v[88:91]\n"
  "v_mfma_f32_16x16x32_bf16 v[124:127], v[228:231], v[172:175], v[124:127]\n"
  "s_add_u32 s45, s78, 40960\n"
  "buffer_load_dwordx4 v[228:231], v248, s[20:23], s45 offen offset:0 sc1\n"
  "v_mfma_f32_16x16x32_bf16 v[20:23], v[232:235], v[160:163], v[20:23]\n"
  "v_mfma_f32_16x16x32_bf16 v[56:59], v[232:235], v[164:167], v[56:59]\n"
  "v_mfma_f32_16x16x32_bf16 v[92:95], v[232:235], v[168:171], v[92:95]\n"
  "v_mfma_f32_16x16x32_bf16 v[128:131], v[232:235], v[172:175], v[128:131]\n"
  "buffer_load_dwordx4 v[232:235], v248, s[20:23], s45 offen offset:1024 sc1\n"
  "v_mfma_f32_16x16x32_bf16 v[24:27], v[236:239], v[160:163], v[24:27]\n"
  "v_mfma_f32_16x16x32_bf16 v[60:63], v[236:239], v[164:167], v[60:63]\n"
  "v_mfma_f32_16x16x32_bf16 v[96:99], v[236:239], v[168:171], v[96:99]\n"
  "v_mfma_f32_16x16x32_bf16 v[132:135], v[236:239], v[172:175], v[132:135]\n"
  "buffer_load_dwordx4 v[236:239], v248, s[20:23], s45 offen offset:2048 sc1\n"
  "v_mfma_f32_16x16x32_bf16 v[28:31], v[240:243], v[160:163], v[28:31]\n"
  "v_mfma_f32_16x16x32_bf16 v[64:67], v[240:243], v[164:167], v[64:67]\n"
  "v_mfma_f32_16x16x32_bf16 v[100:103], v[240:243], v[168:171], v[100:103]\n"
  "v_mfma_f32_16x16x32_bf16 v[136:139], v[240:243], v[172:175], v[136:139]\n"
  "buffer_load_dwordx4 v[240:243], v248, s[20:23], s45 offen offset:3072 sc1\n"
  "s_waitcnt vmcnt(26)\n"
  "v_mfma_f32_16x16x32_bf16 v[32:35], v[244:247], v[160:163], v[32:35]\n"
  "s_add_u32 s43, s8, 4096\n"
  "buffer_load_dwordx2 v[160:161], v248, s[20:23], s43 offen offset:0 sc1\n"
  "buffer_load_dwordx2 v[162:163], v248, s[20:23], s43 offen offset:2048 sc1\n"
  "v_mfma_f32_16x16x32_bf16 v[68:71], v[244:247], v[164:167], v[68:71]\n"
  "s_add_u32 s43, s9, 4096\n"
  "buffer_load_dwordx2 v[164:165], v248, s[20:23], s43 offen offset:0 sc1\n"
  "buffer_load_dwordx2 v[166:167], v248, s[20:23], s43 offen offset:2048 sc1\n"
  "v_mfma_f32_16x16x32_bf16 v[104:107], v[244:247], v[168:171], v[104:107]\n"
  "s_add_u32 s43, s10, 4096\n"
  "buffer_load_dwordx2 v[168:169], v248, s[20:23], s43 offen offset:0 sc1\n"
  "buffer_load_dwordx2 v[170:171], v248, s[20:23], s43 offen offset:2048 sc1\n"
  "v_mfma_f32_16x16x32_bf16 v[140:143], v[244:247], v[172:175], v[140:143]\n"
  "s_add_u32 s43, s11, 4096\n"
  "buffer_load_dwordx2 v[172:173], v248, s[20:23], s43 offen offset:0 sc1\n"
  "buffer_load_dwordx2 v[174:175], v248, s[20:23], s43 offen offset:2048 sc1\n"
  "s_add_u32 s45, s78, 45056\n"
  "buffer_load_dwordx4 v[244:247], v248, s[20:23], s45 offen offset:0 sc1\n"
  "s_waitcnt vmcnt(25)\n"
  "v_mfma_f32_16x16x32_bf16 v[0:3], v[176:179], v[144:147], v[0:3]\n"
  "s_waitcnt vmcnt(17)\n"
  "v_cmp_ne_u32 vcc, s47, v251\n"
  "s_cbranch_vccnz LSX_%=\n"
  "LSXB_%=:\n"
  "v_mfma_f32_16x16x32_bf16 v[36:39], v[176:179], v[148:151], v[36:39]\n"
  "v_mfma_f32_16x16x32_bf16 v[72:75], v[176:179], v[152:155], v[72:75]\n"
  "v_mfma_f32_16x16x32_bf16 v[108:111], v[176:179], v[156:159], v[108:111]\n"
  "s_add_u32 s45, s78, 294912\n"
  "buffer_load_dwordx4 v[176:179], v248, s[20:23], s45 offen offset:0 sc1\n"
  "v_mfma_f32_16x16x32_bf16 v[4:7], v[180:183], v[144:147], v[4:7]\n"
  "v_mfma_f32_16x16x32_bf16 v[40:43], v[180:183], v[148:151], v[40:43]\n"
  "v_mfma_f32_16x16x32_bf16 v[76:79], v[180:183], v[152:155], v[76:79]\n"
  "v_mfma_f32_16x16x32_bf16 v[112:115], v[180:183], v[156:159], v[112:115]\n"
  "buffer_load_dwordx4 v[180:183], v248, s[20:23], s45 offen offset:1024 sc1\n"
  "v_mfma_f32_16x16x32_bf16 v[8:11], v[184:187], v[144:147], v[8:11]\n"
  "v_mfma_f32_16x16x32_bf16 v[44:47], v[184:187], v[148:151], v[44:47]\n"
  "v_mfma_f32_16x16x32_bf16 v[80:83], v[184:187], v[152:155], v[80:83]\n"
  "v_mfma_f32_16x16x32_bf16 v[116:119], v[184:187], v[156:159], v[116:119]\n"
  "buffer_load_dwordx4 v[184:187], v248, s[20:23], s45 offen offset:2048 sc1\n"
  "v_mfma_f32_16x16x32_bf16 v[12:15], v[188:191], v[144:147], v[12:15]\n"
  "v_mfma_f32_16x16x32_bf16 v[48:51], v[188:191], v[148:151], v[48:51]\n"
  "v_mfma_f32_16x16x32_bf16 v[84:87], v[188:191], v[152:155], v[84:87]\n"
  "v_mfma_f32_16x16x32_bf16 v[120:123], v[188:191], v[156:159], v[120:123]\n"
  "buffer_load_dwordx4 v[188:191], v248, s[20:23], s45 offen offset:3072 sc1\n"
  "v_mfma_f32_16x16x32_bf16 v[16:19], v[192:195], v[144:147], v[16:19]\n"
  "v_mfma_f32_16x16x32_bf16 v[52:55], v[192:195], v[148:151], v[52:55]\n"
  "v_mfma_f32_16x16x32_bf16 v[88:91], v[192:195], v[152:155], v[88:91]\n"
  "v_mfma_f32_16x16x32_bf16 v[124:127], v[192:195], v[156:159], v[124:127]\n"
  "s_add_u32 s45, s78, 299008\n"
  "buffer_load_dwordx4 v[192:195], v248, s[20:23], s45 offen offset:0 sc1\n"
  "v_mfma_f32_16x16x32_bf16 v[20:23], v[196:199], v[144:147], v[20:23]\n"
  "v_mfma_f32_16x16x32_bf16 v[56:59], v[196:199], v[148:151], v[56:59]\n"
  "v_mfma_f32_16x16x32_bf16 v[92:95], v[196:199], v[152:155], v[92:95]\n"
  "v_mfma_f32_16x16x32_bf16 v[128:131], v[196:199], v[156:159], v[128:131]\n"
  "buffer_load_dwordx4 v[196:199], v248, s[20:23], s45 offen offset:1024 sc1\n"
  "v_mfma_f32_16x16x32_bf16 v[24:27], v[200:203], v[144:147], v[24:27]\n"
  "v_mfma_f32_16x16x32_bf16 v[60:63], v[200:203], v[148:151], v[60:63]\n"
  "v_mfma_f32_16x16x32_bf16 v[96:99], v[200:203], v[152:155], v[96:99]\n"
  "v_mfma_f32_16x16x32_bf16 v[132:135], v[200:203], v[156:159], v[132:135]\n"
  "buffer_load_dwordx4 v[200:203], v248, s[20:23], s45 offen offset:2048 sc1\n"
  "v_mfma_f32_16x16x32_bf16 v[28:31], v[204:207], v[144:147], v[28:31]\n"
  "v_mfma_f32_16x16x32_bf16 v[64:67], v[204:207], v[148:151], v[64:67]\n"
  "v_mfma_f32_16x16x32_bf16 v[100:103], v[204:207], v[152:155], v[100:103]\n"
  "v_mfma_f32_16x16x32_bf16 v[136:139], v[204:207], v[156:159], v[136:139]\n"
  "buffer_load_dwordx4 v[204:207], v248, s[20:23], s45 offen offset:3072 sc1\n"
  "v_mfma_f32_16x16x32_bf16 v[32:35], v[208:211], v[144:147], v[32:35]\n"
  "s_add_u32 s43, s8, 32768\n"
  "buffer_load_dwordx2 v[144:145], v248, s[20:23], s43 offen offset:0 sc1\n"
  "buffer_load_dwordx2 v[146:147], v248, s[20:23], s43 offen offset:2048 sc1\n"
  "v_mfma_f32_16x16x32_bf16 v[68:71], v[208:211], v[148:151], v[68:71]\n"
  "s_add_u32 s43, s9, 32768\n"
  "buffer_load_dwordx2 v[148:149], v248, s[20:23], s43 offen offset:0 sc1\n"
  "buffer_load_dwordx2 v[150:151], v248, s[20:23], s43 offen offset:2048 sc1\n"
  "v_mfma_f32_16x16x32_bf16 v[104:107], v[208:211], v[152:155], v[104:107]\n"
  "s_add_u32 s43, s10, 32768\n"
  "buffer_load_dwordx2 v[152:153], v248, s[20:23], s43 offen offset:0 sc1\n"
  "buffer_load_dwordx2 v[154:155], v248, s[20:23], s43 offen offset:2048 sc1\n"
  "v_mfma_f32_16x16x32_bf16 v[140:143], v[208:211], v[156:159], v[140:143]\n"
  "s_add_u32 s43, s11, 32768\n"
  "buffer_load_dwordx2 v[156:157], v248, s[20:23], s43 offen offset:0 sc1\n"
  "buffer_load_dwordx2 v[158:159], v248, s[20:23], s43 offen offset:2048 sc1\n"
  "s_add_u32 s45, s78, 303104\n"
  "buffer_load_dwordx4 v[208:211], v248, s[20:23], s45 offen offset:0 sc1\n"
  "s_waitcnt vmcnt(24)\n"
  "v_mfma_f32_16x16x32_bf16 v[0:3], v[212:215], v[160:163], v[0:3]\n"
  "s_waitcnt vmcnt(22)\n"
  "v_mfma_f32_16x16x32_bf16 v[36:39], v[212:215], v[164:167], v[36:39]\n"
  "s_waitcnt vmcnt(20)\n"
  "v_mfma_f32_16x16x32_bf16 v[72:75], v[212:215], v[168:171], v[72:75]\n"
  "s_waitcnt vmcnt(18)\n"
  "v_mfma_f32_16x16x32_bf16 v[108:111], v[212:215], v[172:175], v[108:111]\n"
  "s_add_u32 s45, s78, 331776\n"
  "buffer_load_dwordx4 v[212:215], v248, s[20:23], s45 offen offset:0 sc1\n"
  "v_mfma_f32_16x16x32_bf16 v[4:7], v[216:219], v[160:163], v[4:7]\n"
  "v_mfma_f32_16x16x32_bf16 v[40:43], v[216:219], v[164:167], v[40:43]\n"
  "v_mfma_f32_16x16x32_bf16 v[76:79], v[216:219], v[168:171], v[76:79]\n"
  "v_mfma_f32_16x16x32_bf16 v[112:115], v[216:219], v[172:175], v[112:115]\n"
  "buffer_load_dwordx4 v[216:219], v248, s[20:23], s45 offen offset:1024 sc1\n"
  "v_mfma_f32_16x16x32_bf16 v[8:11], v[220:223], v[160:163], v[8:11]\n"
  "v_mfma_f32_16x16x32_bf16 v[44:47], v[220:223], v[164:167], v[44:47]\n"
  "v_mfma_f32_16x16x32_bf16 v[80:83], v[220:223], v[168:171], v[80:83]\n"
  "v_mfma_f32_16x16x32_bf16 v[116:119], v[220:223], v[172:175], v[116:119]\n"
  "buffer_load_dwordx4 v[220:223], v248, s[20:23], s45 offen offset:2048 sc1\n"
  "v_mfma_f32_16x16x32_bf16 v[12:15], v[224:227], v[160:163], v[12:15]\n"
  "v_mfma_f32_16x16x32_bf16 v[48:51], v[224:227], v[164:167], v[48:51]\n"
  "v_mfma_f32_16x16x32_bf16 v[84:87], v[224:227], v[168:171], v[84:87]\n"
  "v_mfma_f32_16x16x32_bf16 v[120:123], v[224:227], v[172:175], v[120:123]\n"
  "buffer_load_dwordx4 v[224:227], v248, s[20:23], s45 offen offset:3072 sc1\n"
  "v_mfma_f32_16x16x32_bf16 v[16:19], v[228:231], v[160:163], v[16:19]\n"
  "v_mfma_f32_16x16x32_bf16 v[52:55], v[228:231], v[164:167], v[52:55]\n"
  "v_mfma_f32_16x16x32_bf16 v[88:91], v[228:231], v[168:171], v[88:91]\n"
  "v_mfma_f32_16x16x32_bf16 v[124:127], v[228:231], v[172:175], v[124:127]\n"
  "s_add_u32 s45, s78, 335872\n"
  "buffer_load_dwordx4 v[228:231], v248, s[20:23], s45 offen offset:0 sc1\n"
  "v_mfma_f32_16x16x32_bf16 v[20:23], v[232:235], v[160:163], v[20:23]\n"
  "v_mfma_f32_16x16x32_bf16 v[56:59], v[232:235], v[164:167], v[56:59]\n"
  "v_mfma_f32_16x16x32_bf16 v[92:95], v[232:235], v[168:171], v[92:95]\n"
  "v_mfma_f32_16x16x32_bf16 v[128:131], v[232:235], v[172:175], v[128:131]\n"
  "buffer_load_dwordx4 v[232:235], v248, s[20:23], s45 offen offset:1024 sc1\n"
  "v_mfma_f32_16x16x32_bf16 v[24:27], v[236:239], v[160:163], v[24:27]\n"
  "v_mfma_f32_16x16x32_bf16 v[60:63], v[236:239], v[164:167], v[60:63]\n"
  "v_mfma_f32_16x16x32_bf16 v[96:99], v[236:239], v[168:171], v[96:99]\n"
  "v_mfma_f32_16x16x32_bf16 v[132:135], v[236:239], v[172:175], v[132:135]\n"
  "buffer_load_dwordx4 v[236:239], v248, s[20:23], s45 offen offset:2048 sc1\n"
  "v_mfma_f32_16x16x32_bf16 v[28:31], v[240:243], v[160:163], v[28:31]\n"
  "v_mfma_f32_16x16x32_bf16 v[64:67], v[240:243], v[164:167], v[64:67]\n"
  "v_mfma_f32_16x16x32_bf16 v[100:103], v[240:243], v[168:171], v[100:103]\n"
  "v_mfma_f32_16x16x32_bf16 v[136:139], v[240:243], v[172:175], v[136:139]\n"
  "buffer_load_dwordx4 v[240:243], v248, s[20:23], s45 offen offset:3072 sc1\n"
  "s_waitcnt vmcnt(25)\n"
  "v_mfma_f32_16x16x32_bf16 v[32:35], v[244:247], v[160:163], v[32:35]\n"
  "s_add_u32 s43, s8, 36864\n"
  "buffer_load_dwordx2 v[160:161], v248, s[20:23], s43 offen offset:0 sc1\n"
  "buffer_load_dwordx2 v[162:163], v248, s[20:23], s43 offen offset:2048 sc1\n"
  "v_mfma_f32_16x16x32_bf16 v[68:71], v[244:247], v[164:167], v[68:71]\n"
  "s_add_u32 s43, s9, 36864\n"
  "buffer_load_dwordx2 v[164:165], v248, s[20:23], s43 offen offset:0 sc1\n"
  "buffer_load_dwordx2 v[166:167], v248, s[20:23], s43 offen offset:2048 sc1\n"
  "v_mfma_f32_16x16x32_bf16 v[104:107], v[244:247], v[168:171], v[104:107]\n"
  "s_add_u32 s43, s10, 36864\n"
  "buffer_load_dwordx2 v[168:169], v248, s[20:23], s43 offen offset:0 sc1\n"
  "buffer_load_dwordx2 v[170:171], v248, s[20:23], s43 offen offset:2048 sc1\n"
  "v_mfma_f32_16x16x32_bf16 v[140:143], v[244:247], v[172:175], v[140:143]\n"
  "s_add_u32 s43, s11, 36864\n"
  "buffer_load_dwordx2 v[172:173], v248, s[20:23], s43 offen offset:0 sc1\n"
  "buffer_load_dwordx2 v[174:175], v248, s[20:23], s43 offen offset:2048 sc1\n"
  "s_add_u32 s45, s78, 339968\n"
  "buffer_load_dwordx4 v[244:247], v248, s[20:23], s45 offen offset:0 sc1\n"
  "LODD_%=:\n"
  "v_lshrrev_b32 v255, 1, v248\n"
  "s_waitcnt vmcnt(17)\n"
  "v_mfma_f32_16x16x32_bf16 v[0:3], v[176:179], v[144:147], v[0:3]\n"
  "s_waitcnt vmcnt(16)\n"
  "v_mfma_f32_16x16x32_bf16 v[36:39], v[176:179], v[148:151], v[36:39]\n"
  "s_waitcnt vmcnt(15)\n"
  "v_mfma_f32_16x16x32_bf16 v[72:75], v[176:179], v[152:155], v[72:75]\n"
  "s_setprio 2\n"
  "s_waitcnt vmcnt(14)\n"
  "v_mfma_f32_16x16x32_bf16 v[108:111], v[176:179], v[156:159], v[108:111]\n"
  "s_add_u32 s12, s86, 0\n"
  "buffer_load_dwordx4 v[176:179], v248, s[20:23], s12 offen offset:0 sc1\n"
  "v_mfma_f32_16x16x32_bf16 v[4:7], v[180:183], v[144:147], v[4:7]\n"
  "v_mfma_f32_16x16x32_bf16 v[40:43], v[180:183], v[148:151], v[40:43]\n"
  "v_mfma_f32_16x16x32_bf16 v[76:79], v[180:183], v[152:155], v[76:79]\n"
  "v_mfma_f32_16x16x32_bf16 v[112:115], v[180:183], v[156:159], v[112:115]\n"
  "s_add_u32 s12, s86, 0\n"
  "buffer_load_dwordx4 v[180:183], v248, s[20:23], s12 offen offset:1024 sc1\n"
  "v_mfma_f32_16x16x32_bf16 v[8:11], v[184:187], v[144:147], v[8:11]\n"
  "v_mfma_f32_16x16x32_bf16 v[44:47], v[184:187], v[148:151], v[44:47]\n"
  "v_mfma_f32_16x16x32_bf16 v[80:83], v[184:187], v[152:155], v[80:83]\n"
  "v_mfma_f32_16x16x32_bf16 v[116:119], v[184:187], v[156:159], v[116:119]\n"
  "s_add_u32 s12, s86, 36864\n"
  "buffer_load_dwordx4 v[184:187], v248, s[20:23], s12 offen offset:0 sc1\n"
  "v_mfma_f32_16x16x32_bf16 v[12:15], v[188:191], v[144:147], v[12:15]\n"
  "v_mfma_f32_16x16x32_bf16 v[48:51], v[188:191], v[148:151], v[48:51]\n"
  "v_mfma_f32_16x16x32_bf16 v[84:87], v[188:191], v[152:155], v[84:87]\n"
  "v_mfma_f32_16x16x32_bf16 v[120:123], v[188:191], v[156:159], v[120:123]\n"
  "s_add_u32 s12, s86, 36864\n"
  "buffer_load_dwordx4 v[188:191], v248, s[20:23], s12 offen offset:1024 sc1\n"
  "v_mfma_f32_16x16x32_bf16 v[16:19], v[192:195], v[144:147], v[16:19]\n"
  "v_mfma_f32_16x16x32_bf16 v[52:55], v[192:195], v[148:151], v[52:55]\n"
  "v_mfma_f32_16x16x32_bf16 v[88:91], v[192:195], v[152:155], v[88:91]\n"
  "v_mfma_f32_16x16x32_bf16 v[124:127], v[192:195], v[156:159], v[124:127]\n"
  "s_add_u32 s12, s86, 73728\n"
  "buffer_load_dwordx4 v[192:195], v248, s[20:23], s12 offen offset:0 sc1\n"
  "v_mfma_f32_16x16x32_bf16 v[20:23], v[196:199], v[144:147], v[20:23]\n"
  "v_mfma_f32_16x16x32_bf16 v[56:59], v[196:199], v[148:151], v[56:59]\n"
  "v_mfma_f32_16x16x32_bf16 v[92:95], v[196:199], v[152:155], v[92:95]\n"
  "v_mfma_f32_16x16x32_bf16 v[128:131], v[196:199], v[156:159], v[128:131]\n"
  "s_add_u32 s12, s86, 73728\n"
  "buffer_load_dwordx4 v[196:199], v248, s[20:23], s12 offen offset:1024 sc1\n"
  "v_mfma_f32_16x16x32_bf16 v[24:27], v[200:203], v[144:147], v[24:27]\n"
  "v_mfma_f32_16x16x32_bf16 v[60:63], v[200:203], v[148:151], v[60:63]\n"
  "v_mfma_f32_16x16x32_bf16 v[96:99], v[200:203], v[152:155], v[96:99]\n"
  "v_mfma_f32_16x16x32_bf16 v[132:135], v[200:203], v[156:159], v[132:135]\n"
  "s_add_u32 s12, s86, 110592\n"
  "buffer_load_dwordx4 v[200:203], v248, s[20:23], s12 offen offset:0 sc1\n"
  "v_mfma_f32_16x16x32_bf16 v[28:31], v[204:207], v[144:147], v[28:31]\n"
  "v_mfma_f32_16x16x32_bf16 v[64:67], v[204:207], v[148:151], v[64:67]\n"
  "v_mfma_f32_16x16x32_bf16 v[100:103], v[204:207], v[152:155], v[100:103]\n"
  "v_mfma_f32_16x16x32_bf16 v[136:139], v[204:207], v[156:159], v[136:139]\n"
  "s_add_u32 s12, s86, 110592\n"
  "buffer_load_dwordx4 v[204:207], v248, s[20:23], s12 offen offset:1024 sc1\n"
  "s_waitcnt vmcnt(21)\n"
  "v_mfma_f32_16x16x32_bf16 v[32:35], v[208:211], v[144:147], v[32:35]\n"
  "v_mfma_f32_16x16x32_bf16 v[68:71], v[208:211], v[148:151], v[68:71]\n"
  "v_mfma_f32_16x16x32_bf16 v[104:107], v[208:211], v[152:155], v[104:107]\n"
  "v_mfma_f32_16x16x32_bf16 v[140:143], v[208:211], v[156:159], v[140:143]\n"
  "s_add_u32 s12, s86, 147456\n"
  "buffer_load_dwordx4 v[208:211], v248, s[20:23], s12 offen offset:0 sc1\n"
  "s_waitcnt vmcnt(12)\n"
  "v_mfma_f32_16x16x32_bf16 v[36:39], v[212:215], v[164:167], v[36:39]\n"
  "v_mov_b32 v150, 163776\n"
  "ds_read_b128 v[144:147], v150 offset:32\n"
  "s_xor_b32 s12, s81, 1\n"
  "s_mul_i32 s12, s12, 27648\n"
  "s_add_u32 s12, s12, 36864\n"
  "v_add_u32 v148, s12, v248\n"
  "s_xor_b32 s12, s81, 2\n"
  "s_mul_i32 s12, s12, 27648\n"
  "s_add_u32 s12, s12, 36864\n"
  "v_add_u32 v149, s12, v248\n"
  "s_xor_b32 s12, s81, 3\n"
  "s_mul_i32 s12, s12, 27648\n"
  "s_add_u32 s12, s12, 36864\n"
  "v_add_u32 v151, s12, v248\n"
  "s_waitcnt vmcnt(11)\n"
  "v_mfma_f32_16x16x32_bf16 v[72:75], v[212:215], v[168:171], v[72:75]\n"
  "s_add_u32 s12, s86, 331776\n"
  "buffer_load_dwordx4 v[152:155], v248, s[20:23], s12 offen offset:0 sc1\n"
  "s_waitcnt vmcnt(11)\n"
  "v_mfma_f32_16x16x32_bf16 v[108:111], v[212:215], v[172:175], v[108:111]\n"
  "s_add_u32 s12, s86, 331776\n"
  "buffer_load_dwordx4 v[156:159], v248, s[20:23], s12 offen offset:1024 sc1\n"
  "v_mfma_f32_16x16x32_bf16 v[0:3], v[212:215], v[160:163], v[0:3]\n"
  "s_waitcnt lgkmcnt(0)\n"
  "v_min3_u32 v144, v144, v145, v146\n"
  "v_min_u32 v144, v144, v147\n"
  "v_cmp_gt_u32 vcc, 3, v144\n"
  "s_cbranch_vccnz LSP_%=\n"
  "LSPB_%=:\n"
  "s_add_u32 s12, s86, 147456\n"
  "buffer_load_dwordx4 v[212:215], v248, s[20:23], s12 offen offset:1024 sc1\n"
  "v_mfma_f32_16x16x32_bf16 v[40:43], v[216:219], v[164:167], v[40:43]\n"
  "ds_write_b128 v148, v[36:39] offset:0\n"
  "v_mfma_f32_16x16x32_bf16 v[76:79], v[216:219], v[168:171], v[76:79]\n"
  "ds_write_b128 v149, v[72:75] offset:9216\n"
  "v_mfma_f32_16x16x32_bf16 v[112:115], v[216:219], v[172:175], v[112:115]\n"
  "ds_write_b128 v151, v[108:111] offset:18432\n"
  "v_mfma_f32_16x16x32_bf16 v[4:7], v[216:219], v[160:163], v[4:7]\n"
  "s_add_u32 s12, s86, 184320\n"
  "buffer_load_dwordx4 v[216:219], v248, s[20:23], s12 offen offset:0 sc1\n"
  "v_mfma_f32_16x16x32_bf16 v[44:47], v[220:223], v[164:167], v[44:47]\n"
  "ds_write_b128 v148, v[40:43] offset:1024\n"
  "v_mfma_f32_16x16x32_bf16 v[80:83], v[220:223], v[168:171], v[80:83]\n"
  "ds_write_b128 v149, v[76:79] offset:10240\n"
  "v_mfma_f32_16x16x32_bf16 v[116:119], v[220:223], v[172:175], v[116:119]\n"
  "ds_write_b128 v151, v[112:115] offset:19456\n"
  "v_mfma_f32_16x16x32_bf16 v[8:11], v[220:223], v[160:163], v[8:11]\n"
  "s_add_u32 s12, s86, 442368\n"
  "buffer_load_dwordx4 v[36:39], v248, s[20:23], s12 offen offset:0 sc1\n"
  "s_add_u32 s12, s86, 589824\n"
  "buffer_load_dwordx4 v[72:75], v248, s[20:23], s12 offen offset:1024 sc1\n"
  "s_add_u32 s12, s86, 184320\n"
  "buffer_load_dwordx4 v[220:223], v248, s[20:23], s12 offen offset:1024 sc1\n"
  "v_mfma_f32_16x16x32_bf16 v[48:51], v[224:227], v[164:167], v[48:51]\n"
  "ds_write_b128 v148, v[44:47] offset:2048\n"
  "v_mfma_f32_16x16x32_bf16 v[84:87], v[224:227], v[168:171], v[84:87]\n"
  "ds_write_b128 v149, v[80:83] offset:11264\n"
  "v_mfma_f32_16x16x32_bf16 v[120:123], v[224:227], v[172:175], v[120:123]\n"
  "ds_write_b128 v151, v[116:119] offset:20480\n"
  "v_mfma_f32_16x16x32_bf16 v[12:15], v[224:227], v[160:163], v[12:15]\n"
  "s_add_u32 s12, s86, 442368\n"
  "buffer_load_dwordx4 v[40:43], v248, s[20:23], s12 offen offset:1024 sc1\n"
  "s_add_u32 s12, s86, 626688\n"
  "buffer_load_dwordx4 v[76:79], v248, s[20:23], s12 offen offset:0 sc1\n"
  "s_add_u32 s12, s86, 221184\n"
  "buffer_load_dwordx4 v[224:227], v248, s[20:23], s12 offen offset:0 sc1\n"
  "v_mfma_f32_16x16x32_bf16 v[52:55], v[228:231], v[164:167], v[52:55]\n"
  "ds_write_b128 v148, v[48:51] offset:3072\n"
  "v_mfma_f32_16x16x32_bf16 v[88:91], v[228:231], v[168:171], v[88:91]\n"
  "ds_write_b128 v149, v[84:87] offset:12288\n"
  "v_mfma_f32_16x16x32_bf16 v[124:127], v[228:231], v[172:175], v[124:127]\n"
  "ds_write_b128 v151, v[120:123] offset:21504\n"
  "v_mfma_f32_16x16x32_bf16 v[16:19], v[228:231], v[160:163], v[16:19]\n"
  "s_add_u32 s12, s86, 479232\n"
  "buffer_load_dwordx4 v[44:47], v248, s[20:23], s12 offen offset:0 sc1\n"
  "s_add_u32 s12, s86, 626688\n"
  "buffer_load_dwordx4 v[80:83], v248, s[20:23], s12 offen offset:1024 sc1\n"
  "s_add_u32 s12, s86, 221184\n"
  "buffer_load_dwordx4 v[228:231], v248, s[20:23], s12 offen offset:1024 sc1\n"
  "v_mfma_f32_16x16x32_bf16 v[56:59], v[232:235], v[164:167], v[56:59]\n"
  "ds_write_b128 v148, v[52:55] offset:4096\n"
  "v_mfma_f32_16x16x32_bf16 v[92:95], v[232:235], v[168:171], v[92:95]\n"
  "ds_write_b128 v149, v[88:91] offset:13312\n"
  "v_mfma_f32_16x16x32_bf16 v[128:131], v[232:235], v[172:175], v[128:131]\n"
  "ds_write_b128 v151, v[124:127] offset:22528\n"
  "v_mfma_f32_16x16x32_bf16 v[20:23], v[232:235], v[160:163], v[20:23]\n"
  "s_add_u32 s12, s86, 479232\n"
  "buffer_load_dwordx4 v[48:51], v248, s[20:23], s12 offen offset:1024 sc1\n"
  "s_add_u32 s12, s86, 258048\n"
  "buffer_load_dwordx4 v[232:235], v248, s[20:23], s12 offen offset:0 sc1\n"
  "v_mfma_f32_16x16x32_bf16 v[60:63], v[236:239], v[164:167], v[60:63]\n"
  "s_waitcnt lgkmcnt(14)\n"
  "ds_write_b128 v148, v[56:59] offset:5120\n"
  "v_mfma_f32_16x16x32_bf16 v[96:99], v[236:239], v[168:171], v[96:99]\n"
  "s_waitcnt lgkmcnt(14)\n"
  "ds_write_b128 v149, v[92:95] offset:14336\n"
  "v_mfma_f32_16x16x32_bf16 v[132:135], v[236:239], v[172:175], v[132:135]\n"
  "s_waitcnt lgkmcnt(14)\n"
  "ds_write_b128 v151, v[128:131] offset:23552\n"
  "v_mfma_f32_16x16x32_bf16 v[24:27], v[236:239], v[160:163], v[24:27]\n"
  "s_add_u32 s12, s86, 516096\n"
  "buffer_load_dwordx4 v[52:55], v248, s[20:23], s12 offen offset:0 sc1\n"
  "s_add_u32 s12, s86, 258048\n"
  "buffer_load_dwordx4 v[236:239], v248, s[20:23], s12 offen offset:1024 sc1\n"
  "v_mfma_f32_16x16x32_bf16 v[64:67], v[240:243], v[164:167], v[64:67]\n"
  "s_waitcnt lgkmcnt(14)\n"
  "ds_write_b128 v148, v[60:63] offset:6144\n"
  "v_mfma_f32_16x16x32_bf16 v[100:103], v[240:243], v[168:171], v[100:103]\n"
  "s_waitcnt lgkmcnt(14)\n"
  "ds_write_b128 v149, v[96:99] offset:15360\n"
  "v_mfma_f32_16x16x32_bf16 v[136:139], v[240:243], v[172:175], v[136:139]\n"
  "s_waitcnt lgkmcnt(14)\n"
  "ds_write_b128 v151, v[132:135] offset:24576\n"
  "v_mfma_f32_16x16x32_bf16 v[28:31], v[240:243], v[160:163], v[28:31]\n"
  "s_add_u32 s12, s86, 516096\n"
  "buffer_load_dwordx4 v[56:59], v248, s[20:23], s12 offen offset:1024 sc1\n"
  "s_add_u32 s12, s86, 294912\n"
  "buffer_load_dwordx4 v[240:243], v248, s[20:23], s12 offen offset:0 sc1\n"
  "s_waitcnt vmcnt(28)\n"
  "v_mfma_f32_16x16x32_bf16 v[68:71], v[244:247], v[164:167], v[68:71]\n"
  "s_waitcnt lgkmcnt(14)\n"
  "ds_write_b128 v148, v[64:67] offset:7168\n"
  "v_mfma_f32_16x16x32_bf16 v[104:107], v[244:247], v[168:171], v[104:107]\n"
  "s_waitcnt lgkmcnt(14)\n"
  "ds_write_b128 v149, v[100:103] offset:16384\n"
  "v_mfma_f32_16x16x32_bf16 v[140:143], v[244:247], v[172:175], v[140:143]\n"
  "s_waitcnt lgkmcnt(14)\n"
  "ds_write_b128 v151, v[136:139] offset:25600\n"
  "v_mfma_f32_16x16x32_bf16 v[32:35], v[244:247], v[160:163], v[32:35]\n"
  "s_add_u32 s12, s86, 552960\n"
  "buffer_load_dwordx4 v[60:63], v248, s[20:23], s12 offen offset:0 sc1\n"
  "s_add_u32 s12, s86, 294912\n"
  "buffer_load_dwordx4 v[244:247], v248, s[20:23], s12 offen offset:1024 sc1\n"
  "s_waitcnt lgkmcnt(14)\n"
  "ds_write_b128 v148, v[68:71] offset:8192\n"
  "s_waitcnt lgkmcnt(14)\n"
  "ds_write_b128 v149, v[104:107] offset:17408\n"
  "s_waitcnt lgkmcnt(14)\n"
  "ds_write_b128 v151, v[140:143] offset:26624\n"
  "s_mul_i32 s12, s81, 27648\n"
  "s_add_u32 s12, s12, 36864\n"
  "v_mov_b32 v249, 1\n"
  "s_waitcnt lgkmcnt(14)\n"
  "ds_write_b32 v253, v249 offset:32\n"
  "v_add_u32 v148, s12, v248\n"
  "s_waitcnt lgkmcnt(14)\n"
  "ds_read_b128 v[144:147], v150 offset:48\n"
  "s_add_u32 s12, s86, 368640\n"
  "buffer_load_dwordx4 v[160:163], v248, s[20:23], s12 offen offset:0 sc1\n"
  "s_add_u32 s12, s86, 368640\n"
  "buffer_load_dwordx4 v[164:167], v248, s[20:23], s12 offen offset:1024 sc1\n"
  "s_add_u32 s12, s86, 405504\n"
  "buffer_load_dwordx4 v[168:171], v248, s[20:23], s12 offen offset:0 sc1\n"
  "s_add_u32 s12, s86, 405504\n"
  "buffer_load_dwordx4 v[172:175], v248, s[20:23], s12 offen offset:1024 sc1\n"
  "s_add_u32 s12, s86, 552960\n"
  "buffer_load_dwordx4 v[64:67], v248, s[20:23], s12 offen offset:1024 sc1\n"
  "s_add_u32 s12, s86, 589824\n"
  "buffer_load_dwordx4 v[68:71], v248, s[20:23], s12 offen offset:0 sc1\n"
  "s_waitcnt lgkmcnt(0)\n"
  "v_min3_u32 v144, v144, v145, v146\n"
  "v_min_u32 v144, v144, v147\n"
  "v_cmp_gt_u32 vcc, 1, v144\n"
  "s_cbranch_vccnz LSR_%=\n"
  "LSRB_%=:\n"
  "ds_read_b128 v[84:87], v148 offset:0\n"
  "ds_read_b128 v[88:91], v148 offset:9216\n"
  "ds_read_b128 v[92:95], v148 offset:18432\n"
  "ds_read_b128 v[96:99], v148 offset:1024\n"
  "ds_read_b128 v[100:103], v148 offset:10240\n"
  "ds_read_b128 v[104:107], v148 offset:19456\n"
  "ds_read_b128 v[108:111], v148 offset:2048\n"
  "ds_read_b128 v[112:115], v148 offset:11264\n"
  "ds_read_b128 v[116:119], v148 offset:20480\n"
  "ds_read_b128 v[120:123], v148 offset:3072\n"
  "ds_read_b128 v[124:127], v148 offset:12288\n"
  "ds_read_b128 v[128:131], v148 offset:21504\n"
  "ds_read_b128 v[132:135], v148 offset:4096\n"
  "ds_read_b128 v[136:139], v148 offset:13312\n"
  "ds_read_b128 v[140:143], v148 offset:22528\n"
  "v_mov_b32 v249, 7\n"
  "s_waitcnt lgkmcnt(14)\n"
  "v_add_f32 v0, v0, v84\n"
  "s_waitcnt lgkmcnt(12)\n"
  "v_add_f32 v88, v88, v92\n"
  "v_add_f32 v1, v1, v85\n"
  "v_add_f32 v89, v89, v93\n"
  "v_add_f32 v2, v2, v86\n"
  "v_add_f32 v90, v90, v94\n"
  "v_add_f32 v3, v3, v87\n"
  "v_add_f32 v91, v91, v95\n"
  "v_add_f32 v0, v0, v88\n"
  "v_add_f32 v1, v1, v89\n"
  "v_add_f32 v2, v2, v90\n"
  "v_add_f32 v3, v3, v91\n"
  "ds_read_b128 v[84:87], v148 offset:5120\n"
  "ds_read_b128 v[88:91], v148 offset:14336\n"
  "ds_read_b128 v[92:95], v148 offset:23552\n"
  "s_waitcnt lgkmcnt(14)\n"
  "v_add_f32 v4, v4, v96\n"
  "s_waitcnt lgkmcnt(12)\n"
  "v_add_f32 v100, v100, v104\n"
  "v_add_f32 v5, v5, v97\n"
  "v_add_f32 v101, v101, v105\n"
  "v_add_f32 v6, v6, v98\n"
  "v_add_f32 v102, v102, v106\n"
  "v_add_f32 v7, v7, v99\n"
  "v_add_f32 v103, v103, v107\n"
  "v_add_f32 v4, v4, v100\n"
  "v_add_f32 v5, v5, v101\n"
  "v_add_f32 v6, v6, v102\n"
  "v_add_f32 v7, v7, v103\n"
  "ds_read_b128 v[96:99], v148 offset:6144\n"
  "ds_read_b128 v[100:103], v148 offset:15360\n"
  "ds_read_b128 v[104:107], v148 offset:24576\n"
  "s_waitcnt lgkmcnt(14)\n"
  "v_add_f32 v8, v8, v108\n"
  "s_waitcnt lgkmcnt(12)\n"
  "v_add_f32 v112, v112, v116\n"
  "v_add_f32 v9, v9, v109\n"
  "v_add_f32 v113, v113, v117\n"
  "v_add_f32 v10, v10, v110\n"
  "v_add_f32 v114, v114, v118\n"
  "v_add_f32 v11, v11, v111\n"
  "v_add_f32 v115, v115, v119\n"
  "v_add_f32 v8, v8, v112\n"
  "v_add_f32 v9, v9, v113\n"
  "v_add_f32 v10, v10, v114\n"
  "v_add_f32 v11, v11, v115\n"
  "ds_read_b128 v[108:111], v148 offset:7168\n"
  "ds_read_b128 v[112:115], v148 offset:16384\n"
  "ds_read_b128 v[116:119], v148 offset:25600\n"
  "s_waitcnt lgkmcnt(14)\n"
  "v_add_f32 v12, v12, v120\n"
  "s_waitcnt lgkmcnt(12)\n"
  "v_add_f32 v124, v124, v128\n"
  "v_add_f32 v13, v13, v121\n"
  "v_add_f32 v125, v125, v129\n"
  "v_add_f32 v14, v14, v122\n"
  "v_add_f32 v126, v126, v130\n"
  "v_add_f32 v15, v15, v123\n"
  "v_add_f32 v127, v127, v131\n"
  "v_add_f32 v12, v12, v124\n"
  "v_add_f32 v13, v13, v125\n"
  "v_add_f32 v14, v14, v126\n"
  "v_add_f32 v15, v15, v127\n"
  "ds_read_b128 v[120:123], v148 offset:8192\n"
  "ds_read_b128 v[124:127], v148 offset:17408\n"
  "ds_read_b128 v[128:131], v148 offset:26624\n"
  "s_waitcnt lgkmcnt(14)\n"
  "v_add_f32 v16, v16, v132\n"
  "s_waitcnt lgkmcnt(12)\n"
  "v_add_f32 v136, v136, v140\n"
  "v_add_f32 v17, v17, v133\n"
  "v_add_f32 v137, v137, v141\n"
  "v_add_f32 v18, v18, v134\n"
  "v_add_f32 v138, v138, v142\n"
  "v_add_f32 v19, v19, v135\n"
  "v_add_f32 v139, v139, v143\n"
  "v_add_f32 v16, v16, v136\n"
  "v_add_f32 v17, v17, v137\n"
  "v_add_f32 v18, v18, v138\n"
  "v_add_f32 v19, v19, v139\n"
  "s_waitcnt lgkmcnt(11)\n"
  "v_add_f32 v20, v20, v84\n"
  "s_waitcnt lgkmcnt(9)\n"
  "v_add_f32 v88, v88, v92\n"
  "v_add_f32 v21, v21, v85\n"
  "v_add_f32 v89, v89, v93\n"
  "v_add_f32 v22, v22, v86\n"
  "v_add_f32 v90, v90, v94\n"
  "v_add_f32 v23, v23, v87\n"
  "v_add_f32 v91, v91, v95\n"
  "v_add_f32 v20, v20, v88\n"
  "v_add_f32 v21, v21, v89\n"
  "v_add_f32 v22, v22, v90\n"
  "v_add_f32 v23, v23, v91\n"
  "s_waitcnt lgkmcnt(8)\n"
  "v_add_f32 v24, v24, v96\n"
  "s_waitcnt lgkmcnt(6)\n"
  "v_add_f32 v100, v100, v104\n"
  "v_add_f32 v25, v25, v97\n"
  "v_add_f32 v101, v101, v105\n"
  "v_add_f32 v26, v26, v98\n"
  "v_add_f32 v102, v102, v106\n"
  "v_add_f32 v27, v27, v99\n"
  "v_add_f32 v103, v103, v107\n"
  "v_add_f32 v24, v24, v100\n"
  "v_add_f32 v25, v25, v101\n"
  "v_add_f32 v26, v26, v102\n"
  "v_add_f32 v27, v27, v103\n"
  "s_waitcnt lgkmcnt(5)\n"
  "v_add_f32 v28, v28, v108\n"
  "s_waitcnt lgkmcnt(3)\n"
  "v_add_f32 v112, v112, v116\n"
  "v_add_f32 v29, v29, v109\n"
  "v_add_f32 v113, v113, v117\n"
  "v_add_f32 v30, v30, v110\n"
  "v_add_f32 v114, v114, v118\n"
  "v_add_f32 v31, v31, v111\n"
  "v_add_f32 v115, v115, v119\n"
  "v_add_f32 v28, v28, v112\n"
  "v_add_f32 v29, v29, v113\n"
  "v_add_f32 v30, v30, v114\n"
  "v_add_f32 v31, v31, v115\n"
  "s_waitcnt lgkmcnt(2)\n"
  "v_add_f32 v32, v32, v120\n"
  "s_waitcnt lgkmcnt(0)\n"
  "v_add_f32 v124, v124, v128\n"
  "v_add_f32 v33, v33, v121\n"
  "v_add_f32 v125, v125, v129\n"
  "v_add_f32 v34, v34, v122\n"
  "v_add_f32 v126, v126, v130\n"
  "v_add_f32 v35, v35, v123\n"
  "v_add_f32 v127, v127, v131\n"
  "v_add_f32 v32, v32, v124\n"
  "v_add_f32 v33, v33, v125\n"
  "v_add_f32 v34, v34, v126\n"
  "v_add_f32 v35, v35, v127\n"
  "s_cmp_lg_u32 s84, 0\n"
  "s_cbranch_scc1 LKEEP_%=\n"
  "ds_write_b32 v253, v249 offset:32\n"
  "v_cvt_pk_bf16_f32 v108, v0, v1\n"
  "v_cvt_pk_bf16_f32 v109, v2, v3\n"
  "v_cvt_pk_bf16_f32 v110, v4, v5\n"
  "v_cvt_pk_bf16_f32 v111, v6, v7\n"
  "v_cvt_pk_bf16_f32 v112, v8, v9\n"
  "v_cvt_pk_bf16_f32 v113, v10, v11\n"
  "v_cvt_pk_bf16_f32 v114, v12, v13\n"
  "v_cvt_pk_bf16_f32 v115, v14, v15\n"
  "v_cvt_pk_bf16_f32 v116, v16, v17\n"
  "v_cvt_pk_bf16_f32 v117, v18, v19\n"
  "v_cvt_pk_bf16_f32 v118, v20, v21\n"
  "v_cvt_pk_bf16_f32 v119, v22, v23\n"
  "v_cvt_pk_bf16_f32 v120, v24, v25\n"
  "v_cvt_pk_bf16_f32 v121, v26, v27\n"
  "v_cvt_pk_bf16_f32 v122, v28, v29\n"
  "v_cvt_pk_bf16_f32 v123, v30, v31\n"
  "v_cvt_pk_bf16_f32 v124, v32, v33\n"
  "v_cvt_pk_bf16_f32 v125, v34, v35\n"
  "v_min_u32 v108, -2, v108\n"
  "v_min_u32 v109, -2, v109\n"
  "v_min_u32 v110, -2, v110\n"
  "v_min_u32 v111, -2, v111\n"
  "v_min_u32 v112, -2, v112\n"
  "v_min_u32 v113, -2, v113\n"
  "v_min_u32 v114, -2, v114\n"
  "v_min_u32 v115, -2, v115\n"
  "v_min_u32 v116, -2, v116\n"
  "v_min_u32 v117, -2, v117\n"
  "v_min_u32 v118, -2, v118\n"
  "v_min_u32 v119, -2, v119\n"
  "v_min_u32 v120, -2, v120\n"
  "v_min_u32 v121, -2, v121\n"
  "v_min_u32 v122, -2, v122\n"
  "v_min_u32 v123, -2, v123\n"
  "v_min_u32 v124, -2, v124\n"
  "v_min_u32 v125, -2, v125\n"
  "s_bitcmp1_b32 s88, 10\n"
  "s_cbranch_scc1 LEXO_%=\n"
  "buffer_store_dwordx4 v[108:111], v248, s[20:23], s83 offen offset:0 sc1\n"
  "buffer_store_dwordx4 v[112:115], v248, s[20:23], s83 offen offset:1024 sc1\n"
  "buffer_store_dwordx4 v[116:119], v248, s[20:23], s83 offen offset:2048 sc1\n"
  "buffer_store_dwordx4 v[120:123], v248, s[20:23], s83 offen offset:3072 sc1\n"
  "buffer_store_dwordx2 v[124:125], v255, s[20:23], s15 offen sc1\n"
  "s_branch LGATE_%=\n"
  "LEXO_%=:\n"
  "buffer_store_dwordx4 v[110:113], v248, s[20:23], s83 offen offset:0 sc1\n"
  "buffer_store_dwordx4 v[114:117], v248, s[20:23], s83 offen offset:1024 sc1\n"
  "buffer_store_dwordx4 v[118:121], v248, s[20:23], s83 offen offset:2048 sc1\n"
  "buffer_store_dwordx4 v[122:125], v248, s[20:23], s83 offen offset:3072 sc1\n"
  "buffer_store_dwordx2 v[108:109], v255, s[20:23], s15 offen sc1\n"
  "s_branch LGATE_%=\n"
  "LKEEP_%=:\n"
  "ds_write_b128 v148, v[0:3] offset:0\n"
  "ds_write_b128 v148, v[4:7] offset:1024\n"
  "ds_write_b128 v148, v[8:11] offset:2048\n"
  "ds_write_b128 v148, v[12:15] offset:3072\n"
  "ds_write_b128 v148, v[16:19] offset:4096\n"
  "ds_write_b128 v148, v[20:23] offset:5120\n"
  "ds_write_b128 v148, v[24:27] offset:6144\n"
  "ds_write_b128 v148, v[28:31] offset:7168\n"
  "ds_write_b128 v148, v[32:35] offset:8192\n"
  "ds_write_b32 v253, v249 offset:32\n"
  "LGATE_%=:\n"
  "s_nop 1\n"
  "s_setprio 2\n"
  "ds_read_b128 v[132:135], v150 offset:32\n"
  "v_bfe_u32 v149, v248, 4, 4\n"
  "v_mul_u32_u24 v149, 1152, v149\n"
  "v_lshrrev_b32 v144, 8, v248\n"
  "v_lshl_add_u32 v149, v144, 4, v149\n"
  "s_add_u32 s14, s87, 18432\n"
  "s_cmp_eq_u32 s89, 36864\n"
  "s_cselect_b32 s12, 9216, 0\n"
  "s_add_u32 s17, s83, s12\n"
  "s_mul_i32 s12, s81, 3584\n"
  "s_sub_u32 s17, s17, s12\n"
  "s_add_u32 s18, s17, 4608\n"
  "s_lshl_b32 s12, s81, 13\n"
  "s_add_u32 s13, s12, s89\n"
  "v_add_u32 v126, s13, v248\n"
  "s_add_u32 s8, s85, s12\n"
  "s_add_u32 s9, s8, 4096\n"
  "s_lshl_b32 s12, s81, 10\n"
  "s_add_u32 s10, s85, s12\n"
  "s_add_u32 s10, s10, 32768\n"
  "s_lshr_b32 s12, s12, 1\n"
  "s_add_u32 s12, s12, s89\n"
  "s_add_u32 s12, s12, 32768\n"
  "v_bfe_u32 v128, v248, 6, 1\n"
  "v_lshlrev_b32 v127, 11, v128\n"
  "v_lshrrev_b32 v128, 9, v248\n"
  "v_lshl_add_u32 v127, v128, 8, v127\n"
  "v_bfe_u32 v128, v248, 4, 2\n"
  "v_lshl_add_u32 v127, v128, 6, v127\n"
  "v_bfe_u32 v128, v248, 7, 2\n"
  "v_lshl_add_u32 v127, v128, 2, v127\n"
  "v_add_u32 v127, s12, v127\n"
  "v_add_u32 v148, s88, v248\n"
  "v_add_u32 v150, s89, v248\n"
  "s_movk_i32 s16, 0x3ff\n"
  "s_waitcnt lgkmcnt(0)\n"
  "v_min3_u32 v132, v132, v133, v134\n"
  "v_min_u32 v132, v132, v135\n"
  "v_cmp_gt_u32 vcc, 5, v132\n"
  "s_cbranch_vccnz LERL_%=\n"
  "s_setprio 2\n"
  "buffer_load_dwordx4 v[84:87], v248, s[20:23], s17 offen sc1\n"
  "buffer_load_dwordx4 v[88:91], v248, s[20:23], s18 offen sc1\n"
  "buffer_load_dwordx4 v[0:3], v248, s[20:23], s8 offen offset:0 sc1\n"
  "buffer_load_dwordx4 v[4:7], v248, s[20:23], s8 offen offset:1024 sc1\n"
  "buffer_load_dwordx4 v[8:11], v248, s[20:23], s8 offen offset:2048 sc1\n"
  "buffer_load_dwordx4 v[12:15], v248, s[20:23], s8 offen offset:3072 sc1\n"
  "buffer_load_dwordx4 v[16:19], v248, s[20:23], s9 offen offset:0 sc1\n"
  "buffer_load_dwordx4 v[20:23], v248, s[20:23], s9 offen offset:1024 sc1\n"
  "buffer_load_dwordx4 v[24:27], v248, s[20:23], s9 offen offset:2048 sc1\n"
  "buffer_load_dwordx4 v[28:31], v248, s[20:23], s9 offen offset:3072 sc1\n"
  "buffer_load_dwordx4 v[32:35], v248, s[20:23], s10 offen offset:0 sc1\n"
  "ds_read_b32 v251, v252 offset:32\n"
  "s_waitcnt lgkmcnt(0)\n"
  "v_cmp_gt_u32 vcc, 5, v251\n"
  "s_cbranch_vccnz LSZ_%=\n"
  "LSZB_%=:\n"
  "buffer_load_dwordx4 v[100:103], v149, s[92:95], s87 offen\n"
  "buffer_load_dwordx4 v[104:107], v149, s[92:95], s14 offen\n"
  "s_waitcnt vmcnt(10)\n"
  "v_max3_u32 v128, v0, v1, v2\n"
  "v_max_u32 v128, v128, v3\n"
  "v_cmp_eq_u32 vcc, -1, v128\n"
  "s_cbranch_vccnz LOM0_%=\n"
  "ds_write_b128 v126, v[0:3] offset:0\n"
  "s_bitset0_b32 s16, 0\n"
  "LOM0_%=:\n"
  "s_waitcnt vmcnt(9)\n"
  "v_max3_u32 v128, v4, v5, v6\n"
  "v_max_u32 v128, v128, v7\n"
  "v_cmp_eq_u32 vcc, -1, v128\n"
  "s_cbranch_vccnz LOM1_%=\n"
  "ds_write_b128 v126, v[4:7] offset:1024\n"
  "s_bitset0_b32 s16, 1\n"
  "LOM1_%=:\n"
  "s_waitcnt vmcnt(8)\n"
  "v_max3_u32 v128, v8, v9, v10\n"
  "v_max_u32 v128, v128, v11\n"
  "v_cmp_eq_u32 vcc, -1, v128\n"
  "s_cbranch_vccnz LOM2_%=\n"
  "ds_write_b128 v126, v[8:11] offset:2048\n"
  "s_bitset0_b32 s16, 2\n"
  "LOM2_%=:\n"
  "s_waitcnt vmcnt(7)\n"
  "v_max3_u32 v128, v12, v13, v14\n"
  "v_max_u32 v128, v128, v15\n"
  "v_cmp_eq_u32 vcc, -1, v128\n"
  "s_cbranch_vccnz LOM3_%=\n"
  "ds_write_b128 v126, v[12:15] offset:3072\n"
  "s_bitset0_b32 s16, 3\n"
  "LOM3_%=:\n"
  "s_waitcnt vmcnt(6)\n"
  "v_max3_u32 v128, v16, v17, v18\n"
  "v_max_u32 v128, v128, v19\n"
  "v_cmp_eq_u32 vcc, -1, v128\n"
  "s_cbranch_vccnz LOM4_%=\n"
  "ds_write_b128 v126, v[16:19] offset:4096\n"
  "s_bitset0_b32 s16, 4\n"
  "LOM4_%=:\n"
  "s_waitcnt vmcnt(5)\n"
  "v_max3_u32 v128, v20, v21, v22\n"
  "v_max_u32 v128, v128, v23\n"
  "v_cmp_eq_u32 vcc, -1, v128\n"
  "s_cbranch_vccnz LOM5_%=\n"
  "ds_write_b128 v126, v[20:23] offset:5120\n"
  "s_bitset0_b32 s16, 5\n"
  "LOM5_%=:\n"
  "s_waitcnt vmcnt(4)\n"
  "v_max3_u32 v128, v24, v25, v26\n"
  "v_max_u32 v128, v128, v27\n"
  "v_cmp_eq_u32 vcc, -1, v128\n"
  "s_cbranch_vccnz LOM6_%=\n"
  "ds_write_b128 v126, v[24:27] offset:6144\n"
  "s_bitset0_b32 s16, 6\n"
  "LOM6_%=:\n"
  "s_waitcnt vmcnt(3)\n"
  "v_max3_u32 v128, v28, v29, v30\n"
  "v_max_u32 v128, v128, v31\n"
  "v_cmp_eq_u32 vcc, -1, v128\n"
  "s_cbranch_vccnz LOM7_%=\n"
  "ds_write_b128 v126, v[28:31] offset:7168\n"
  "s_bitset0_b32 s16, 7\n"
  "LOM7_%=:\n"
  "s_waitcnt vmcnt(2)\n"
  "v_max3_u32 v128, v32, v33, v34\n"
  "v_max_u32 v128, v128, v35\n"
  "v_cmp_eq_u32 vcc, -1, v128\n"
  "s_cbranch_vccnz LOM8_%=\n"
  "ds_write2_b32 v127, v32, v33 offset0:0 offset1:4\n"
  "ds_write2_b32 v127, v34, v35 offset0:8 offset1:12\n"
  "s_bitset0_b32 s16, 8\n"
  "LOM8_%=:\n"
  "LOE_%=:\n"
  "s_and_b32 s12, s16, 0x1ff\n"
  "s_cbranch_scc1 LOS_%=\n"
  "LOD_%=:\n"
  "s_and_b32 s16, s16, 0x200\n"
  "s_cbranch_scc1 LYP_%=\n"
  "LYPB_%=:\n"
  "s_setprio 2\n"
  "s_waitcnt lgkmcnt(0)\n"
  "s_barrier\n"
  "ds_read_b128 v[0:3], v150 offset:0\n"
  "ds_read_b128 v[4:7], v150 offset:16384\n"
  "ds_read_b128 v[8:11], v150 offset:1024\n"
  "ds_read_b128 v[12:15], v150 offset:17408\n"
  "ds_read_b128 v[16:19], v150 offset:2048\n"
  "s_waitcnt lgkmcnt(4)\n"
  "v_mfma_f32_16x16x32_bf16 v[132:135], v[176:179], v[0:3], 0\n"
  "ds_read_b128 v[20:23], v150 offset:18432\n"
  "v_mfma_f32_16x16x32_bf16 v[136:139], v[180:183], v[0:3], 0\n"
  "s_waitcnt lgkmcnt(4)\n"
  "v_mfma_f32_16x16x32_bf16 v[140:143], v[176:179], v[4:7], 0\n"
  "ds_read_b128 v[0:3], v150 offset:3072\n"
  "v_mfma_f32_16x16x32_bf16 v[144:147], v[180:183], v[4:7], 0\n"
  "s_waitcnt lgkmcnt(4)\n"
  "v_mfma_f32_16x16x32_bf16 v[132:135], v[184:187], v[8:11], v[132:135]\n"
  "ds_read_b128 v[4:7], v150 offset:19456\n"
  "v_mfma_f32_16x16x32_bf16 v[136:139], v[188:191], v[8:11], v[136:139]\n"
  "s_waitcnt lgkmcnt(4)\n"
  "v_mfma_f32_16x16x32_bf16 v[140:143], v[184:187], v[12:15], v[140:143]\n"
  "ds_read_b128 v[8:11], v150 offset:4096\n"
  "v_mfma_f32_16x16x32_bf16 v[144:147], v[188:191], v[12:15], v[144:147]\n"
  "s_waitcnt lgkmcnt(4)\n"
  "v_mfma_f32_16x16x32_bf16 v[132:135], v[192:195], v[16:19], v[132:135]\n"
  "ds_read_b128 v[12:15], v150 offset:20480\n"
  "v_mfma_f32_16x16x32_bf16 v[136:139], v[196:199], v[16:19], v[136:139]\n"
  "s_waitcnt lgkmcnt(4)\n"
  "v_mfma_f32_16x16x32_bf16 v[140:143], v[192:195], v[20:23], v[140:143]\n"
  "ds_read_b128 v[16:19], v150 offset:5120\n"
  "v_mfma_f32_16x16x32_bf16 v[144:147], v[196:199], v[20:23], v[144:147]\n"
  "s_waitcnt lgkmcnt(4)\n"
  "v_mfma_f32_16x16x32_bf16 v[132:135], v[200:203], v[0:3], v[132:135]\n"
  "ds_read_b128 v[20:23], v150 offset:21504\n"
  "v_mfma_f32_16x16x32_bf16 v[136:139], v[204:207], v[0:3], v[136:139]\n"
  "s_waitcnt lgkmcnt(4)\n"
  "v_mfma_f32_16x16x32_bf16 v[140:143], v[200:203], v[4:7], v[140:143]\n"
  "ds_read_b128 v[0:3], v150 offset:6144\n"
  "v_mfma_f32_16x16x32_bf16 v[144:147], v[204:207], v[4:7], v[144:147]\n"
  "s_waitcnt lgkmcnt(4)\n"
  "v_mfma_f32_16x16x32_bf16 v[132:135], v[208:211], v[8:11], v[132:135]\n"
  "ds_read_b128 v[4:7], v150 offset:22528\n"
  "v_mfma_f32_16x16x32_bf16 v[136:139], v[212:215], v[8:11], v[136:139]\n"
  "s_waitcnt lgkmcnt(4)\n"
  "v_mfma_f32_16x16x32_bf16 v[140:143], v[208:211], v[12:15], v[140:143]\n"
  "ds_read_b128 v[8:11], v150 offset:7168\n"
  "v_mfma_f32_16x16x32_bf16 v[144:147], v[212:215], v[12:15], v[144:147]\n"
  "s_waitcnt lgkmcnt(4)\n"
  "v_mfma_f32_16x16x32_bf16 v[132:135], v[216:219], v[16:19], v[132:135]\n"
  "ds_read_b128 v[12:15], v150 offset:23552\n"
  "v_mfma_f32_16x16x32_bf16 v[136:139], v[220:223], v[16:19], v[136:139]\n"
  "s_waitcnt lgkmcnt(4)\n"
  "v_mfma_f32_16x16x32_bf16 v[140:143], v[216:219], v[20:23], v[140:143]\n"
  "ds_read_b128 v[16:19], v150 offset:8192\n"
  "v_mfma_f32_16x16x32_bf16 v[144:147], v[220:223], v[20:23], v[144:147]\n"
  "s_waitcnt lgkmcnt(4)\n"
  "v_mfma_f32_16x16x32_bf16 v[132:135], v[224:227], v[0:3], v[132:135]\n"
  "ds_read_b128 v[20:23], v150 offset:24576\n"
  "v_mfma_f32_16x16x32_bf16 v[136:139], v[228:231], v[0:3], v[136:139]\n"
  "s_waitcnt lgkmcnt(4)\n"
  "v_mfma_f32_16x16x32_bf16 v[140:143], v[224:227], v[4:7], v[140:143]\n"
  "ds_read_b128 v[0:3], v150 offset:9216\n"
  "v_mfma_f32_16x16x32_bf16 v[144:147], v[228:231], v[4:7], v[144:147]\n"
  "s_waitcnt lgkmcnt(4)\n"
  "v_mfma_f32_16x16x32_bf16 v[132:135], v[232:235], v[8:11], v[132:135]\n"
  "ds_read_b128 v[4:7], v150 offset:25600\n"
  "v_mfma_f32_16x16x32_bf16 v[136:139], v[236:239], v[8:11], v[136:139]\n"
  "s_waitcnt lgkmcnt(4)\n"
  "v_mfma_f32_16x16x32_bf16 v[140:143], v[232:235], v[12:15], v[140:143]\n"
  "ds_read_b128 v[8:11], v150 offset:10240\n"
  "v_mfma_f32_16x16x32_bf16 v[144:147], v[236:239], v[12:15], v[144:147]\n"
  "s_waitcnt lgkmcnt(4)\n"
  "v_mfma_f32_16x16x32_bf16 v[132:135], v[240:243], v[16:19], v[132:135]\n"
  "ds_read_b128 v[12:15], v150 offset:26624\n"
  "v_mfma_f32_16x16x32_bf16 v[136:139], v[244:247], v[16:19], v[136:139]\n"
  "s_waitcnt lgkmcnt(4)\n"
  "v_mfma_f32_16x16x32_bf16 v[140:143], v[240:243], v[20:23], v[140:143]\n"
  "ds_read_b128 v[16:19], v150 offset:11264\n"
  "v_mfma_f32_16x16x32_bf16 v[144:147], v[244:247], v[20:23], v[144:147]\n"
  "s_waitcnt lgkmcnt(4)\n"
  "v_mfma_f32_16x16x32_bf16 v[132:135], v[152:155], v[0:3], v[132:135]\n"
  "ds_read_b128 v[20:23], v150 offset:27648\n"
  "v_mfma_f32_16x16x32_bf16 v[136:139], v[156:159], v[0:3], v[136:139]\n"
  "s_waitcnt lgkmcnt(4)\n"
  "v_mfma_f32_16x16x32_bf16 v[140:143], v[152:155], v[4:7], v[140:143]\n"
  "ds_read_b128 v[0:3], v150 offset:12288\n"
  "v_mfma_f32_16x16x32_bf16 v[144:147], v[156:159], v[4:7], v[144:147]\n"
  "s_waitcnt lgkmcnt(4)\n"
  "v_mfma_f32_16x16x32_bf16 v[132:135], v[160:163], v[8:11], v[132:135]\n"
  "ds_read_b128 v[4:7], v150 offset:28672\n"
  "v_mfma_f32_16x16x32_bf16 v[136:139], v[164:167], v[8:11], v[136:139]\n"
  "s_waitcnt lgkmcnt(4)\n"
  "v_mfma_f32_16x16x32_bf16 v[140:143], v[160:163], v[12:15], v[140:143]\n"
  "ds_read_b128 v[8:11], v150 offset:13312\n"
  "v_mfma_f32_16x16x32_bf16 v[144:147], v[164:167], v[12:15], v[144:147]\n"
  "s_waitcnt lgkmcnt(4)\n"
  "v_mfma_f32_16x16x32_bf16 v[132:135], v[168:171], v[16:19], v[132:135]\n"
  "ds_read_b128 v[12:15], v150 offset:29696\n"
  "v_mfma_f32_16x16x32_bf16 v[136:139], v[172:175], v[16:19], v[136:139]\n"
  "s_waitcnt lgkmcnt(4)\n"
  "v_mfma_f32_16x16x32_bf16 v[140:143], v[168:171], v[20:23], v[140:143]\n"
  "ds_read_b128 v[16:19], v150 offset:14336\n"
  "v_mfma_f32_16x16x32_bf16 v[144:147], v[172:175], v[20:23], v[144:147]\n"
  "s_waitcnt lgkmcnt(4)\n"
  "v_mfma_f32_16x16x32_bf16 v[132:135], v[36:39], v[0:3], v[132:135]\n"
  "ds_read_b128 v[20:23], v150 offset:30720\n"
  "v_mfma_f32_16x16x32_bf16 v[136:139], v[40:43], v[0:3], v[136:139]\n"
  "s_waitcnt lgkmcnt(4)\n"
  "v_mfma_f32_16x16x32_bf16 v[140:143], v[36:39], v[4:7], v[140:143]\n"
  "ds_read_b128 v[0:3], v150 offset:15360\n"
  "v_mfma_f32_16x16x32_bf16 v[144:147], v[40:43], v[4:7], v[144:147]\n"
  "s_waitcnt lgkmcnt(4)\n"
  "v_mfma_f32_16x16x32_bf16 v[132:135], v[44:47], v[8:11], v[132:135]\n"
  "ds_read_b128 v[4:7], v150 offset:31744\n"
  "v_mfma_f32_16x16x32_bf16 v[136:139], v[48:51], v[8:11], v[136:139]\n"
  "s_waitcnt lgkmcnt(4)\n"
  "v_mfma_f32_16x16x32_bf16 v[140:143], v[44:47], v[12:15], v[140:143]\n"
  "ds_read_b128 v[8:11], v150 offset:32768\n"
  "v_mfma_f32_16x16x32_bf16 v[144:147], v[48:51], v[12:15], v[144:147]\n"
  "s_waitcnt lgkmcnt(4)\n"
  "v_mfma_f32_16x16x32_bf16 v[132:135], v[52:55], v[16:19], v[132:135]\n"
  "ds_read_b128 v[12:15], v150 offset:34816\n"
  "v_mfma_f32_16x16x32_bf16 v[136:139], v[56:59], v[16:19], v[136:139]\n"
  "s_waitcnt lgkmcnt(4)\n"
  "v_mfma_f32_16x16x32_bf16 v[140:143], v[52:55], v[20:23], v[140:143]\n"
  "ds_read_b128 v[16:19], v150 offset:33792\n"
  "v_mfma_f32_16x16x32_bf16 v[144:147], v[56:59], v[20:23], v[144:147]\n"
  "s_waitcnt lgkmcnt(4)\n"
  "v_mfma_f32_16x16x32_bf16 v[132:135], v[60:63], v[0:3], v[132:135]\n"
  "ds_read_b128 v[20:23], v150 offset:35840\n"
  "v_mfma_f32_16x16x32_bf16 v[136:139], v[64:67], v[0:3], v[136:139]\n"
  "s_waitcnt lgkmcnt(4)\n"
  "v_mfma_f32_16x16x32_bf16 v[140:143], v[60:63], v[4:7], v[140:143]\n"
  "v_mfma_f32_16x16x32_bf16 v[144:147], v[64:67], v[4:7], v[144:147]\n"
  "s_waitcnt lgkmcnt(3)\n"
  "v_mfma_f32_16x16x32_bf16 v[132:135], v[68:71], v[8:11], v[132:135]\n"
  "s_waitcnt vmcnt(1)\n"
  "v_lshlrev_b32 v92, 16, v100\n"
  "v_and_b32 v93, 0xffff0000, v100\n"
  "v_mfma_f32_16x16x32_bf16 v[136:139], v[72:75], v[8:11], v[136:139]\n"
  "v_lshlrev_b32 v94, 16, v101\n"
  "v_and_b32 v95, 0xffff0000, v101\n"
  "s_waitcnt lgkmcnt(2)\n"
  "v_mfma_f32_16x16x32_bf16 v[140:143], v[68:71], v[12:15], v[140:143]\n"
  "v_lshlrev_b32 v96, 16, v102\n"
  "v_and_b32 v97, 0xffff0000, v102\n"
  "v_mfma_f32_16x16x32_bf16 v[144:147], v[72:75], v[12:15], v[144:147]\n"
  "v_lshlrev_b32 v98, 16, v103\n"
  "v_and_b32 v99, 0xffff0000, v103\n"
  "s_waitcnt lgkmcnt(1)\n"
  "v_mfma_f32_16x16x32_bf16 v[132:135], v[76:79], v[16:19], v[132:135]\n"
  "s_waitcnt vmcnt(0)\n"
  "v_lshlrev_b32 v100, 16, v104\n"
  "v_and_b32 v101, 0xffff0000, v104\n"
  "v_mfma_f32_16x16x32_bf16 v[136:139], v[80:83], v[16:19], v[136:139]\n"
  "v_lshlrev_b32 v102, 16, v105\n"
  "v_and_b32 v103, 0xffff0000, v105\n"
  "s_waitcnt lgkmcnt(0)\n"
  "v_mfma_f32_16x16x32_bf16 v[140:143], v[76:79], v[20:23], v[140:143]\n"
  "v_lshlrev_b32 v104, 16, v106\n"
  "v_and_b32 v105, 0xffff0000, v106\n"
  "v_mfma_f32_16x16x32_bf16 v[144:147], v[80:83], v[20:23], v[144:147]\n"
  "v_lshlrev_b32 v106, 16, v107\n"
  "v_and_b32 v107, 0xffff0000, v107\n"
  "s_cmp_lg_u32 s16, 0\n"
  "s_cbranch_scc1 LYL_%=\n"
  "LFN_%=:\n"
  "v_fmac_f32 v92, v132, v108\n"
  "v_fmac_f32 v93, v133, v109\n"
  "v_fmac_f32 v94, v134, v110\n"
  "v_fmac_f32 v95, v135, v111\n"
  "v_fmac_f32 v96, v136, v112\n"
  "v_fmac_f32 v97, v137, v113\n"
  "v_fmac_f32 v98, v138, v114\n"
  "v_fmac_f32 v99, v139, v115\n"
  "v_cvt_pk_bf16_f32 v84, v92, v93\n"
  "v_cvt_pk_bf16_f32 v85, v94, v95\n"
  "v_cvt_pk_bf16_f32 v86, v96, v97\n"
  "v_cvt_pk_bf16_f32 v87, v98, v99\n"
  "buffer_store_dwordx4 v[84:87], v149, s[96:99], s87 offen\n"
  "v_fmac_f32 v100, v140, v116\n"
  "v_fmac_f32 v101, v141, v117\n"
  "v_fmac_f32 v102, v142, v118\n"
  "v_fmac_f32 v103, v143, v119\n"
  "v_fmac_f32 v104, v144, v120\n"
  "v_fmac_f32 v105, v145, v121\n"
  "v_fmac_f32 v106, v146, v122\n"
  "v_fmac_f32 v107, v147, v123\n"
  "v_cvt_pk_bf16_f32 v88, v100, v101\n"
  "v_cvt_pk_bf16_f32 v89, v102, v103\n"
  "v_cvt_pk_bf16_f32 v90, v104, v105\n"
  "v_cvt_pk_bf16_f32 v91, v106, v107\n"
  "buffer_store_dwordx4 v[88:91], v149, s[96:99], s14 offen\n"
  "s_branch LEND_%=\n"
  "LSB_%=:\n"
  "s_cmp_eq_u32 s46, 0\n"
  "s_cbranch_scc1 LSBB_%=\n"
  "s_sub_u32 s46, s46, 1\n"
  "s_sleep 2\n"
  "buffer_load_dword v251, v250, s[20:23], s80 offen sc1\n"
  "s_waitcnt vmcnt(0) lgkmcnt(0)\n"
  "v_cmp_ne_u32 vcc, s47, v251\n"
  "s_cbranch_vccnz LSB_%=\n"
  "s_branch LSBB_%=\n"
  "LSE_%=:\n"
  "s_cmp_eq_u32 s46, 0\n"
  "s_cbranch_scc1 LSEB_%=\n"
  "s_sub_u32 s46, s46, 1\n"
  "s_sleep 1\n"
  "ds_read_b32 v251, v252\n"
  "s_waitcnt lgkmcnt(0)\n"
  "v_cmp_gt_u32 vcc, 1, v251\n"
  "s_cbranch_vccnz LSE_%=\n"
  "s_branch LSEB_%=\n"
  "LSX_%=:\n"
  "s_cmp_eq_u32 s46, 0\n"
  "s_cbranch_scc1 LSXB_%=\n"
  "s_sub_u32 s46, s46, 1\n"
  "s_sleep 2\n"
  "buffer_load_dword v251, v255, s[20:23], s80 offen sc1\n"
  "s_waitcnt vmcnt(0) lgkmcnt(0)\n"
  "v_cmp_ne_u32 vcc, s47, v251\n"
  "s_cbranch_vccnz LSX_%=\n"
  "s_branch LSXB_%=\n"
  "LS9_%=:\n"
  "s_cmp_eq_u32 s46, 0\n"
  "s_cbranch_scc1 LS9B_%=\n"
  "s_sub_u32 s46, s46, 1\n"
  "s_sleep 2\n"
  "ds_read_b32 v249, v252 offset:32\n"
  "s_waitcnt lgkmcnt(0)\n"
  "v_cmp_gt_u32 vcc, 2, v249\n"
  "s_and_b64 vcc, vcc, 15\n"
  "s_cbranch_vccnz LS9_%=\n"
  "s_branch LS9B_%=\n"
  "LSP_%=:\n"
  "s_cmp_eq_u32 s46, 0\n"
  "s_cbranch_scc1 LSPB_%=\n"
  "s_sub_u32 s46, s46, 1\n"
  "s_sleep 2\n"
  "ds_read_b128 v[144:147], v150 offset:32\n"
  "s_waitcnt lgkmcnt(0)\n"
  "v_min3_u32 v144, v144, v145, v146\n"
  "v_min_u32 v144, v144, v147\n"
  "v_cmp_gt_u32 vcc, 3, v144\n"
  "s_cbranch_vccnz LSP_%=\n"
  "s_branch LSPB_%=\n"
  "LSR_%=:\n"
  "s_cmp_eq_u32 s46, 0\n"
  "s_cbranch_scc1 LSRB_%=\n"
  "s_sub_u32 s46, s46, 1\n"
  "s_sleep 1\n"
  "ds_read_b128 v[144:147], v150 offset:48\n"
  "s_waitcnt lgkmcnt(0)\n"
  "v_min3_u32 v144, v144, v145, v146\n"
  "v_min_u32 v144, v144, v147\n"
  "v_cmp_gt_u32 vcc, 1, v144\n"
  "s_cbranch_vccnz LSR_%=\n"
  "s_branch LSRB_%=\n"
  "LSZ_%=:\n"
  "s_cmp_eq_u32 s46, 0\n"
  "s_cbranch_scc1 LSZB_%=\n"
  "s_sub_u32 s46, s46, 1\n"
  "s_sleep 1\n"
  "ds_read_b32 v251, v252 offset:32\n"
  "s_waitcnt lgkmcnt(0)\n"
  "v_cmp_gt_u32 vcc, 5, v251\n"
  "s_cbranch_vccnz LSZ_%=\n"
  "s_branch LSZB_%=\n"
  "LERL_%=:\n"
  "buffer_load_dwordx4 v[100:103], v149, s[92:95], s87 offen\n"
  "buffer_load_dwordx4 v[104:107], v149, s[92:95], s14 offen\n"
  "buffer_load_dwordx4 v[84:87], v248, s[20:23], s17 offen sc1\n"
  "buffer_load_dwordx4 v[88:91], v248, s[20:23], s18 offen sc1\n"
  "s_bitset1_b32 s16, 10\n"
  "s_bfe_u32 s12, s69, 0x2000c\n"
  "v_lshrrev_b32 v140, 6, v248\n"
  "v_lshlrev_b32 v140, 7, v140\n"
  "s_lshl_b32 s13, s12, 11\n"
  "v_add_u32 v140, s13, v140\n"
  "v_lshrrev_b32 v141, 9, v248\n"
  "v_lshlrev_b32 v141, 7, v141\n"
  "s_lshl_b32 s13, s12, 8\n"
  "v_add_u32 v141, s13, v141\n"
  "LSG_%=:\n"
  "s_cmp_eq_u32 s46, 0\n"
  "s_cbranch_scc1 LSGB_%=\n"
  "s_sub_u32 s46, s46, 1\n"
  "s_sleep 1\n"
  "ds_read_b32 v251, v252 offset:32\n"
  "s_waitcnt lgkmcnt(0)\n"
  "v_cmp_gt_u32 vcc, 5, v251\n"
  "s_and_b64 vcc, vcc, 0xf0\n"
  "s_cbranch_vccnz LSG_%=\n"
  "s_branch LSGB_%=\n"
  "LSGB_%=:\n"
  "s_setprio 2\n"
  "s_branch LW3_%=\n"
  "LW1_%=:\n"
  "s_waitcnt vmcnt(0)\n"
  "s_bitcmp1_b32 s16, 9\n"
  "s_cbranch_scc0 LHO2_%=\n"
  "v_max3_u32 v128, v84, v85, v86\n"
  "v_max3_u32 v128, v128, v87, v88\n"
  "v_max3_u32 v128, v128, v89, v90\n"
  "v_max_u32 v128, v128, v91\n"
  "v_cmp_eq_u32 vcc, -1, v128\n"
  "s_cbranch_vccz LSIL_%=\n"
  "buffer_load_dwordx4 v[84:87], v248, s[20:23], s17 offen sc1\n"
  "buffer_load_dwordx4 v[88:91], v248, s[20:23], s18 offen sc1\n"
  "LHO2_%=:\n"
  "LW3_%=:\n"
  "buffer_load_dword v142, v140, s[20:23], s8 offen sc1\n"
  "buffer_load_dword v143, v141, s[20:23], s10 offen sc1\n"
  "s_mov_b32 s19, 3\n"
  "LW2_%=:\n"
  "ds_read_b32 v251, v252 offset:32\n"
  "s_waitcnt lgkmcnt(0)\n"
  "v_cmp_gt_u32 vcc, 5, v251\n"
  "s_cbranch_vccz LW4_%=\n"
  "s_sleep 1\n"
  "s_sub_u32 s19, s19, 1\n"
  "s_cbranch_scc0 LW2_%=\n"
  "s_sub_u32 s46, s46, 1\n"
  "s_cbranch_scc0 LW1_%=\n"
  "s_mov_b32 s46, 0\n"
  "LW4_%=:\n"
  "s_setprio 2\n"
  "buffer_load_dwordx4 v[0:3], v248, s[20:23], s8 offen offset:0 sc1\n"
  "buffer_load_dwordx4 v[4:7], v248, s[20:23], s8 offen offset:1024 sc1\n"
  "buffer_load_dwordx4 v[8:11], v248, s[20:23], s8 offen offset:2048 sc1\n"
  "buffer_load_dwordx4 v[12:15], v248, s[20:23], s8 offen offset:3072 sc1\n"
  "buffer_load_dwordx4 v[16:19], v248, s[20:23], s9 offen offset:0 sc1\n"
  "buffer_load_dwordx4 v[20:23], v248, s[20:23], s9 offen offset:1024 sc1\n"
  "buffer_load_dwordx4 v[24:27], v248, s[20:23], s9 offen offset:2048 sc1\n"
  "buffer_load_dwordx4 v[28:31], v248, s[20:23], s9 offen offset:3072 sc1\n"
  "buffer_load_dwordx4 v[32:35], v248, s[20:23], s10 offen offset:0 sc1\n"
  "s_bitset0_b32 s16, 10\n"
  "s_bitcmp1_b32 s16, 9\n"
  "s_cbranch_scc0 LOW_%=\n"
  "buffer_load_dwordx4 v[84:87], v248, s[20:23], s17 offen sc1\n"
  "buffer_load_dwordx4 v[88:91], v248, s[20:23], s18 offen sc1\n"
  "s_branch LOW_%=\n"
  "LOP_%=:\n"
  "s_bitcmp1_b32 s16, 0\n"
  "s_cbranch_scc0 LON0_%=\n"
  "v_max3_u32 v128, v0, v1, v2\n"
  "v_max_u32 v128, v128, v3\n"
  "v_cmp_eq_u32 vcc, -1, v128\n"
  "s_cbranch_vccnz LON0_%=\n"
  "ds_write_b128 v126, v[0:3] offset:0\n"
  "s_bitset0_b32 s16, 0\n"
  "LON0_%=:\n"
  "s_bitcmp1_b32 s16, 1\n"
  "s_cbranch_scc0 LON1_%=\n"
  "v_max3_u32 v128, v4, v5, v6\n"
  "v_max_u32 v128, v128, v7\n"
  "v_cmp_eq_u32 vcc, -1, v128\n"
  "s_cbranch_vccnz LON1_%=\n"
  "ds_write_b128 v126, v[4:7] offset:1024\n"
  "s_bitset0_b32 s16, 1\n"
  "LON1_%=:\n"
  "s_bitcmp1_b32 s16, 2\n"
  "s_cbranch_scc0 LON2_%=\n"
  "v_max3_u32 v128, v8, v9, v10\n"
  "v_max_u32 v128, v128, v11\n"
  "v_cmp_eq_u32 vcc, -1, v128\n"
  "s_cbranch_vccnz LON2_%=\n"
  "ds_write_b128 v126, v[8:11] offset:2048\n"
  "s_bitset0_b32 s16, 2\n"
  "LON2_%=:\n"
  "s_bitcmp1_b32 s16, 3\n"
  "s_cbranch_scc0 LON3_%=\n"
  "v_max3_u32 v128, v12, v13, v14\n"
  "v_max_u32 v128, v128, v15\n"
  "v_cmp_eq_u32 vcc, -1, v128\n"
  "s_cbranch_vccnz LON3_%=\n"
  "ds_write_b128 v126, v[12:15] offset:3072\n"
  "s_bitset0_b32 s16, 3\n"
  "LON3_%=:\n"
  "s_bitcmp1_b32 s16, 4\n"
  "s_cbranch_scc0 LON4_%=\n"
  "v_max3_u32 v128, v16, v17, v18\n"
  "v_max_u32 v128, v128, v19\n"
  "v_cmp_eq_u32 vcc, -1, v128\n"
  "s_cbranch_vccnz LON4_%=\n"
  "ds_write_b128 v126, v[16:19] offset:4096\n"
  "s_bitset0_b32 s16, 4\n"
  "LON4_%=:\n"
  "s_bitcmp1_b32 s16, 5\n"
  "s_cbranch_scc0 LON5_%=\n"
  "v_max3_u32 v128, v20, v21, v22\n"
  "v_max_u32 v128, v128, v23\n"
  "v_cmp_eq_u32 vcc, -1, v128\n"
  "s_cbranch_vccnz LON5_%=\n"
  "ds_write_b128 v126, v[20:23] offset:5120\n"
  "s_bitset0_b32 s16, 5\n"
  "LON5_%=:\n"
  "s_bitcmp1_b32 s16, 6\n"
  "s_cbranch_scc0 LON6_%=\n"
  "v_max3_u32 v128, v24, v25, v26\n"
  "v_max_u32 v128, v128, v27\n"
  "v_cmp_eq_u32 vcc, -1, v128\n"
  "s_cbranch_vccnz LON6_%=\n"
  "ds_write_b128 v126, v[24:27] offset:6144\n"
  "s_bitset0_b32 s16, 6\n"
  "LON6_%=:\n"
  "s_bitcmp1_b32 s16, 7\n"
  "s_cbranch_scc0 LON7_%=\n"
  "v_max3_u32 v128, v28, v29, v30\n"
  "v_max_u32 v128, v128, v31\n"
  "v_cmp_eq_u32 vcc, -1, v128\n"
  "s_cbranch_vccnz LON7_%=\n"
  "ds_write_b128 v126, v[28:31] offset:7168\n"
  "s_bitset0_b32 s16, 7\n"
  "LON7_%=:\n"
  "s_bitcmp1_b32 s16, 8\n"
  "s_cbranch_scc0 LON8_%=\n"
  "v_max3_u32 v128, v32, v33, v34\n"
  "v_max_u32 v128, v128, v35\n"
  "v_cmp_eq_u32 vcc, -1, v128\n"
  "s_cbranch_vccnz LON8_%=\n"
  "ds_write2_b32 v127, v32, v33 offset0:0 offset1:4\n"
  "ds_write2_b32 v127, v34, v35 offset0:8 offset1:12\n"
  "s_bitset0_b32 s16, 8\n"
  "LON8_%=:\n"
  "s_and_b32 s12, s16, 0x1ff\n"
  "s_cbranch_scc0 LOD_%=\n"
  "LOS_%=:\n"
  "s_bitcmp1_b32 s16, 0\n"
  "s_cbranch_scc0 LOR0_%=\n"
  "buffer_load_dwordx4 v[0:3], v248, s[20:23], s8 offen offset:0 sc1\n"
  "LOR0_%=:\n"
  "s_bitcmp1_b32 s16, 1\n"
  "s_cbranch_scc0 LOR1_%=\n"
  "buffer_load_dwordx4 v[4:7], v248, s[20:23], s8 offen offset:1024 sc1\n"
  "LOR1_%=:\n"
  "s_bitcmp1_b32 s16, 2\n"
  "s_cbranch_scc0 LOR2_%=\n"
  "buffer_load_dwordx4 v[8:11], v248, s[20:23], s8 offen offset:2048 sc1\n"
  "LOR2_%=:\n"
  "s_bitcmp1_b32 s16, 3\n"
  "s_cbranch_scc0 LOR3_%=\n"
  "buffer_load_dwordx4 v[12:15], v248, s[20:23], s8 offen offset:3072 sc1\n"
  "LOR3_%=:\n"
  "s_bitcmp1_b32 s16, 4\n"
  "s_cbranch_scc0 LOR4_%=\n"
  "buffer_load_dwordx4 v[16:19], v248, s[20:23], s9 offen offset:0 sc1\n"
  "LOR4_%=:\n"
  "s_bitcmp1_b32 s16, 5\n"
  "s_cbranch_scc0 LOR5_%=\n"
  "buffer_load_dwordx4 v[20:23], v248, s[20:23], s9 offen offset:1024 sc1\n"
  "LOR5_%=:\n"
  "s_bitcmp1_b32 s16, 6\n"
  "s_cbranch_scc0 LOR6_%=\n"
  "buffer_load_dwordx4 v[24:27], v248, s[20:23], s9 offen offset:2048 sc1\n"
  "LOR6_%=:\n"
  "s_bitcmp1_b32 s16, 7\n"
  "s_cbranch_scc0 LOR7_%=\n"
  "buffer_load_dwordx4 v[28:31], v248, s[20:23], s9 offen offset:3072 sc1\n"
  "LOR7_%=:\n"
  "s_bitcmp1_b32 s16, 8\n"
  "s_cbranch_scc0 LOR8_%=\n"
  "buffer_load_dwordx4 v[32:35], v248, s[20:23], s10 offen offset:0 sc1\n"
  "LOR8_%=:\n"
  "s_bitcmp1_b32 s16, 9\n"
  "s_cbranch_scc0 LOW_%=\n"
  "v_max3_u32 v128, v84, v85, v86\n"
  "v_max3_u32 v128, v128, v87, v88\n"
  "v_max3_u32 v128, v128, v89, v90\n"
  "v_max_u32 v128, v128, v91\n"
  "v_cmp_eq_u32 vcc, -1, v128\n"
  "s_cbranch_vccz LSIL_%=\n"
  "buffer_load_dwordx4 v[84:87], v248, s[20:23], s17 offen sc1\n"
  "buffer_load_dwordx4 v[88:91], v248, s[20:23], s18 offen sc1\n"
  "LOW_%=:\n"
  "s_waitcnt vmcnt(0)\n"
  "s_cmp_eq_u32 s46, 0\n"
  "s_cbranch_scc1 LOD_%=\n"
  "s_sub_u32 s46, s46, 1\n"
  "s_branch LOP_%=\n"
  "LYP_%=:\n"
  "v_max3_u32 v128, v84, v85, v86\n"
  "v_max3_u32 v128, v128, v87, v88\n"
  "v_max3_u32 v128, v128, v89, v90\n"
  "v_max_u32 v128, v128, v91\n"
  "v_cmp_eq_u32 vcc, -1, v128\n"
  "s_bitset1_b32 s16, 11\n"
  "s_cbranch_vccz LSIL_%=\n"
  "s_bitset0_b32 s16, 11\n"
  "buffer_load_dwordx4 v[84:87], v248, s[20:23], s17 offen sc1\n"
  "buffer_load_dwordx4 v[88:91], v248, s[20:23], s18 offen sc1\n"
  "s_branch LYPB_%=\n"
  "LSY_%=:\n"
  "s_cmp_eq_u32 s46, 0\n"
  "s_cbranch_scc1 LSYB_%=\n"
  "s_sub_u32 s46, s46, 1\n"
  "s_sleep 1\n"
  "buffer_load_dwordx4 v[84:87], v248, s[20:23], s17 offen sc1\n"
  "buffer_load_dwordx4 v[88:91], v248, s[20:23], s18 offen sc1\n"
  "s_waitcnt vmcnt(0) lgkmcnt(0)\n"
  "v_max3_u32 v128, v84, v85, v86\n"
  "v_max3_u32 v128, v128, v87, v88\n"
  "v_max3_u32 v128, v128, v89, v90\n"
  "v_max_u32 v128, v128, v91\n"
  "v_cmp_eq_u32 vcc, -1, v128\n"
  "s_cbranch_vccnz LSY_%=\n"
  "s_branch LSYB_%=\n"
  "LYL_%=:\n"
  "s_waitcnt vmcnt(0)\n"
  "v_max3_u32 v128, v84, v85, v86\n"
  "v_max3_u32 v128, v128, v87, v88\n"
  "v_max3_u32 v128, v128, v89, v90\n"
  "v_max_u32 v128, v128, v91\n"
  "v_cmp_eq_u32 vcc, -1, v128\n"
  "s_cbranch_vccnz LSY_%=\n"
  "LSYB_%=:\n"
  "LSIL_%=:\n"
  "ds_read_b128 v[108:111], v148 offset:0\n"
  "ds_read_b128 v[112:115], v148 offset:1024\n"
  "ds_read_b128 v[116:119], v148 offset:27648\n"
  "ds_read_b128 v[120:123], v148 offset:28672\n"
  "v_lshlrev_b32 v128, 16, v84\n"
  "v_and_b32 v129, 0xffff0000, v84\n"
  "v_lshlrev_b32 v130, 16, v85\n"
  "v_and_b32 v131, 0xffff0000, v85\n"
  "v_lshlrev_b32 v124, 16, v86\n"
  "v_and_b32 v125, 0xffff0000, v86\n"
  "v_lshlrev_b32 v249, 16, v87\n"
  "v_and_b32 v253, 0xffff0000, v87\n"
  "s_waitcnt lgkmcnt(3)\n"
  "v_add_f32 v108, v108, v128\n"
  "v_add_f32 v109, v109, v129\n"
  "v_add_f32 v110, v110, v130\n"
  "v_add_f32 v111, v111, v131\n"
  "s_waitcnt lgkmcnt(2)\n"
  "v_add_f32 v112, v112, v124\n"
  "v_add_f32 v113, v113, v125\n"
  "v_add_f32 v114, v114, v249\n"
  "v_add_f32 v115, v115, v253\n"
  "v_mul_f32 v128, 0xbfb8aa3b, v108\n"
  "v_mul_f32 v129, 0xbfb8aa3b, v109\n"
  "v_mul_f32 v130, 0xbfb8aa3b, v110\n"
  "v_mul_f32 v131, 0xbfb8aa3b, v111\n"
  "v_mul_f32 v124, 0xbfb8aa3b, v112\n"
  "v_mul_f32 v125, 0xbfb8aa3b, v113\n"
  "v_mul_f32 v249, 0xbfb8aa3b, v114\n"
  "v_mul_f32 v253, 0xbfb8aa3b, v115\n"
  "v_exp_f32 v128, v128\n"
  "v_exp_f32 v129, v129\n"
  "v_exp_f32 v130, v130\n"
  "v_exp_f32 v131, v131\n"
  "v_exp_f32 v124, v124\n"
  "v_exp_f32 v125, v125\n"
  "v_exp_f32 v249, v249\n"
  "v_exp_f32 v253, v253\n"
  "v_add_f32 v128, 1.0, v128\n"
  "v_add_f32 v129, 1.0, v129\n"
  "v_add_f32 v130, 1.0, v130\n"
  "v_add_f32 v131, 1.0, v131\n"
  "v_add_f32 v124, 1.0, v124\n"
  "v_add_f32 v125, 1.0, v125\n"
  "v_add_f32 v249, 1.0, v249\n"
  "v_add_f32 v253, 1.0, v253\n"
  "v_rcp_f32 v128, v128\n"
  "v_rcp_f32 v129, v129\n"
  "v_rcp_f32 v130, v130\n"
  "v_rcp_f32 v131, v131\n"
  "v_rcp_f32 v124, v124\n"
  "v_rcp_f32 v125, v125\n"
  "v_rcp_f32 v249, v249\n"
  "v_rcp_f32 v253, v253\n"
  "v_mul_f32 v108, v108, v128\n"
  "v_mul_f32 v109, v109, v129\n"
  "v_mul_f32 v110, v110, v130\n"
  "v_mul_f32 v111, v111, v131\n"
  "v_mul_f32 v112, v112, v124\n"
  "v_mul_f32 v113, v113, v125\n"
  "v_mul_f32 v114, v114, v249\n"
  "v_mul_f32 v115, v115, v253\n"
  "v_lshlrev_b32 v128, 16, v88\n"
  "v_and_b32 v129, 0xffff0000, v88\n"
  "v_lshlrev_b32 v130, 16, v89\n"
  "v_and_b32 v131, 0xffff0000, v89\n"
  "v_lshlrev_b32 v124, 16, v90\n"
  "v_and_b32 v125, 0xffff0000, v90\n"
  "v_lshlrev_b32 v249, 16, v91\n"
  "v_and_b32 v253, 0xffff0000, v91\n"
  "s_waitcnt lgkmcnt(1)\n"
  "v_add_f32 v116, v116, v128\n"
  "v_add_f32 v117, v117, v129\n"
  "v_add_f32 v118, v118, v130\n"
  "v_add_f32 v119, v119, v131\n"
  "s_waitcnt lgkmcnt(0)\n"
  "v_add_f32 v120, v120, v124\n"
  "v_add_f32 v121, v121, v125\n"
  "v_add_f32 v122, v122, v249\n"
  "v_add_f32 v123, v123, v253\n"
  "v_mul_f32 v128, 0xbfb8aa3b, v116\n"
  "v_mul_f32 v129, 0xbfb8aa3b, v117\n"
  "v_mul_f32 v130, 0xbfb8aa3b, v118\n"
  "v_mul_f32 v131, 0xbfb8aa3b, v119\n"
  "v_mul_f32 v124, 0xbfb8aa3b, v120\n"
  "v_mul_f32 v125, 0xbfb8aa3b, v121\n"
  "v_mul_f32 v249, 0xbfb8aa3b, v122\n"
  "v_mul_f32 v253, 0xbfb8aa3b, v123\n"
  "v_exp_f32 v128, v128\n"
  "v_exp_f32 v129, v129\n"
  "v_exp_f32 v130, v130\n"
  "v_exp_f32 v131, v131\n"
  "v_exp_f32 v124, v124\n"
  "v_exp_f32 v125, v125\n"
  "v_exp_f32 v249, v249\n"
  "v_exp_f32 v253, v253\n"
  "v_add_f32 v128, 1.0, v128\n"
  "v_add_f32 v129, 1.0, v129\n"
  "v_add_f32 v130, 1.0, v130\n"
  "v_add_f32 v131, 1.0, v131\n"
  "v_add_f32 v124, 1.0, v124\n"
  "v_add_f32 v125, 1.0, v125\n"
  "v_add_f32 v249, 1.0, v249\n"
  "v_add_f32 v253, 1.0, v253\n"
  "v_rcp_f32 v128, v128\n"
  "v_rcp_f32 v129, v129\n"
  "v_rcp_f32 v130, v130\n"
  "v_rcp_f32 v131, v131\n"
  "v_rcp_f32 v124, v124\n"
  "v_rcp_f32 v125, v125\n"
  "v_rcp_f32 v249, v249\n"
  "v_rcp_f32 v253, v253\n"
  "v_mul_f32 v116, v116, v128\n"
  "v_mul_f32 v117, v117, v129\n"
  "v_mul_f32 v118, v118, v130\n"
  "v_mul_f32 v119, v119, v131\n"
  "v_mul_f32 v120, v120, v124\n"
  "v_mul_f32 v121, v121, v125\n"
  "v_mul_f32 v122, v122, v249\n"
  "v_mul_f32 v123, v123, v253\n"
  "s_bitset0_b32 s16, 9\n"
  "s_bitcmp1_b32 s16, 10\n"
  "s_cbranch_scc1 LHO2_%=\n"
  "s_bitcmp1_b32 s16, 11\n"
  "s_bitset0_b32 s16, 11\n"
  "s_cbranch_scc1 LYPB_%=\n"
  "s_and_b32 s16, s16, 0x1ff\n"
  "s_cbranch_scc1 LOW_%=\n"
  "s_branch LFN_%=\n"
  "LSAgemm_%=:\n"
  "s_cmp_eq_u32 s46, 0\n"
  "s_cbranch_scc1 LSAgemmB_%=\n"
  "s_sub_u32 s46, s46, 1\n"
  "s_sleep 2\n"
  "buffer_load_dword v99, v98, s[20:23], s80 offen sc1\n"
  "s_waitcnt vmcnt(0) lgkmcnt(0)\n"
  "v_cmp_ne_u32 vcc, s47, v99\n"
  "s_cbranch_vccnz LSAgemm_%=\n"
  "s_branch LSAgemmB_%=\n"
  "LEND_%=:\n"
  "KCODE1:\n"
  : "+{v[88:91]}"(rd), "+{v131}"(ep), "+{v[84:85]}"(vrel)
  :
    "{v92}"(rd16), "{v93}"(vwq), "{v94}"(vwg), "{v95}"(vw16), "{v[96:99]}"(st), "{v[100:101]}"(v100), "{v[110:111]}"(v110),
    "{v136}"(vpx),
    "{s[8:11]}"(sq), "{s[12:15]}"(sg), "{s[16:19]}"(su), "{s[20:23]}"(sw), "{s[24:27]}"(ssel), "{s[28:31]}"(s28), "{s[36:39]}"(srel), "{s[40:43]}"(s40), "{s45}"(s16x), "{s35}"(srb),
    "{s[68:83]}"(sa), "{s[84:91]}"(sb), "{s[92:95]}"(sxs), "{s[96:99]}"(sout)
  : "v0", "v1", "v2", "v3", "v4", "v5", "v6", "v7", "v8", "v9", "v10", "v11", "v12", "v13", "v14", "v15", "v16", "v17", "v18", "v19", "v20", "v21", "v22", "v23", "v24", "v25", "v26", "v27", "v28", "v29", "v30", "v31", "v32", "v33", "v34", "v35", "v36", "v37", "v38", "v39", "v40", "v41", "v42", "v43", "v44", "v45", "v46", "v47", "v48", "v49", "v50", "v51", "v52", "v53", "v54", "v55", "v56", "v57", "v58", "v59", "v60", "v61", "v62", "v63", "v64", "v65", "v66", "v67", "v68", "v69", "v70", "v71", "v72", "v73", "v74", "v75", "v76", "v77", "v78", "v79", "v80", "v81", "v82", "v83", "v86", "v87", "v102", "v103", "v104", "v105", "v106", "v107", "v108", "v109", "v112", "v113", "v114", "v115", "v116", "v117", "v118", "v119", "v120", "v121", "v122", "v123", "v124", "v125", "v126", "v127", "v128", "v129", "v130", "v132", "v133", "v134", "v135", "v137", "v138", "v139", "v140", "v141", "v142", "v143", "v144", "v145", "v146", "v147", "v148", "v149", "v150", "v151", "v152", "v153", "v154", "v155", "v156", "v157", "v158", "v159", "v160", "v161", "v162", "v163", "v164", "v165", "v166", "v167", "v168", "v169", "v170", "v171", "v172", "v173", "v174", "v175", "v176", "v177", "v178", "v179", "v180", "v181", "v182", "v183", "v184", "v185", "v186", "v187", "v188", "v189", "v190", "v191", "v192", "v193", "v194", "v195", "v196", "v197", "v198", "v199", "v200", "v201", "v202", "v203", "v204", "v205", "v206", "v207", "v208", "v209", "v210", "v211", "v212", "v213", "v214", "v215", "v216", "v217", "v218", "v219", "v220", "v221", "v222", "v223", "v224", "v225", "v226", "v227", "v228", "v229", "v230", "v231", "v232", "v233", "v234", "v235", "v236", "v237", "v238", "v239", "v240", "v241", "v242", "v243", "v244", "v245", "v246", "v247", "v248", "v249", "v250", "v251", "v252", "v253", "v254", "v255", "s44", "s46", "s47", "vcc", "scc", "memory");
}

extern "C" void kernel_launch(void* const* d_in, const int* in_sizes, int n_in, void* d_out, int out_size, void* d_ws, size_t ws_size, hipStream_t stream) {
  k_main<<<256, 512, 0, stream>>>((const u16*)d_in[0], (char*)d_ws, (const u16*)d_in[1], (const u16*)d_in[4], (const u16*)d_in[6], (const u16*)d_in[2], (const u16*)d_in[5], (u16*)d_out);
}
```
